```python
import torch

def _softmax(x, axis=-1):
    return torch.softmax(x, dim=axis)
def _gelu(x):
    return torch.nn.functional.gelu(x, approximate="tanh")
def _silu(x):
    return torch.nn.functional.silu(x)
def _sigmoid(x):
    return torch.sigmoid(x)
def _layernorm(x, axis=-1, eps=1e-5):
    m = x.mean(dim=axis, keepdim=True)
    v = x.var(dim=axis, keepdim=True, unbiased=False)
    return (x - m) / torch.sqrt(v + eps)
def _scatter_add(tgt, idx, src, axis=0):
    return tgt.clone().index_add_(axis, idx, src)
def _segment_sum(src, seg, n):
    out = torch.zeros((n,) + src.shape[1:], dtype=src.dtype)
    return out.index_add_(0, seg, src)
def _pad(x, pw):
    flat = tuple(v for pair in reversed(pw) for v in pair)
    return torch.nn.functional.pad(x, flat)

kernel_name = 'sequential_preLN__spectral_gfnet__geglu_ffn__prompt'

def setup_inputs():

    _w = (lambda a: (lambda t: t.to(torch.bfloat16) if t.is_floating_point() else t)(torch.as_tensor(a)))
    def _A_setup():
        B, S, D = 1, 2048, 640
        Df = D // 2 + 1
        return {
            "x": _w((lambda *s: torch.randn(*s, dtype=torch.bfloat16) * 0.05)(B, S, D)),
            "w_r": _w((lambda *s: torch.randn(*s, dtype=torch.bfloat16) * 0.4)(S, Df) * 0.02),
            "w_i": _w((lambda *s: torch.randn(*s, dtype=torch.bfloat16) * 0.4)(S, Df) * 0.02),
        }
    _A_in = _A_setup()

    def _B_setup():
        B, S, D, F = 1, 2048, 640, 1728
        return {
            "x": _w((lambda *s: torch.randn(*s, dtype=torch.bfloat16) * 0.05)(B, S, D)),
            "w_gate": _w((lambda *s: torch.randn(*s, dtype=torch.bfloat16) * 0.05)(D, F)),
            "w_up": _w((lambda *s: torch.randn(*s, dtype=torch.bfloat16) * 0.05)(D, F)),
            "w_down": _w((lambda *s: torch.randn(*s, dtype=torch.bfloat16) * 0.05)(F, D)),
        }
    _B_in = _B_setup()

    return {
        "A_x": _A_in["x"],
        "A_w_r": _A_in["w_r"],
        "A_w_i": _A_in["w_i"],
        "B_x": _B_in["x"],
        "B_w_gate": _B_in["w_gate"],
        "B_w_up": _B_in["w_up"],
        "B_w_down": _B_in["w_down"],
    }

def reference(A_x, A_w_r, A_w_i, B_x, B_w_gate, B_w_up, B_w_down):
    def _A_ref(x, w_r, w_i):
        B, S, D = x.shape
        X = (lambda x, axes=(-2,-1): torch.fft.rfft2(x.float(), dim=axes))(x, axes=(-2, -1))
        W = w_r + 1j * w_i
        X = X * W
        y = (lambda x, s, axes=(-2,-1): torch.fft.irfft2(x, s=s, dim=axes).to(torch.bfloat16))(X, (S, D), axes=(-2, -1))
        return y

    def _B_ref(x, w_gate, w_up, w_down):
        return (_gelu(x @ w_gate) * (x @ w_up)) @ w_down

    x = A_x
    h = x + _A_ref(_layernorm(x, axis=-1), A_w_r, A_w_i)
    h2 = h + _B_ref(_layernorm(h, axis=-1), B_w_gate, B_w_up, B_w_down)
    return h2
```

```cpp
#include <hip/hip_runtime.h>
#include <hip/hip_bf16.h>
#include <stdint.h>
#include <utility>

typedef _Float16 h8 __attribute__((ext_vector_type(8)));
typedef _Float16 h4 __attribute__((ext_vector_type(4)));
typedef _Float16 h2 __attribute__((ext_vector_type(2)));
typedef float f4 __attribute__((ext_vector_type(4)));
typedef float f2 __attribute__((ext_vector_type(2)));
typedef short s8 __attribute__((ext_vector_type(8)));
typedef short s4 __attribute__((ext_vector_type(4)));
typedef unsigned u4 __attribute__((ext_vector_type(4)));
typedef unsigned u2 __attribute__((ext_vector_type(2)));
typedef __bf16 bf2 __attribute__((ext_vector_type(2)));
typedef unsigned short u16;
#define DI __device__ __forceinline__
#define LAS __attribute__((address_space(3)))
#define GAS __attribute__((address_space(1)))

constexpr int SS = 2048, DD = 640, FF = 1728, DFQ = 321;

constexpr size_t OFF_Z1 = 0;
constexpr size_t OFF_Z2 = OFF_Z1 + (size_t)640 * 256 * 16;
constexpr size_t OFF_WT = OFF_Z2 + (size_t)640 * 256 * 16;
constexpr size_t OFF_WM = OFF_WT + (size_t)321 * 512 * 16;
constexpr size_t OFF_HP = OFF_WM + 1024;
constexpr size_t OFF_W1 = OFF_HP + (size_t)20 * 128 * 1024;
constexpr size_t OFF_AP = OFF_W1 + (size_t)20 * 216 * 1024;
constexpr size_t OFF_W2 = OFF_AP + (size_t)54 * 128 * 1024;

template <class Fn, int... I>
DI void static_for_impl(Fn&& f, std::integer_sequence<int, I...>) { (f(std::integral_constant<int, I>{}), ...); }
template <int N, class Fn>
DI void static_for(Fn&& f) { static_for_impl(f, std::make_integer_sequence<int, N>{}); }

DI unsigned pk_bf16(float a, float b) { f2 v = {a, b}; return __builtin_bit_cast(unsigned, __builtin_convertvector(v, bf2)); }
DI unsigned pk_f16(float a, float b) { f2 v = {a, b}; return __builtin_bit_cast(unsigned, __builtin_convertvector(v, h2)); }
DI float bf_lo(unsigned u) { return __builtin_bit_cast(float, u << 16); }
DI float bf_hi(unsigned u) { return __builtin_bit_cast(float, u & 0xffff0000u); }
DI float rbf(float a) { return bf_hi(pk_bf16(0.f, a)); }
DI float fsin(float r) { return __builtin_amdgcn_sinf(r); }
DI float fcos(float r) { return __builtin_amdgcn_cosf(r); }
DI f4 mfma_h(u4 a, u4 b, f4 c) { return __builtin_amdgcn_mfma_f32_16x16x32_f16(__builtin_bit_cast(h8, a), __builtin_bit_cast(h8, b), c, 0, 0, 0); }
DI f4 mfma_b(u4 a, u4 b, f4 c) { return __builtin_amdgcn_mfma_f32_16x16x32_bf16(__builtin_bit_cast(s8, a), __builtin_bit_cast(s8, b), c, 0, 0, 0); }
DI u2 tr_read(const char* p) { return __builtin_bit_cast(u2, __builtin_amdgcn_ds_read_tr16_b64_v4i16((LAS s4*)p)); }

template <int CTRL> DI float dpp(float v) {
  return __builtin_bit_cast(float, __builtin_amdgcn_update_dpp(0, __builtin_bit_cast(int, v), CTRL, 0xf, 0xf, true));
}
DI float swap16_sum(float v) {
  unsigned u = __builtin_bit_cast(unsigned, v);
  auto r = __builtin_amdgcn_permlane16_swap(u, u, false, false);
  return __builtin_bit_cast(float, (unsigned)r[0]) + __builtin_bit_cast(float, (unsigned)r[1]);
}
DI float swap32_sum(float v) {
  unsigned u = __builtin_bit_cast(unsigned, v);
  auto r = __builtin_amdgcn_permlane32_swap(u, u, false, false);
  return __builtin_bit_cast(float, (unsigned)r[0]) + __builtin_bit_cast(float, (unsigned)r[1]);
}
DI float half_sum(float v) {
  v += dpp<0x121>(v);
  v += dpp<0x122>(v);
  v += dpp<0x124>(v);
  v += dpp<0x128>(v);
  return swap16_sum(v);
}
DI float parity_sum(float v) {
  v += dpp<0x122>(v);
  v += dpp<0x124>(v);
  v += dpp<0x128>(v);
  v = swap16_sum(v);
  return swap32_sum(v);
}
DI unsigned wave_umax(unsigned v) {
  for (int o = 32; o >= 1; o >>= 1) { unsigned t = __shfl_xor(v, o); v = v > t ? v : t; }
  return v;
}
DI int srow_of(int q, int i, int j) { return 32 * (4 * (q >> 4) + i) + (q & 15) + 16 * j; }

struct RowC {
  u4 Fr, Fi;
  float tc[5], ts[5];
  u4 G0[3];
  unsigned G1[3];
};
template <int SGN> DI void row_consts(RowC& C, int l, int cb) {
  const int g = l >> 4, r16 = l & 15, c = 16 * cb + r16;
#pragma unroll
  for (int jj = 0; jj < 4; ++jj) {
    float r0 = (float)(((8 * g + 2 * jj) * c) & 31) * (SGN * 0.03125f);
    float r1 = (float)(((8 * g + 2 * jj + 1) * c) & 31) * (SGN * 0.03125f);
    C.Fr[jj] = pk_f16(fcos(r0), fcos(r1));
    C.Fi[jj] = pk_f16(fsin(r0), fsin(r1));
  }
#pragma unroll
  for (int blk = 0; blk < 5; ++blk) {
    float r = (float)((4 * blk + g) * c) * (SGN * (1.f / 640.f));
    C.tc[blk] = fcos(r) * 0.03125f;
    C.ts[blk] = fsin(r) * 0.03125f;
  }
  const float po4 = (float)(r16 & 1) * 0.25f;
#pragma unroll
  for (int ob = 0; ob < 3; ++ob) {
    const int e = 8 * ob + (r16 >> 1);
#pragma unroll
    for (int blk = 0; blk < 5; ++blk) {
      int be = ((4 * blk + g) * e) % 20;
      float r = (float)be * (-SGN * 0.05f) + po4;
      unsigned v = pk_f16(fcos(r), fsin(r));
      if (blk < 4) C.G0[ob][blk] = v; else C.G1[ob] = v;
    }
  }
}
DI void row_stage_a(const RowC& C, const char* lds, int l, int ih, u4 (&T0)[2], unsigned (&T1)[2]) {
  const int g = l >> 4, q4 = (l & 15) >> 2, p = l & 3;
  const char* base = lds + (8 * g + q4) * 320 + p * 16 + ih * 8;
#pragma unroll
  for (int blk = 0; blk < 5; ++blk) {
    u2 lo = tr_read(base + blk * 64);
    u2 hi = tr_read(base + blk * 64 + 4 * 320);
    u4 a = {lo[0], lo[1], hi[0], hi[1]};
    f4 z = {0.f, 0.f, 0.f, 0.f};
    f4 pr = mfma_h(a, C.Fr, z);
    f4 pi = mfma_h(a, C.Fi, z);
#pragma unroll
    for (int ii = 0; ii < 2; ++ii) {
      float tr = pr[2 * ii] - pi[2 * ii + 1];
      float ti = pi[2 * ii] + pr[2 * ii + 1];
      float ur = tr * C.tc[blk] - ti * C.ts[blk];
      float ui = ti * C.tc[blk] + tr * C.ts[blk];
      unsigned v = pk_f16(ur, ui);
      if (blk < 4) T0[ii][blk] = v; else T1[ii] = v;
    }
  }
}

__global__ __launch_bounds__(256) void k1_kernel(const u16* __restrict__ x, const u16* __restrict__ wr, const u16* __restrict__ wi,
                                                 char* __restrict__ ws) {
  __shared__ __attribute__((aligned(16))) char smem[41088 + 64];
  const int b = blockIdx.x, tid = threadIdx.x, l = tid & 63;
  const int w = __builtin_amdgcn_readfirstlane(tid >> 6);
  if (b >= 256) {
    const int task = b - 256, u = task & 7, v = task >> 3;
    for (int idx = tid; idx < 8 * 321; idx += 256) {
      int ch = idx / 321, pc = idx - ch * 321;
      int arr = ch >> 2, reg = ch & 3;
      const u4* src = (const u4*)((arr ? wi : wr) + (size_t)(64 * (4 * v + reg) + 8 * u) * 321);
      *(u4*)(smem + ch * 5136 + pc * 16) = src[pc];
    }
    __syncthreads();
    u4* Wt = (u4*)(ws + OFF_WT);
    const int obase = ((u >> 1) * 2 + (v >> 2)) * 64 + 16 * (v & 3) + 8 * (u & 1);
    unsigned mx = 0;
    for (int idx = tid; idx < 8 * 321; idx += 256) {
      int kd = idx >> 3, r8 = idx & 7;
      const u16* s = (const u16*)smem + r8 * 321 + kd;
      u4 o;
#pragma unroll
      for (int reg = 0; reg < 4; ++reg) {
        unsigned a = s[reg * 2568], c = s[(4 + reg) * 2568];
        unsigned am = a & 0x7fffu, cm = c & 0x7fffu;
        mx = max(mx, max(am, cm));
        o[reg] = a | (c << 16);
      }
      Wt[kd * 512 + obase + r8] = o;
    }
    mx = wave_umax(mx);
    __syncthreads();
    if (l == 0) ((unsigned*)smem)[w] = mx;
    __syncthreads();
    if (tid == 0) {
      const unsigned* m = (const unsigned*)smem;
      ((unsigned*)(ws + OFF_WM))[task] = max(max(m[0], m[1]), max(m[2], m[3]));
    }
    return;
  }
  const int q = (b & 7) * 32 + (b >> 3);
  const int j = l >> 5, li = l & 31;
  const u2* xr = (const u2*)(x + (size_t)srow_of(q, w, j) * DD);
  u2 xv[5];
#pragma unroll
  for (int c = 0; c < 5; ++c) xv[c] = xr[li + 32 * c];
  const int ih = w >> 1, cb = w & 1;
  RowC C;
  row_consts<-1>(C, l, cb);
  float v[20];
#pragma unroll
  for (int c = 0; c < 5; ++c) {
    v[4 * c] = bf_lo(xv[c][0]); v[4 * c + 1] = bf_hi(xv[c][0]);
    v[4 * c + 2] = bf_lo(xv[c][1]); v[4 * c + 3] = bf_hi(xv[c][1]);
  }
  float s = 0.f;
#pragma unroll
  for (int t = 0; t < 20; ++t) s += v[t];
  const float mean = half_sum(s) * (1.f / 640.f);
  float s2 = 0.f;
#pragma unroll
  for (int t = 0; t < 20; ++t) { float d = v[t] - mean; s2 += d * d; }
  const float var = half_sum(s2) * (1.f / 640.f);
  const float mb = rbf(mean);
  const float sd = rbf(__builtin_sqrtf(rbf(rbf(var) + 1e-5f)));
  const float rs = 1.f / sd;
  char* lin = smem;
  char* lout = smem + 10240;
#pragma unroll
  for (int c = 0; c < 5; ++c) {
    unsigned d01 = pk_bf16(v[4 * c] - mb, v[4 * c + 1] - mb);
    unsigned d23 = pk_bf16(v[4 * c + 2] - mb, v[4 * c + 3] - mb);
    unsigned n01 = pk_bf16(bf_lo(d01) * rs, bf_hi(d01) * rs);
    unsigned n23 = pk_bf16(bf_lo(d23) * rs, bf_hi(d23) * rs);
    unsigned p01 = pk_f16(bf_lo(n01) * 16.f, bf_hi(n01) * 16.f);
    unsigned p23 = pk_f16(bf_lo(n23) * 16.f, bf_hi(n23) * 16.f);
    auto r = __builtin_amdgcn_permlane32_swap(p01, p23, false, false);
    unsigned pa = r[0], pb = r[1];
    unsigned o0 = __builtin_amdgcn_perm(pb, pa, 0x05040100u);
    unsigned o1 = __builtin_amdgcn_perm(pb, pa, 0x07060302u);
    int n = 4 * (li + 32 * c) + 2 * j;
    *(unsigned*)(lin + n * 16 + w * 4) = o0;
    *(unsigned*)(lin + n * 16 + 16 + w * 4) = o1;
  }
  __syncthreads();
  u4 T0[2]; unsigned T1[2];
  row_stage_a(C, lin, l, ih, T0, T1);
  const int g = l >> 4, cc = 16 * cb + (l & 15);
#pragma unroll
  for (int ob = 0; ob < 3; ++ob) {
    f4 o[2];
#pragma unroll
    for (int ii = 0; ii < 2; ++ii) {
      f4 z = {0.f, 0.f, 0.f, 0.f};
      z = mfma_h(C.G0[ob], T0[ii], z);
      u4 ga = {C.G1[ob], 0u, 0u, 0u}, tb = {T1[ii], 0u, 0u, 0u};
      o[ii] = mfma_h(ga, tb, z);
    }
    if (ob < 2 || g < 2) {
#pragma unroll
      for (int hh = 0; hh < 2; ++hh) {
        int k = cc + 32 * (8 * ob + 2 * g + hh);
        u2 st = {pk_f16(o[0][2 * hh], o[0][2 * hh + 1]), pk_f16(o[1][2 * hh], o[1][2 * hh + 1])};
        *(u2*)(lout + k * 16 + ih * 8) = st;
      }
    }
  }
  __syncthreads();
  u4* Z1 = (u4*)(ws + OFF_Z1);
#pragma unroll
  for (int it = 0; it < 3; ++it) {
    int k = tid + 256 * it;
    if (k < 640) Z1[k * 256 + q] = *(const u4*)(lout + k * 16);
  }
}

DI unsigned wmax_exp(const char* ws, int l) {
  unsigned m = ((const unsigned*)(ws + OFF_WM))[l];
  m = wave_umax(m);
  unsigned eb = m >> 7;
  return eb > 251u ? 251u : eb;
}
__global__ __launch_bounds__(256) void k2_kernel(const u16* __restrict__ wg, const u16* __restrict__ wu, char* __restrict__ ws) {
  __shared__ __attribute__((aligned(16))) char smem[32 * 272];
  const int b = blockIdx.x, tid = threadIdx.x, l = tid & 63;
  const int w = __builtin_amdgcn_readfirstlane(tid >> 6);
  const int g = l >> 4, r16 = l & 15;
  if (b >= 328) {
    const int task = b - 328, ks = task / 27, fg = task - ks * 27;
    const int k = tid >> 3, seg = tid & 7;
    const size_t off = (size_t)(32 * ks + k) * FF + 64 * fg + 8 * seg;
    u4 a = *(const u4*)(wg + off), c = *(const u4*)(wu + off);
    u4 o0, o1;
    o0[0] = __builtin_amdgcn_perm(c[0], a[0], 0x05040100u); o0[1] = __builtin_amdgcn_perm(c[0], a[0], 0x07060302u);
    o0[2] = __builtin_amdgcn_perm(c[1], a[1], 0x05040100u); o0[3] = __builtin_amdgcn_perm(c[1], a[1], 0x07060302u);
    o1[0] = __builtin_amdgcn_perm(c[2], a[2], 0x05040100u); o1[1] = __builtin_amdgcn_perm(c[2], a[2], 0x07060302u);
    o1[2] = __builtin_amdgcn_perm(c[3], a[3], 0x05040100u); o1[3] = __builtin_amdgcn_perm(c[3], a[3], 0x07060302u);
    *(u4*)(smem + k * 272 + seg * 32) = o0;
    *(u4*)(smem + k * 272 + seg * 32 + 16) = o1;
    __syncthreads();
    u4* W1 = (u4*)(ws + OFF_W1);
    const char* base = smem + (8 * g + (r16 >> 2)) * 272 + (l & 3) * 8;
#pragma unroll
    for (int t = 0; t < 2; ++t) {
      int nb = 2 * w + t;
      u2 lo = tr_read(base + nb * 32), hi = tr_read(base + nb * 32 + 4 * 272);
      u4 o = {lo[0], lo[1], hi[0], hi[1]};
      W1[(size_t)(ks * 216 + fg * 8 + nb) * 64 + l] = o;
    }
    return;
  }
  const int xi = b >> 3;
  int kd = 40 * (b & 7) + xi;
  if (xi == 40) { if ((b & 7) != 7) return; kd = 320; }
  const int kdm = kd == 0 ? 0 : 640 - kd;
  const u4* Z1 = (const u4*)(ws + OFF_Z1);
  u4 z1[4], z2[4];
#pragma unroll
  for (int t = 0; t < 4; ++t) { z1[t] = Z1[kd * 256 + 64 * t + l]; z2[t] = Z1[kdm * 256 + 64 * t + l]; }
  const u4* Wt = (const u4*)(ws + OFF_WT);
  u4 wv[2];
#pragma unroll
  for (int ob = 0; ob < 2; ++ob) wv[ob] = Wt[kd * 512 + (2 * w + ob) * 64 + l];
  const unsigned eb = wmax_exp(ws, l);
  const float fs = __builtin_bit_cast(float, (252u - eb) << 23) * 0.125f;
  u4 M1[4], M2[4], N1[2][2], N2[2][2];
  const int k1 = 16 * w + r16;
#pragma unroll
  for (int t = 0; t < 4; ++t)
#pragma unroll
    for (int jj = 0; jj < 4; ++jj) {
      float r = (float)(((16 * t + 4 * g + jj) * k1) & 63) * (1.f / 64.f);
      float c = fcos(r), s = fsin(r);
      M1[t][jj] = pk_f16(c, s);
      M2[t][jj] = pk_f16(-s, c);
    }
#pragma unroll
  for (int xx = 0; xx < 2; ++xx)
#pragma unroll
    for (int yy = 0; yy < 2; ++yy)
#pragma unroll
      for (int jj = 0; jj < 4; ++jj) {
        float r = (float)(((16 * xx + r16) * (16 * yy + 4 * g + jj)) & 31) * (1.f / 32.f);
        float c = fcos(r), s = fsin(r);
        N1[xx][yy][jj] = pk_f16(c, s);
        N2[xx][yy][jj] = pk_f16(-s, c);
      }
  float fc[2][4], fsn[2][4], ic[2][4], isn[2][4];
#pragma unroll
  for (int rb = 0; rb < 2; ++rb)
#pragma unroll
    for (int rg = 0; rg < 4; ++rg) {
      float r = (float)((16 * rb + 4 * g + rg) * k1) * (1.f / 2048.f);
      fc[rb][rg] = fcos(r) * (1.f / 64.f); fsn[rb][rg] = fsin(r) * (1.f / 64.f);
      float r2 = (float)((16 * rb + r16) * (16 * w + 4 * g + rg)) * (1.f / 2048.f);
      ic[rb][rg] = fcos(r2) * 0.125f; isn[rb][rg] = fsin(r2) * 0.125f;
    }
  const f4 zero = {0.f, 0.f, 0.f, 0.f};
  f4 Tr[2] = {zero, zero}, Ti[2] = {zero, zero};
#pragma unroll
  for (int t = 0; t < 4; ++t) {
    u4 A0, A1;
#pragma unroll
    for (int jj = 0; jj < 4; ++jj) {
      h2 p = __builtin_bit_cast(h2, (unsigned)z1[t][jj]), qq = __builtin_bit_cast(h2, (unsigned)z2[t][jj]);
      h2 qa = {qq[0], -qq[1]};
      h2 a = p + qa;
      h2 pb = {p[1], -p[0]}, qb = {qq[1], qq[0]};
      h2 bb = pb + qb;
      A0[jj] = __builtin_bit_cast(unsigned, a);
      A1[jj] = __builtin_bit_cast(unsigned, bb);
    }
    Tr[0] = mfma_h(A0, M1[t], Tr[0]); Ti[0] = mfma_h(A0, M2[t], Ti[0]);
    Tr[1] = mfma_h(A1, M1[t], Tr[1]); Ti[1] = mfma_h(A1, M2[t], Ti[1]);
  }
  u4 X[2];
#pragma unroll
  for (int rb = 0; rb < 2; ++rb)
#pragma unroll
    for (int rg = 0; rg < 4; ++rg) {
      float a = Tr[rb][rg], c = Ti[rb][rg];
      X[rb][rg] = pk_f16(a * fc[rb][rg] + c * fsn[rb][rg], c * fc[rb][rg] - a * fsn[rb][rg]);
    }
  u4 Y[2];
#pragma unroll
  for (int ob = 0; ob < 2; ++ob) {
    f4 xr = mfma_h(N1[ob][0], X[0], zero); xr = mfma_h(N1[ob][1], X[1], xr);
    f4 xim = mfma_h(N2[ob][0], X[0], zero); xim = mfma_h(N2[ob][1], X[1], xim);
#pragma unroll
    for (int rg = 0; rg < 4; ++rg) {
      float wr_ = bf_lo(wv[ob][rg]) * fs, wi_ = bf_hi(wv[ob][rg]) * fs;
      float yr = xr[rg] * wr_ - xim[rg] * wi_;
      float yi = xr[rg] * wi_ + xim[rg] * wr_;
      Y[ob][rg] = pk_f16(yi, yr);
    }
  }
  u4* ex = (u4*)smem;
#pragma unroll
  for (int cb = 0; cb < 2; ++cb) {
    f4 ui = mfma_h(Y[0], N1[cb][0], zero); ui = mfma_h(Y[1], N1[cb][1], ui);
    f4 ur = mfma_h(Y[0], N2[cb][0], zero); ur = mfma_h(Y[1], N2[cb][1], ur);
    u4 U;
#pragma unroll
    for (int rg = 0; rg < 4; ++rg) {
      float re = ur[rg] * ic[cb][rg] - ui[rg] * isn[cb][rg];
      float im = ui[rg] * ic[cb][rg] + ur[rg] * isn[cb][rg];
      U[rg] = pk_f16(im, re);
    }
    ex[(w * 2 + cb) * 64 + l] = U;
  }
  __syncthreads();
  f4 Vi[2] = {zero, zero}, Vr[2] = {zero, zero};
#pragma unroll
  for (int t = 0; t < 4; ++t)
#pragma unroll
    for (int cb = 0; cb < 2; ++cb) {
      u4 U = ex[(t * 2 + cb) * 64 + l];
      Vi[cb] = mfma_h(M1[t], U, Vi[cb]);
      Vr[cb] = mfma_h(M2[t], U, Vr[cb]);
    }
  u4* Z2 = (u4*)(ws + OFF_Z2) + (size_t)(64 * w + l) * 640;
  if (kd == 0 || kd == 320) {
    u4 o;
#pragma unroll
    for (int rg = 0; rg < 4; ++rg) o[rg] = pk_f16(Vr[0][rg], Vr[1][rg]);
    Z2[kd] = o;
  } else {
    u4 o, om;
#pragma unroll
    for (int rg = 0; rg < 4; ++rg) {
      o[rg] = pk_f16(Vr[0][rg] - Vi[1][rg], Vi[0][rg] + Vr[1][rg]);
      om[rg] = pk_f16(Vr[0][rg] + Vi[1][rg], Vr[1][rg] - Vi[0][rg]);
    }
    Z2[kd] = o;
    Z2[kdm] = om;
  }
}

__global__ __launch_bounds__(256) void k3_kernel(const u16* __restrict__ x, const u16* __restrict__ wd, u16* __restrict__ out,
                                                 char* __restrict__ ws) {
  __shared__ __attribute__((aligned(16))) char smem[10240 + 256];
  const int b = blockIdx.x, tid = threadIdx.x, l = tid & 63;
  const int w = __builtin_amdgcn_readfirstlane(tid >> 6);
  const int g = l >> 4, r16 = l & 15;
  if (b >= 256) {
    const int task = b - 256, ks = task / 5, dg = task - ks * 5;
    const int k = tid >> 3, seg = tid & 7;
    const u4* src = (const u4*)(wd + (size_t)(32 * ks + k) * DD + 128 * dg);
    u4 a = src[seg], c = src[seg + 8];
    *(u4*)(smem + k * 272 + seg * 16) = a;
    *(u4*)(smem + k * 272 + 128 + seg * 16) = c;
    __syncthreads();
    u4* W2 = (u4*)(ws + OFF_W2);
    const char* base = smem + (8 * g + (r16 >> 2)) * 272 + (l & 3) * 8;
#pragma unroll
    for (int t = 0; t < 2; ++t) {
      int nb = 2 * w + t;
      u2 lo = tr_read(base + nb * 32), hi = tr_read(base + nb * 32 + 4 * 272);
      u4 o = {lo[0], lo[1], hi[0], hi[1]};
      W2[(size_t)(ks * 40 + dg * 8 + nb) * 64 + l] = o;
    }
    return;
  }
  const int q = b;
  const int ih = w >> 1, cb = w & 1, j = l & 1;
  const u4* Z2 = (const u4*)(ws + OFF_Z2) + (size_t)q * 640;
  u4 zv[3];
#pragma unroll
  for (int it = 0; it < 3; ++it) { int k = tid + 256 * it; if (k < 640) zv[it] = Z2[k]; }
  const int e0 = r16 >> 1;
  const bool v2 = e0 < 4;
  int srow[2];
  u2 xv[2][3];
#pragma unroll
  for (int ii = 0; ii < 2; ++ii) {
    srow[ii] = srow_of(q, 2 * ih + ii, j);
    const u16* xr = x + (size_t)srow[ii] * DD + 16 * cb + 4 * g + 32 * e0;
 #pragma unroll
    for (int ob = 0; ob < 3; ++ob) {
      if (ob < 2 || v2) xv[ii][ob] = *(const u2*)(xr + 256 * ob); else xv[ii][ob] = u2{0u, 0u};
    }
  }
  const unsigned eb = wmax_exp(ws, l);
  const float ysc = __builtin_bit_cast(float, (eb + 2u) << 23) * 0.1f;
  RowC C;
  row_consts<1>(C, l, cb);
#pragma unroll
  for (int it = 0; it < 3; ++it) { int k = tid + 256 * it; if (k < 640) *(u4*)(smem + k * 16) = zv[it]; }
  __syncthreads();
  u4 T0[2]; unsigned T1[2];
  row_stage_a(C, smem, l, ih, T0, T1);
  float h[2][12];
  float mloc[2], m2loc[2];
#pragma unroll
  for (int ii = 0; ii < 2; ++ii) {
    float s = 0.f;
#pragma unroll
    for (int ob = 0; ob < 3; ++ob) {
      f4 z = {0.f, 0.f, 0.f, 0.f};
      z = mfma_h(T0[ii], C.G0[ob], z);
      u4 ta = {T1[ii], 0u, 0u, 0u}, gb = {C.G1[ob], 0u, 0u, 0u};
      z = mfma_h(ta, gb, z);
      unsigned y01 = pk_bf16(z[0] * ysc, z[1] * ysc), y23 = pk_bf16(z[2] * ysc, z[3] * ysc);
      unsigned h01 = pk_bf16(bf_lo(xv[ii][ob][0]) + bf_lo(y01), bf_hi(xv[ii][ob][0]) + bf_hi(y01));
      unsigned h23 = pk_bf16(bf_lo(xv[ii][ob][1]) + bf_lo(y23), bf_hi(xv[ii][ob][1]) + bf_hi(y23));
      if (ob == 2 && !v2) { h01 = 0u; h23 = 0u; }
      else {
        u2 st = {h01, h23};
        *(u2*)(out + (size_t)srow[ii] * DD + 16 * cb + 4 * g + 32 * e0 + 256 * ob) = st;
      }
      h[ii][4 * ob] = bf_lo(h01); h[ii][4 * ob + 1] = bf_hi(h01); h[ii][4 * ob + 2] = bf_lo(h23); h[ii][4 * ob + 3] = bf_hi(h23);
      s += (h[ii][4 * ob] + h[ii][4 * ob + 1]) + (h[ii][4 * ob + 2] + h[ii][4 * ob + 3]);
    }
    mloc[ii] = parity_sum(s) * (1.f / 320.f);
    float s2 = 0.f;
#pragma unroll
    for (int t = 0; t < 12; ++t) {
      float d = h[ii][t] - mloc[ii];
      if (t < 8) s2 += d * d; else s2 += v2 ? d * d : 0.f;
    }
    m2loc[ii] = parity_sum(s2);
  }
  float* ex = (float*)(smem + 10240);
  if (l < 2) {
#pragma unroll
    for (int ii = 0; ii < 2; ++ii) { ex[(w * 2 + ii) * 4 + l * 2] = mloc[ii]; ex[(w * 2 + ii) * 4 + l * 2 + 1] = m2loc[ii]; }
  }
  __syncthreads();
  u2* Hp = (u2*)(ws + OFF_HP);
#pragma unroll
  for (int ii = 0; ii < 2; ++ii) {
    float mo = ex[((w ^ 1) * 2 + ii) * 4 + j * 2], m2o = ex[((w ^ 1) * 2 + ii) * 4 + j * 2 + 1];
    float mean = 0.5f * (mloc[ii] + mo);
    float dl = mloc[ii] - mo;
    float var = (m2loc[ii] + m2o + dl * dl * 160.f) * (1.f / 640.f);
    const float mb = rbf(mean);
    const float sd = rbf(__builtin_sqrtf(rbf(rbf(var) + 1e-5f)));
    const float rs = 1.f / sd;
    const int mp = 8 * q + 2 * (2 * ih + ii) + j;
    const int slot = (mp >> 4) * 64 + (mp & 15) + 16 * (2 * cb + (g >> 1));
#pragma unroll
    for (int ob = 0; ob < 3; ++ob) {
      if (ob < 2 || v2) {
        unsigned d01 = pk_bf16(h[ii][4 * ob] - mb, h[ii][4 * ob + 1] - mb);
        unsigned d23 = pk_bf16(h[ii][4 * ob + 2] - mb, h[ii][4 * ob + 3] - mb);
        u2 st = {pk_bf16(bf_lo(d01) * rs, bf_hi(d01) * rs), pk_bf16(bf_lo(d23) * rs, bf_hi(d23) * rs)};
        Hp[((size_t)(8 * ob + e0) * 128 * 64 + slot) * 2 + (g & 1)] = st;
      }
    }
  }
}

#define GLDS(src, dst) __builtin_amdgcn_global_load_lds((const GAS void*)(src), (LAS void*)(dst), 16, 0, 0)
template <int N> DI void wait_vm() { asm volatile("s_waitcnt vmcnt(%0)" ::"n"(N) : "memory"); }
DI void raw_barrier() { asm volatile("s_waitcnt lgkmcnt(0)" ::: "memory"); __builtin_amdgcn_s_barrier(); }

DI float geglu(float g, float u) {
  float t = g * g;
  float z = g * (t * (-2.f * 1.4426950408889634f * 0.7978845608028654f * 0.044715f) + (-2.f * 1.4426950408889634f * 0.7978845608028654f));
  float e = __builtin_amdgcn_exp2f(z);
  float gl = g * __builtin_amdgcn_rcpf(1.f + e);
  return rbf(gl) * u;
}

constexpr int G1_STG = 22 * 1024, G1_NB = 7, G1_NS = 20;
__global__ __launch_bounds__(512, 2) void k4_kernel(char* __restrict__ ws) {
  extern __shared__ __attribute__((aligned(16))) char smem[];
  const int b = blockIdx.x, tid = threadIdx.x, l = tid & 63;
  const int w = __builtin_amdgcn_readfirstlane(tid >> 6);
  const int xcd = b & 7, idx = b >> 3;
  const int mt = 8 * (xcd & 1) + (idx & 7), nt = 4 * (xcd >> 1) + (idx >> 3);
  const int nb0 = (27 * nt) >> 1, mb0 = 8 * mt;
  const u4* srcA = (const u4*)(ws + OFF_HP) + (mb0 + w) * 64 + l;
  const u4* srcB = (const u4*)(ws + OFF_W1) + (nb0 + w) * 64 + l;
  const bool three = w < 6;
  auto issue = [&](int ks, int buf) {
    char* d = smem + buf * G1_STG + w * 1024;
    GLDS(srcA + ks * (128 * 64), d);
    GLDS(srcB + ks * (216 * 64), d + 8 * 1024);
    if (three) GLDS(srcB + ks * (216 * 64) + 8 * 64, d + 16 * 1024);
  };
#pragma unroll
  for (int s = 0; s < G1_NB; ++s) issue(s, s);
  const int wm = w >> 1, wn = w & 1;
  const char* rdA = smem + (2 * wm) * 1024 + l * 16;
  const char* rdB = smem + (8 + 7 * wn) * 1024 + l * 16;
  f4 acc[7][2];
#pragma unroll
  for (int i = 0; i < 7; ++i) { acc[i][0] = f4{0.f, 0.f, 0.f, 0.f}; acc[i][1] = f4{0.f, 0.f, 0.f, 0.f}; }
  u4 af[2][2], wf[2][7];
  if (three) wait_vm<18>(); else wait_vm<12>();
  raw_barrier();
#pragma unroll
  for (int i = 0; i < 2; ++i) af[0][i] = *(const u4*)(rdA + i * 1024);
#pragma unroll
  for (int i = 0; i < 7; ++i) wf[0][i] = *(const u4*)(rdB + i * 1024);
  static_for<G1_NS>([&](auto S_) {
    constexpr int s = decltype(S_)::value;
    constexpr int cur = s & 1, nxt = cur ^ 1;
    if constexpr (s + 1 < G1_NS) {
      constexpr int last = (s + G1_NB - 1 < G1_NS - 1) ? s + G1_NB - 1 : G1_NS - 1;
      constexpr int allow = last - (s + 1);
      if (three) wait_vm<3 * allow>(); else wait_vm<2 * allow>();
      raw_barrier();
      if constexpr (s + G1_NB < G1_NS) issue(s + G1_NB, s % G1_NB);
      constexpr int bo = ((s + 1) % G1_NB) * G1_STG;
#pragma unroll
      for (int i = 0; i < 2; ++i) af[nxt][i] = *(const u4*)(rdA + bo + i * 1024);
#pragma unroll
      for (int i = 0; i < 7; ++i) wf[nxt][i] = *(const u4*)(rdB + bo + i * 1024);
    }
#pragma unroll
    for (int i = 0; i < 7; ++i)
#pragma unroll
      for (int m = 0; m < 2; ++m) acc[i][m] = mfma_b(wf[cur][i], af[cur][m], acc[i][m]);
  });
  const int g = l >> 4, r16 = l & 15;
#pragma unroll
  for (int i = 0; i < 7; ++i)
#pragma unroll
    for (int m = 0; m < 2; ++m) {
      unsigned p0 = pk_bf16(acc[i][m][0], acc[i][m][1]), p1 = pk_bf16(acc[i][m][2], acc[i][m][3]);
      float a0 = geglu(bf_lo(p0), bf_hi(p0)), a1 = geglu(bf_lo(p1), bf_hi(p1));
      *(unsigned*)(smem + ((7 * wn + i) * 128 + 16 * (2 * wm + m) + r16) * 16 + 4 * g) = pk_bf16(a0, a1);
    }
  raw_barrier();
  u4* Ap = (u4*)(ws + OFF_AP);
#pragma unroll
  for (int it = 0; it < 4; ++it) {
    int id = tid + 512 * it;
    if (id < 14 * 128) {
      int blk = id >> 7, m = id & 127, B = nb0 + blk;
      Ap[((size_t)(B >> 2) * 128 + mb0 + (m >> 4)) * 64 + (m & 15) + 16 * (B & 3)] = *(const u4*)(smem + id * 16);
    }
  }
}

constexpr int G2_STG = 18 * 1024, G2_NB = 8, G2_NS = 27;
__global__ __launch_bounds__(512, 2) void k5_kernel(u16* __restrict__ out, char* __restrict__ ws) {
  extern __shared__ __attribute__((aligned(16))) char smem[];
  const int b = blockIdx.x, tid = threadIdx.x, l = tid & 63;
  const int w = __builtin_amdgcn_readfirstlane(tid >> 6);
  const int xcd = b & 7, idx = b >> 3;
  const int mt = 8 * (xcd & 3) + (idx & 7), nt = 4 * (xcd >> 2) + (idx >> 3);
  const int mb0 = 4 * mt, db0 = 5 * nt;
  const u4* Ap = (const u4*)(ws + OFF_AP);
  const u4* W2 = (const u4*)(ws + OFF_W2);
  const u4* src[3];
  int strd[3];
#pragma unroll
  for (int t = 0; t < 3; ++t) {
    int fi = w + 8 * t; if (fi > 17) fi = 17;
    int ks = fi >= 9, r = fi - 9 * ks;
    bool isA = r < 4;
    src[t] = (isA ? Ap + (ks * 128 + mb0 + r) * 64 : W2 + (ks * 40 + db0 + r - 4) * 64) + l;
    strd[t] = isA ? 2 * 128 * 64 : 2 * 40 * 64;
  }
  const bool three = w < 2;
  auto issue = [&](int st, int buf) {
    char* d = smem + buf * G2_STG + w * 1024;
    GLDS(src[0] + st * strd[0], d);
    GLDS(src[1] + st * strd[1], d + 8 * 1024);
    if (three) GLDS(src[2] + st * strd[2], d + 16 * 1024);
  };
  const int kp = w >> 2, mb = w & 3, g = l >> 4, r16 = l & 15;
  const int mp = 16 * (mb0 + mb) + r16;
  const int srow = srow_of(mp >> 3, (mp >> 1) & 3, mp & 1);
  u16* orow = out + (size_t)srow * DD + 16 * db0 + 4 * g;
  u2 hv[5];
  if (kp == 0) {
#pragma unroll
    for (int i = 0; i < 5; ++i) hv[i] = *(const u2*)(orow + 16 * i);
  }
#pragma unroll
  for (int s = 0; s < G2_NB; ++s) issue(s, s);
  const char* rdA = smem + (9 * kp + mb) * 1024 + l * 16;
  const char* rdB = smem + (9 * kp + 4) * 1024 + l * 16;
  f4 acc[5];
#pragma unroll
  for (int i = 0; i < 5; ++i) acc[i] = f4{0.f, 0.f, 0.f, 0.f};
  u4 af[2], wf[2][5];
  if (three) wait_vm<3 * (G2_NB - 1)>(); else wait_vm<2 * (G2_NB - 1)>();
  raw_barrier();
  af[0] = *(const u4*)(rdA);
#pragma unroll
  for (int i = 0; i < 5; ++i) wf[0][i] = *(const u4*)(rdB + i * 1024);
  static_for<G2_NS>([&](auto S_) {
    constexpr int s = decltype(S_)::value;
    constexpr int cur = s & 1, nxt = cur ^ 1;
    if constexpr (s + 1 < G2_NS) {
      constexpr int last = (s + G2_NB - 1 < G2_NS - 1) ? s + G2_NB - 1 : G2_NS - 1;
      constexpr int allow = last - (s + 1);
      if (three) wait_vm<3 * allow>(); else wait_vm<2 * allow>();
      raw_barrier();
      if constexpr (s + G2_NB < G2_NS) issue(s + G2_NB, s % G2_NB);
      constexpr int bo = ((s + 1) % G2_NB) * G2_STG;
      af[nxt] = *(const u4*)(rdA + bo);
#pragma unroll
      for (int i = 0; i < 5; ++i) wf[nxt][i] = *(const u4*)(rdB + bo + i * 1024);
    }
#pragma unroll
    for (int i = 0; i < 5; ++i) acc[i] = mfma_b(wf[cur][i], af[cur], acc[i]);
  });
  f4* red = (f4*)smem;
  if (kp == 1) {
#pragma unroll
    for (int i = 0; i < 5; ++i) red[(mb * 5 + i) * 64 + l] = acc[i];
  }
  raw_barrier();
  if (kp == 0) {
#pragma unroll
    for (int i = 0; i < 5; ++i) {
      f4 o = acc[i] + red[(mb * 5 + i) * 64 + l];
      unsigned o01 = pk_bf16(o[0], o[1]), o23 = pk_bf16(o[2], o[3]);
      u2 st = {pk_bf16(bf_lo(hv[i][0]) + bf_lo(o01), bf_hi(hv[i][0]) + bf_hi(o01)),
               pk_bf16(bf_lo(hv[i][1]) + bf_lo(o23), bf_hi(hv[i][1]) + bf_hi(o23))};
      *(u2*)(orow + 16 * i) = st;
    }
  }
}

extern "C" void kernel_launch(void* const* d_in, const int* in_sizes, int n_in, void* d_out, int out_size, void* d_ws,
                              size_t ws_size, hipStream_t stream) {
  const u16* x = (const u16*)d_in[0];
  const u16* wr = (const u16*)d_in[1];
  const u16* wi = (const u16*)d_in[2];
  const u16* wg = (const u16*)d_in[4];
  const u16* wu = (const u16*)d_in[5];
  const u16* wd = (const u16*)d_in[6];
  u16* out = (u16*)d_out;
  char* ws = (char*)d_ws;
  hipFuncSetAttribute((const void*)k4_kernel, hipFuncAttributeMaxDynamicSharedMemorySize, G1_STG * G1_NB);
  hipFuncSetAttribute((const void*)k5_kernel, hipFuncAttributeMaxDynamicSharedMemorySize, G2_STG * G2_NB);
  k1_kernel<<<256 + 64, 256, 0, stream>>>(x, wr, wi, ws);
  k2_kernel<<<328 + 540, 256, 0, stream>>>(wg, wu, ws);
  k3_kernel<<<256 + 270, 256, 0, stream>>>(x, wd, out, ws);
  k4_kernel<<<256, 512, G1_STG * G1_NB, stream>>>(ws);
  k5_kernel<<<256, 512, G2_STG * G2_NB, stream>>>(out, ws);
}
```

```cpp
#include <hip/hip_runtime.h>
#include <hip/hip_bf16.h>
#include <stdint.h>
#include <utility>

#if defined(__HIP_DEVICE_COMPILE__)
#pragma clang attribute push(__attribute__((target("no-packed-fp32-ops"))), apply_to = function)
#endif

typedef _Float16 h8 __attribute__((ext_vector_type(8)));
typedef _Float16 h2 __attribute__((ext_vector_type(2)));
typedef float f4 __attribute__((ext_vector_type(4)));
typedef float f2 __attribute__((ext_vector_type(2)));
typedef short s8 __attribute__((ext_vector_type(8)));
typedef short s4 __attribute__((ext_vector_type(4)));
typedef unsigned u4 __attribute__((ext_vector_type(4)));
typedef unsigned u4u __attribute__((ext_vector_type(4), aligned(2)));
typedef unsigned u2 __attribute__((ext_vector_type(2)));
typedef unsigned short us2 __attribute__((ext_vector_type(2)));
typedef __bf16 bf2 __attribute__((ext_vector_type(2)));
typedef unsigned short u16;
#define DI __device__ __forceinline__
#define LAS __attribute__((address_space(3)))
#define GAS __attribute__((address_space(1)))

constexpr int DD = 640, FF = 1728;

constexpr size_t OFF_Z1 = 0;
constexpr size_t OFF_Z2 = OFF_Z1 + (size_t)640 * 256 * 16;
constexpr size_t OFF_WT = OFF_Z2 + (size_t)640 * 256 * 16;
constexpr size_t OFF_WM = OFF_WT + (size_t)321 * 512 * 16;
constexpr size_t OFF_HP = OFF_WM + 1024;
constexpr size_t OFF_W1 = OFF_HP + (size_t)20 * 128 * 1024;
constexpr size_t OFF_AP = OFF_W1 + (size_t)20 * 216 * 1024;
constexpr size_t OFF_W2 = OFF_AP + (size_t)54 * 128 * 1024;
constexpr size_t OFF_HH = OFF_W2 + (size_t)54 * 40 * 1024;

#define ST_DECL
#define ST(i)
#define ST_FLUSH(kid)
#define SCHED_FENCE() __builtin_amdgcn_sched_barrier(0)
#define NTS(p, v) (*(p) = (v))
#define Z1L 2
#define Z2L 2
#define K4_BAR2 0
#define Z1IDX(q, k) (Z1L == 0 ? (k) * 256 + (q) : Z1L == 1 ? (q) * 640 + (k) : Z1L == 2 ? (((k) >> 2) * 256 + (q)) * 4 + ((k) & 3) : (((k) >> 3) * 256 + (q)) * 8 + ((k) & 7))
#define Z2IDX(q, k) (Z2L == 0 ? (q) * 640 + (k) : Z2L == 1 ? (k) * 256 + (q) : Z2L == 2 ? (((q) >> 2) * 640 + (k)) * 4 + ((q) & 3) : (((q) >> 3) * 640 + (k)) * 8 + ((q) & 7))

template <class Fn, int... I>
DI void static_for_impl(Fn&& f, std::integer_sequence<int, I...>) { (f(std::integral_constant<int, I>{}), ...); }
template <int N, class Fn>
DI void static_for(Fn&& f) { static_for_impl(f, std::make_integer_sequence<int, N>{}); }

DI unsigned pk_bf16(float a, float b) { f2 v = {a, b}; return __builtin_bit_cast(unsigned, __builtin_convertvector(v, bf2)); }
DI unsigned pk_f16(float a, float b) { f2 v = {a, b}; return __builtin_bit_cast(unsigned, __builtin_convertvector(v, h2)); }
DI float bf_lo(unsigned u) { return __builtin_bit_cast(float, u << 16); }
DI float bf_hi(unsigned u) { return __builtin_bit_cast(float, u & 0xffff0000u); }
DI float rbf(float a) { return bf_hi(pk_bf16(0.f, a)); }
DI float asf(unsigned u) { return __builtin_bit_cast(float, u); }
DI unsigned asu(float f) { return __builtin_bit_cast(unsigned, f); }
DI float fsin(float r) { return __builtin_amdgcn_sinf(r); }
DI float fcos(float r) { return __builtin_amdgcn_cosf(r); }
DI f4 mfma_h(u4 a, u4 b, f4 c) { return __builtin_amdgcn_mfma_f32_16x16x32_f16(__builtin_bit_cast(h8, a), __builtin_bit_cast(h8, b), c, 0, 0, 0); }
DI f4 mfma_b(u4 a, u4 b, f4 c) { return __builtin_amdgcn_mfma_f32_16x16x32_bf16(__builtin_bit_cast(s8, a), __builtin_bit_cast(s8, b), c, 0, 0, 0); }
DI u2 tr_read(const char* p) { return __builtin_bit_cast(u2, __builtin_amdgcn_ds_read_tr16_b64_v4i16((LAS s4*)p)); }
DI unsigned perm_lo(unsigned hi, unsigned lo) { return __builtin_amdgcn_perm(hi, lo, 0x05040100u); }
DI unsigned perm_hi(unsigned hi, unsigned lo) { return __builtin_amdgcn_perm(hi, lo, 0x07060302u); }

template <int CTRL> DI unsigned dppu(unsigned v) { return (unsigned)__builtin_amdgcn_update_dpp(0, (int)v, CTRL, 0xf, 0xf, true); }
template <int CTRL> DI float dpp(float v) { return asf(dppu<CTRL>(__builtin_bit_cast(unsigned, v))); }
DI float swap16_sum(float v) {
  unsigned u = __builtin_bit_cast(unsigned, v);
  auto r = __builtin_amdgcn_permlane16_swap(u, u, false, false);
  return asf((unsigned)r[0]) + asf((unsigned)r[1]);
}
DI float swap32_sum(float v) {
  unsigned u = __builtin_bit_cast(unsigned, v);
  auto r = __builtin_amdgcn_permlane32_swap(u, u, false, false);
  return asf((unsigned)r[0]) + asf((unsigned)r[1]);
}
DI float half_sum(float v) {
  v += dpp<0x121>(v); v += dpp<0x122>(v); v += dpp<0x124>(v); v += dpp<0x128>(v);
  return swap16_sum(v);
}
DI float parity_sum(float v) {
  v += dpp<0x122>(v); v += dpp<0x124>(v); v += dpp<0x128>(v);
  v = swap16_sum(v);
  return swap32_sum(v);
}
DI unsigned umax(unsigned a, unsigned b) { return a > b ? a : b; }
DI unsigned wave_umax(unsigned v) {
  v = umax(v, dppu<0x121>(v)); v = umax(v, dppu<0x122>(v)); v = umax(v, dppu<0x124>(v)); v = umax(v, dppu<0x128>(v));
  auto r = __builtin_amdgcn_permlane16_swap(v, v, false, false);
  v = umax((unsigned)r[0], (unsigned)r[1]);
  auto r2 = __builtin_amdgcn_permlane32_swap(v, v, false, false);
  return umax((unsigned)r2[0], (unsigned)r2[1]);
}
DI void raw_barrier() {
  asm volatile("s_waitcnt lgkmcnt(0)" ::: "memory");
  __builtin_amdgcn_s_barrier();
  asm volatile("" ::: "memory");
}
DI int srow_of(int q, int i, int j) { return 32 * (4 * (q >> 4) + i) + (q & 15) + 16 * j; }
DI unsigned pkmax_abs(unsigned m, unsigned v) {
  us2 a = __builtin_bit_cast(us2, m), b = __builtin_bit_cast(us2, v & 0x7fff7fffu);
  return __builtin_bit_cast(unsigned, __builtin_elementwise_max(a, b));
}

struct RowC { u4 t[9]; };
template <int SGN> DI void rc_F(char* ct, int l, int cb) {
  const float gf = (float)(l >> 4), cf = (float)(16 * cb + (l & 15));
  const float c32 = cf * (SGN / 32.f), a0 = gf * 8.f * c32;
  u4 fr, fi;
#pragma unroll
  for (int jj = 0; jj < 4; ++jj) {
    float r0 = (float)(2 * jj) * c32 + a0, r1 = (float)(2 * jj + 1) * c32 + a0;
    fr[jj] = pk_f16(fcos(r0), fcos(r1));
    fi[jj] = pk_f16(fsin(r0), fsin(r1));
  }
  *(u4*)(ct) = fr; *(u4*)(ct + 1024) = fi;
}
template <int SGN, int OB> DI void rc_G(char* ct, int l) {
  const int r16 = l & 15;
  const float gf = (float)(l >> 4), po4 = (float)(r16 & 1) * 0.25f;
  const float e20 = (float)((r16 >> 1) + 8 * OB) * (-SGN / 20.f), g0 = gf * e20 + po4;
  u4 v;
#pragma unroll
  for (int blk = 0; blk < 4; ++blk) { float r = (float)(4 * blk) * e20 + g0; v[blk] = pk_f16(fcos(r), fsin(r)); }
  *(u4*)(ct + (2 + OB) * 1024) = v;
  float r = 16.f * e20 + g0;
  *(unsigned*)(ct + 5 * 1024 + OB * 4) = pk_f16(fcos(r), fsin(r));
}
template <int SGN, int BLK> DI void rc_T(char* ct, int l, int cb, float tws) {
  const float r = (float)(((l >> 4) + 4 * BLK) * (16 * cb + (l & 15))) * (SGN / 640.f);
  const float tc = fcos(r) * tws, ts = fsin(r) * tws;
  if (BLK < 4) { *(float*)(ct + 6 * 1024 + BLK * 4) = tc; *(float*)(ct + 7 * 1024 + BLK * 4) = ts; }
  else { *(float*)(ct + 5 * 1024 + 12) = tc; *(float*)(ct + 8 * 1024) = ts; }
}
template <int SGN> DI void rc_part(char* ct, int l, int cb, float tws, int part) {
  if (part == 0) rc_F<SGN>(ct, l, cb);
  else if (part == 1) { rc_G<SGN, 0>(ct, l); rc_T<SGN, 0>(ct, l, cb, tws); rc_T<SGN, 1>(ct, l, cb, tws); }
  else if (part == 2) { rc_G<SGN, 1>(ct, l); rc_T<SGN, 2>(ct, l, cb, tws); rc_T<SGN, 3>(ct, l, cb, tws); }
  else { rc_G<SGN, 2>(ct, l); rc_T<SGN, 4>(ct, l, cb, tws); }
}
DI void rc_load(RowC& C, const char* ct) {
#pragma unroll
  for (int j = 0; j < 8; ++j) C.t[j] = *(const u4*)(ct + j * 1024);
  C.t[8][0] = *(const unsigned*)(ct + 8 * 1024);
}
DI void row_stage_a(const RowC& C, const char* lds, int l, int ih, bool ii, u4& T0, u4& T1) {
  const int g = l >> 4, q4 = (l & 15) >> 2, p = l & 3;
  const char* base = lds + (8 * g + q4) * 320 + p * 16 + ih * 8;
  u2 lo[5], hi[5];
#pragma unroll
  for (int blk = 0; blk < 5; ++blk) { lo[blk] = tr_read(base + blk * 64); hi[blk] = tr_read(base + blk * 64 + 4 * 320); }
  T1 = u4{0u, 0u, 0u, 0u};
#pragma unroll
  for (int blk = 0; blk < 5; ++blk) {
    u4 a = {lo[blk][0], lo[blk][1], hi[blk][0], hi[blk][1]};
    f4 z = {0.f, 0.f, 0.f, 0.f};
    f4 pr = mfma_h(a, C.t[0], z);
    f4 pi = mfma_h(a, C.t[1], z);
    float pr0, pr1, pi0, pi1;
    if (ii) { pr0 = pr[2]; pr1 = pr[3]; pi0 = pi[2]; pi1 = pi[3]; } else { pr0 = pr[0]; pr1 = pr[1]; pi0 = pi[0]; pi1 = pi[1]; }
    const float tc = asf(blk < 4 ? C.t[6][blk & 3] : C.t[5][3]), ts = asf(blk < 4 ? C.t[7][blk & 3] : C.t[8][0]);
    float tr = pr0 - pi1, ti = pi0 + pr1;
    unsigned v = pk_f16(tr * tc - ti * ts, ti * tc + tr * ts);
    if (blk < 4) T0[blk & 3] = v; else T1[0] = v;
  }
}

template <bool II> DI void k1_dft(const RowC& C, const char* lin, char* lout, int l, int ih, int cb) {
  u4 T0, T1;
  row_stage_a(C, lin, l, ih, II, T0, T1);
  const int g = l >> 4, cc = 16 * cb + (l & 15);
  char* ob_ = lout + (cc + 64 * g) * 16 + (2 * ih + (int)II) * 4;
#pragma unroll
  for (int ob = 0; ob < 3; ++ob) {
    f4 z = {0.f, 0.f, 0.f, 0.f};
    z = mfma_h(C.t[2 + ob], T0, z);
    u4 ga = {C.t[5][ob], 0u, 0u, 0u};
    z = mfma_h(ga, T1, z);
    *(unsigned*)(ob_ + ob * 4096) = pk_f16(z[0], z[1]);
    *(unsigned*)(ob_ + ob * 4096 + 512) = pk_f16(z[2], z[3]);
  }
}
__global__ __launch_bounds__(512) void k1_kernel(const u16* __restrict__ x, const u16* __restrict__ wr, const u16* __restrict__ wi,
                                                 char* __restrict__ ws) {
  __shared__ __attribute__((aligned(16))) char smem[10240 + 12288 + 18432];
  const int b = blockIdx.x, tid = threadIdx.x, l = tid & 63;
  const int w = __builtin_amdgcn_readfirstlane(tid >> 6);
  ST_DECL; ST(0);
  if (b >= 256) {
    const int task = b - 256, u = task & 7, v = (task >> 3) & 7, hf = task >> 6;
    u4 d[3];
#pragma unroll
    for (int it = 0; it < 3; ++it) {
      int idx = tid + 512 * it; if (idx >= 1280) idx = tid;
      int rho = idx / 20, pc = idx - rho * 20;
      int row = 64 * (4 * v + (rho & 3)) + 8 * u + ((rho >> 2) & 7);
      const u16* src = (rho >= 32 ? wi : wr) + (size_t)row * 321 + 160 * hf + 8 * pc;
      d[it] = *(const u4u*)src;
    }
    unsigned last = 0;
    if (tid < 64) {
      int row = 64 * (4 * v + (tid & 3)) + 8 * u + ((tid >> 2) & 7);
      last = ((tid >= 32 ? wi : wr) + (size_t)row * 321)[320];
    }
    SCHED_FENCE();
    unsigned mx = last & 0x7fffu;
#pragma unroll
    for (int it = 0; it < 3; ++it) {
      int idx = tid + 512 * it; if (idx >= 1280) idx = tid;
      int rho = idx / 20, pc = idx - rho * 20;
      *(u4*)(smem + rho * 336 + pc * 16) = d[it];
#pragma unroll
      for (int e = 0; e < 4; ++e) mx = pkmax_abs(mx, d[it][e]);
    }
    if (tid < 64) *(u16*)(smem + tid * 336 + 320) = (u16)last;
    mx = wave_umax(umax(mx & 0xffffu, mx >> 16));
    if (l == 0) *(unsigned*)(smem + 21504 + w * 4) = mx;
    raw_barrier();
    u4* Wt = (u4*)(ws + OFF_WT);
    const int obase = ((u >> 1) * 2 + (v >> 2)) * 64 + 16 * (v & 3) + 8 * (u & 1);
    const int g = l >> 4, q4 = (l & 15) >> 2, p = l & 3;
#pragma unroll
    for (int rd = 0; rd < 3; ++rd) {
      int job = w + 8 * rd;
      if (job < 20) {
        int cbk = job >> 1, r8 = 4 * (job & 1) + g;
        const char* a = smem + (r8 * 4 + q4) * 336 + (16 * cbk + 4 * p) * 2;
        u2 re = tr_read(a), im = tr_read(a + 32 * 336);
        u4 o = {perm_lo(im[0], re[0]), perm_hi(im[0], re[0]), perm_lo(im[1], re[1]), perm_hi(im[1], re[1])};
        NTS(&Wt[(160 * hf + 16 * cbk + (l & 15)) * 512 + obase + r8], o);
      }
    }
    if (hf == 1 && tid < 8) {
      u4 o;
#pragma unroll
      for (int reg = 0; reg < 4; ++reg)
        o[reg] = (unsigned)*(const u16*)(smem + (tid * 4 + reg) * 336 + 320) | ((unsigned)*(const u16*)(smem + (32 + tid * 4 + reg) * 336 + 320) << 16);
      Wt[320 * 512 + obase + tid] = o;
    }
    if (tid == 0) {
      const unsigned* m = (const unsigned*)(smem + 21504);
      ((unsigned*)(ws + OFF_WM))[task] = umax(umax(umax(m[0], m[1]), umax(m[2], m[3])), umax(umax(m[4], m[5]), umax(m[6], m[7])));
    }
    ST_FLUSH(0);
    return;
  }
  const int q = (b & 7) * 32 + (b >> 3);
  const int ih = (w >> 1) & 1, cb = w & 1;
  char* lin = smem;
  char* lout = smem + 10240;
  char* ct = smem + 22528 + cb * 9216 + l * 16;
  RowC C;
  if (w < 4) {
    const int j = l >> 5, li = l & 31;
    const u2* xr = (const u2*)(x + (size_t)srow_of(q, w, j) * DD);
    u2 xv[5];
#pragma unroll
    for (int c = 0; c < 5; ++c) xv[c] = xr[li + 32 * c];
    float v[20];
#pragma unroll
    for (int c = 0; c < 5; ++c) {
      v[4 * c] = bf_lo(xv[c][0]); v[4 * c + 1] = bf_hi(xv[c][0]);
      v[4 * c + 2] = bf_lo(xv[c][1]); v[4 * c + 3] = bf_hi(xv[c][1]);
    }
    float sa = 0.f, sb = 0.f;
#pragma unroll
    for (int t = 0; t < 10; ++t) { sa += v[2 * t]; sb += v[2 * t + 1]; }
    const float mean = half_sum(sa + sb) * (1.f / 640.f);
    ST(1);
    sa = 0.f; sb = 0.f;
#pragma unroll
    for (int t = 0; t < 10; ++t) { float d0 = v[2 * t] - mean, d1 = v[2 * t + 1] - mean; sa += d0 * d0; sb += d1 * d1; }
    const float var = half_sum(sa + sb) * (1.f / 640.f);
    const float sd = rbf(__builtin_sqrtf(rbf(rbf(var) + 1e-5f)));
    const float rs = 16.f * __builtin_amdgcn_rcpf(sd);
    const float sh = -rbf(mean) * rs;
    char* wp = lin + (4 * li + 2 * j) * 16 + w * 4;
#pragma unroll
    for (int c = 0; c < 5; ++c) {
      unsigned p01 = pk_f16(v[4 * c] * rs + sh, v[4 * c + 1] * rs + sh);
      unsigned p23 = pk_f16(v[4 * c + 2] * rs + sh, v[4 * c + 3] * rs + sh);
      auto r = __builtin_amdgcn_permlane32_swap(p01, p23, false, false);
      unsigned pa = r[0], pb = r[1];
      *(unsigned*)(wp + c * 2048) = perm_lo(pb, pa);
      *(unsigned*)(wp + c * 2048 + 16) = perm_hi(pb, pa);
    }
    ST(2);
    raw_barrier();
    ST(3);
    rc_load(C, ct);
    k1_dft<false>(C, lin, lout, l, ih, cb);
  } else {
    rc_part<-1>(ct, l, cb, 1.f / 256.f, 2 * ih);
    rc_part<-1>(ct, l, cb, 1.f / 256.f, 2 * ih + 1);
    raw_barrier();
    rc_load(C, ct);
    k1_dft<true>(C, lin, lout, l, ih, cb);
  }
  raw_barrier();
  ST(4);
  u4* Z1 = (u4*)(ws + OFF_Z1);
  Z1[Z1IDX(q, tid)] = *(const u4*)(lout + tid * 16);
  if (tid < 128) Z1[Z1IDX(q, tid + 512)] = *(const u4*)(lout + (tid + 512) * 16);
  ST_FLUSH(0);
}

DI unsigned wmax_exp(u2 m) {
  unsigned eb = wave_umax(umax(m[0], m[1])) >> 7;
  return eb > 251u ? 251u : eb;
}
__global__ __launch_bounds__(256) void k2_kernel(const u16* __restrict__ wg, const u16* __restrict__ wu, char* __restrict__ ws) {
  __shared__ __attribute__((aligned(16))) char smem[32 * 880];
  const int b = blockIdx.x, tid = threadIdx.x, l = tid & 63;
  const int w = __builtin_amdgcn_readfirstlane(tid >> 6);
  const int g = l >> 4, r16 = l & 15;
  ST_DECL; ST(0);
  if (b >= 328) {
    const int fg = b & 7, ks = (b - 328) >> 3;
    u4 a[4], c[4];
#pragma unroll
    for (int it = 0; it < 4; ++it) {
      int idx = tid + 256 * it; idx = idx < 864 ? idx : tid;
      int k = idx / 27, seg = idx - k * 27;
      const size_t off = (size_t)(32 * ks + k) * FF + 216 * fg + 8 * seg;
      a[it] = *(const u4*)(wg + off); c[it] = *(const u4*)(wu + off);
    }
    SCHED_FENCE();
#pragma unroll
    for (int it = 0; it < 4; ++it) {
      int idx = tid + 256 * it; idx = idx < 864 ? idx : tid;
      int k = idx / 27, seg = idx - k * 27;
      u4 o0 = {perm_lo(c[it][0], a[it][0]), perm_hi(c[it][0], a[it][0]), perm_lo(c[it][1], a[it][1]), perm_hi(c[it][1], a[it][1])};
      u4 o1 = {perm_lo(c[it][2], a[it][2]), perm_hi(c[it][2], a[it][2]), perm_lo(c[it][3], a[it][3]), perm_hi(c[it][3], a[it][3])};
      *(u4*)(smem + k * 880 + seg * 32) = o0;
      *(u4*)(smem + k * 880 + seg * 32 + 16) = o1;
    }
    raw_barrier();
    u4* W1 = (u4*)(ws + OFF_W1) + (size_t)(ks * 216 + fg * 27 + w) * 64 + l;
    const char* base = smem + (8 * g + (r16 >> 2)) * 880 + (l & 3) * 8 + w * 32;
#pragma unroll
    for (int t = 0; t < 7; ++t) {
      if (t < 6 || w < 3) {
        u2 lo = tr_read(base + t * 128), hi = tr_read(base + t * 128 + 4 * 880);
        NTS(&W1[t * 256], (u4{lo[0], lo[1], hi[0], hi[1]}));
      }
    }
    ST_FLUSH(1);
    return;
  }
  const int xi = b >> 3;
  int kd = 40 * (b & 7) + xi;
  if (xi == 40) { if ((b & 7) != 7) return; kd = 320; }
  const int kdm = kd == 0 ? 0 : 640 - kd;
  const u4* Z1 = (const u4*)(ws + OFF_Z1);
  const u4 z1 = Z1[Z1IDX(tid, kd)], z2 = Z1[Z1IDX(tid, kdm)];
  u4 wv[2];
  {
    const u4* Wt = (const u4*)(ws + OFF_WT) + kd * 512 + 128 * w + l;
    wv[0] = Wt[0]; wv[1] = Wt[64];
  }
  const u2 wmv = ((const u2*)(ws + OFF_WM))[l];
  SCHED_FENCE();
  u4 M1[4], M2[4], N1[4], N2[4];
  float TC[4][4], TS[4][4];
  {
    const float g4 = (float)(4 * g), k1f = (float)(16 * w + r16);
    const float ka = k1f * (1.f / 64.f), ka0 = g4 * ka;
    const bool odd = r16 & 1;
    const unsigned s1 = (r16 & 2) ? 0x80008000u : 0u;
    const unsigned s1b = (((r16 & 3) + 1) & 2) ? 0x80008000u : 0u;
    const unsigned s2 = odd ? 0x80008000u : 0u;
    const unsigned s3 = ((3 * (r16 & 3)) & 2) ? 0x80008000u : 0u, s3b = ((3 * (r16 & 3) + 1) & 2) ? 0x80008000u : 0u;
#pragma unroll
    for (int jj = 0; jj < 4; ++jj) {
      float r = (float)jj * ka + ka0, c = fcos(r), sn = fsin(r);
      const unsigned a = pk_f16(c, sn), bq = pk_f16(-sn, c);
      const unsigned p = odd ? bq : a, q2 = odd ? a : bq;
      M1[0][jj] = a; M2[0][jj] = bq;
      M1[1][jj] = p ^ s1; M2[1][jj] = q2 ^ s1b;
      M1[2][jj] = a ^ s2; M2[2][jj] = bq ^ s2;
      M1[3][jj] = p ^ s3; M2[3][jj] = q2 ^ s3b;
    }
    const float ro = (float)r16 * (1.f / 32.f), ro0 = g4 * ro;
#pragma unroll
    for (int jj = 0; jj < 4; ++jj) {
      float r = (float)jj * ro + ro0, c = fcos(r), sn = fsin(r);
      const unsigned a = pk_f16(c, sn), bq = pk_f16(-sn, c), sj = (jj & 1) ? 0x80008000u : 0u;
      N1[0][jj] = a; N2[0][jj] = bq;
      N1[1][jj] = a ^ s2; N2[1][jj] = bq ^ s2;
      N1[2][jj] = a ^ sj; N2[2][jj] = bq ^ sj;
      N1[3][jj] = a ^ (s2 ^ sj); N2[3][jj] = bq ^ (s2 ^ sj);
    }
    {
      const float kb = k1f * (1.f / 2048.f);
      const float c1 = fcos(kb), d1 = fsin(kb), c16 = fcos(16.f * kb), d16 = fsin(16.f * kb);
      TC[0][0] = fcos(g4 * kb); TS[0][0] = fsin(g4 * kb);
#pragma unroll
      for (int rg = 1; rg < 4; ++rg) {
        TC[0][rg] = TC[0][rg - 1] * c1 - TS[0][rg - 1] * d1; TS[0][rg] = TS[0][rg - 1] * c1 + TC[0][rg - 1] * d1;
      }
#pragma unroll
      for (int rg = 0; rg < 4; ++rg) { TC[1][rg] = TC[0][rg] * c16 - TS[0][rg] * d16; TS[1][rg] = TS[0][rg] * c16 + TC[0][rg] * d16; }
    }
#pragma unroll
    for (int xx = 0; xx < 2; ++xx) {
      const float nu = (float)(16 * xx + r16) * (1.f / 2048.f), nu0 = ((float)(16 * w) + g4) * nu;
      const float c1 = fcos(nu), d1 = fsin(nu);
      TC[2 + xx][0] = fcos(nu0); TS[2 + xx][0] = fsin(nu0);
#pragma unroll
      for (int rg = 1; rg < 4; ++rg) {
        TC[2 + xx][rg] = TC[2 + xx][rg - 1] * c1 - TS[2 + xx][rg - 1] * d1;
        TS[2 + xx][rg] = TS[2 + xx][rg - 1] * c1 + TC[2 + xx][rg - 1] * d1;
      }
    }
  }
  SCHED_FENCE();
  u4* sa = (u4*)smem;
  u4* ex = (u4*)(smem + 8192);
  {
    u4 A0, A1;
#pragma unroll
    for (int jj = 0; jj < 4; ++jj) {
      h2 p = __builtin_bit_cast(h2, (unsigned)z1[jj]), qq = __builtin_bit_cast(h2, (unsigned)z2[jj]);
      h2 qa = {qq[0], -qq[1]};
      h2 pb = {p[1], -p[0]}, qb = {qq[1], qq[0]};
      A0[jj] = __builtin_bit_cast(unsigned, p + qa);
      A1[jj] = __builtin_bit_cast(unsigned, pb + qb);
    }
    sa[tid] = A0;
    sa[256 + tid] = A1;
  }
  ST(1);
  raw_barrier();
  const f4 zero = {0.f, 0.f, 0.f, 0.f};
  f4 Tr[2] = {zero, zero}, Ti[2] = {zero, zero};
#pragma unroll
  for (int t = 0; t < 4; ++t) {
    u4 A0 = sa[64 * t + l], A1 = sa[256 + 64 * t + l];
    Tr[0] = mfma_h(A0, M1[t], Tr[0]); Tr[1] = mfma_h(A1, M1[t], Tr[1]);
    Ti[0] = mfma_h(A0, M2[t], Ti[0]); Ti[1] = mfma_h(A1, M2[t], Ti[1]);
  }
  ST(2);
  const unsigned eb = wmax_exp(wmv);
  const float fs = asf((252u - eb) << 23) * (1.f / 256.f);
  u4 X[2];
#pragma unroll
  for (int rb = 0; rb < 2; ++rb)
#pragma unroll
    for (int rg = 0; rg < 4; ++rg) {
      float a = Tr[rb][rg], c = Ti[rb][rg], tc = TC[rb][rg], ts = TS[rb][rg];
      X[rb][rg] = pk_f16(a * tc + c * ts, c * tc - a * ts);
    }
  u4 Y[2];
#pragma unroll
  for (int ob = 0; ob < 2; ++ob) {
    f4 xr = mfma_h(N1[2 * ob], X[0], zero); xr = mfma_h(N1[2 * ob + 1], X[1], xr);
    f4 xim = mfma_h(N2[2 * ob], X[0], zero); xim = mfma_h(N2[2 * ob + 1], X[1], xim);
#pragma unroll
    for (int rg = 0; rg < 4; ++rg) {
      float wr_ = bf_lo(wv[ob][rg]) * fs, wi_ = bf_hi(wv[ob][rg]) * fs;
      float yr = xr[rg] * wr_ - xim[rg] * wi_;
      float yi = xr[rg] * wi_ + xim[rg] * wr_;
      Y[ob][rg] = pk_f16(yi, yr);
    }
  }
#pragma unroll
  for (int cb = 0; cb < 2; ++cb) {
    f4 ui = mfma_h(Y[0], N1[2 * cb], zero); ui = mfma_h(Y[1], N1[2 * cb + 1], ui);
    f4 ur = mfma_h(Y[0], N2[2 * cb], zero); ur = mfma_h(Y[1], N2[2 * cb + 1], ur);
    u4 U;
#pragma unroll
    for (int rg = 0; rg < 4; ++rg) {
      float tc = TC[2 + cb][rg], ts = TS[2 + cb][rg];
      U[rg] = pk_f16(ui[rg] * tc + ur[rg] * ts, ur[rg] * tc - ui[rg] * ts);
    }
    ex[(w * 2 + cb) * 64 + l] = U;
  }
  ST(3);
  raw_barrier();
  ST(4);
  f4 Vi[2] = {zero, zero}, Vr[2] = {zero, zero};
#pragma unroll
  for (int t = 0; t < 4; ++t)
#pragma unroll
    for (int cb = 0; cb < 2; ++cb) {
      u4 U = ex[(t * 2 + cb) * 64 + l];
      Vi[cb] = mfma_h(M1[t], U, Vi[cb]);
      Vr[cb] = mfma_h(M2[t], U, Vr[cb]);
    }
  u4* Z2 = (u4*)(ws + OFF_Z2);
  const float sg = (kd == 0 || kd == 320) ? 0.f : 1.f;
  u4 o, om;
#pragma unroll
  for (int rg = 0; rg < 4; ++rg) {
    float ia = Vi[0][rg] * sg, ib = Vi[1][rg] * sg;
    o[rg] = pk_f16(Vr[0][rg] - ib, ia + Vr[1][rg]);
    om[rg] = pk_f16(Vr[0][rg] + ib, Vr[1][rg] - ia);
  }
  Z2[Z2IDX(tid, kd)] = o;
  if (kdm != kd) Z2[Z2IDX(tid, kdm)] = om;
  ST_FLUSH(1);
}

__global__ __launch_bounds__(512) void k3_kernel(const u16* __restrict__ x, const u16* __restrict__ wd, char* __restrict__ ws) {
  __shared__ __attribute__((aligned(16))) char smem[32 * 1296];
  const int b = blockIdx.x, tid = threadIdx.x, l = tid & 63;
  const int w = __builtin_amdgcn_readfirstlane(tid >> 6);
  const int g = l >> 4, r16 = l & 15;
  ST_DECL; ST(0);
  if (b >= 256) {
    const int ks = b - 256;
    if (ks >= 54) return;
    const u4* src = (const u4*)(wd + (size_t)32 * ks * DD) + tid;
    u4 a[5];
#pragma unroll
    for (int it = 0; it < 5; ++it) a[it] = src[512 * it];
    SCHED_FENCE();
#pragma unroll
    for (int it = 0; it < 5; ++it) {
      int idx = tid + 512 * it, k = idx / 80, seg = idx - k * 80;
      *(u4*)(smem + k * 1296 + seg * 16) = a[it];
    }
    raw_barrier();
    u4* W2 = (u4*)(ws + OFF_W2) + (size_t)(ks * 40 + 5 * w) * 64 + l;
    const char* base = smem + (8 * g + (r16 >> 2)) * 1296 + (l & 3) * 8 + w * 160;
#pragma unroll
    for (int t = 0; t < 5; ++t) {
      u2 lo = tr_read(base + t * 32), hi = tr_read(base + t * 32 + 4 * 1296);
      NTS(&W2[t * 64], (u4{lo[0], lo[1], hi[0], hi[1]}));
    }
    ST_FLUSH(2);
    return;
  }
  const int q = 128 * (b & 1) + 32 * ((b >> 1) & 3) + (b >> 3);
  const int ih = (w >> 1) & 1, cb = w & 1, ii = w >> 2, j = l & 1;
  const u4* Z2 = (const u4*)(ws + OFF_Z2);
  const u4 zv0 = Z2[Z2IDX(q, tid)];
  const u4 zv1 = Z2[Z2IDX(q, 512 + (tid & 127))];
  const int e0 = r16 >> 1;
  const bool v2 = e0 < 4;
  const int srow = srow_of(q, 2 * ih + ii, j);
  const size_t roff = (size_t)srow * DD + 16 * cb + 4 * g + 32 * e0;
  u2 xv[3];
  xv[0] = *(const u2*)(x + roff); xv[1] = *(const u2*)(x + roff + 256); xv[2] = *(const u2*)(x + roff + (v2 ? 512 : 0));
  const u2 wmv = ((const u2*)(ws + OFF_WM))[l];
  SCHED_FENCE();
  char* ct = smem + 10496 + cb * 9216 + l * 16;
  rc_part<1>(ct, l, cb, 1.f / 32.f, w >> 1);
  SCHED_FENCE();
  *(u4*)(smem + tid * 16) = zv0;
  if (tid < 128) *(u4*)(smem + (512 + tid) * 16) = zv1;
  ST(1);
  raw_barrier();
  ST(2);
  RowC C;
  rc_load(C, ct);
  u4 T0, T1;
  if (ii) row_stage_a(C, smem, l, ih, true, T0, T1); else row_stage_a(C, smem, l, ih, false, T0, T1);
  const unsigned eb = wmax_exp(wmv);
  const float ysc = asf((eb + 2u) << 23) * 0.05f;
  ST(3);
  float h[12];
  float s = 0.f;
  const int mp = 8 * q + 2 * (2 * ih + ii) + j;
  u2* Hh = (u2*)(ws + OFF_HH) + ((size_t)(2 * e0 + cb) * 2048 + mp) * 4 + g;
#pragma unroll
  for (int ob = 0; ob < 3; ++ob) {
    f4 z = {0.f, 0.f, 0.f, 0.f};
    z = mfma_h(T0, C.t[2 + ob], z);
    u4 gb = {C.t[5][ob], 0u, 0u, 0u};
    z = mfma_h(T1, gb, z);
    unsigned y01 = pk_bf16(z[0] * ysc, z[1] * ysc), y23 = pk_bf16(z[2] * ysc, z[3] * ysc);
    unsigned h01 = pk_bf16(bf_lo(xv[ob][0]) + bf_lo(y01), bf_hi(xv[ob][0]) + bf_hi(y01));
    unsigned h23 = pk_bf16(bf_lo(xv[ob][1]) + bf_lo(y23), bf_hi(xv[ob][1]) + bf_hi(y23));
    if (ob == 2) { h01 = v2 ? h01 : 0u; h23 = v2 ? h23 : 0u; }
    if (ob < 2 || v2) NTS(&Hh[(size_t)ob * 16 * 2048 * 4], (u2{h01, h23}));
    h[4 * ob] = bf_lo(h01); h[4 * ob + 1] = bf_hi(h01); h[4 * ob + 2] = bf_lo(h23); h[4 * ob + 3] = bf_hi(h23);
    s += (h[4 * ob] + h[4 * ob + 1]) + (h[4 * ob + 2] + h[4 * ob + 3]);
  }
  const float mloc = parity_sum(s) * (1.f / 320.f);
  float s2 = 0.f, s3 = 0.f;
#pragma unroll
  for (int t = 0; t < 8; ++t) { float d = h[t] - mloc; s2 += d * d; }
#pragma unroll
  for (int t = 8; t < 12; ++t) { float d = h[t] - mloc; s3 += d * d; }
  const float m2loc = parity_sum(v2 ? s2 + s3 : s2);
  f2* ex = (f2*)(smem + 10240);
  if (l < 2) ex[w * 2 + l] = f2{mloc, m2loc};
  ST(4);
  raw_barrier();
  ST(5);
  {
    const f2 o = ex[(w ^ 1) * 2 + j];
    const float mean = 0.5f * (mloc + o[0]);
    const float dl = mloc - o[0];
    const float var = (m2loc + o[1] + dl * dl * 160.f) * (1.f / 640.f);
    const float mb = rbf(mean);
    const float sd = rbf(__builtin_sqrtf(rbf(rbf(var) + 1e-5f)));
    const float rs = __builtin_amdgcn_rcpf(sd);
    const int slot = (mp >> 4) * 64 + (mp & 15) + 16 * (2 * cb + (g >> 1));
    u2* Hp = (u2*)(ws + OFF_HP) + ((size_t)e0 * 128 * 64 + slot) * 2 + (g & 1);
#pragma unroll
    for (int ob = 0; ob < 3; ++ob) {
      unsigned d01 = pk_bf16(h[4 * ob] - mb, h[4 * ob + 1] - mb);
      unsigned d23 = pk_bf16(h[4 * ob + 2] - mb, h[4 * ob + 3] - mb);
      u2 st = {pk_bf16(bf_lo(d01) * rs, bf_hi(d01) * rs), pk_bf16(bf_lo(d23) * rs, bf_hi(d23) * rs)};
      if (ob < 2 || v2) NTS(&Hp[(size_t)ob * 8 * 128 * 64 * 2], st);
    }
  }
  ST_FLUSH(2);
}

#define GLDS(src, dst) __builtin_amdgcn_global_load_lds((const GAS void*)(src), (LAS void*)(dst), 16, 0, 0)
template <int N> DI void wait_vm() { asm volatile("s_waitcnt vmcnt(%0)" ::"n"(N) : "memory"); }
#define SGB(mask, n) __builtin_amdgcn_sched_group_barrier(mask, n, 0)

DI float geglu(float g, float u) {
  float t = g * g;
  float z = g * (t * (-2.f * 1.4426950408889634f * 0.7978845608028654f * 0.044715f) + (-2.f * 1.4426950408889634f * 0.7978845608028654f));
  float e = __builtin_amdgcn_exp2f(z);
  return (g * u) * __builtin_amdgcn_rcpf(1.f + e);
}

constexpr int G1_STG = 22 * 1024, G1_NB = 7, G1_NS = 20, G1_TAIL = 4, G1_MAIN = G1_NS - G1_TAIL;
template <int NL> DI void k4_loader(char* smem, const char* ws, int mb0, int nb0, unsigned loff, int j, int xcd, int idx) {
  const char* sA = ws + OFF_HP + (size_t)(mb0 + j) * 1024 + loff;
  const char* sB = ws + OFF_W1 + (size_t)(nb0 + j) * 1024 + loff;
  auto issue = [&](int ks, int buf) {
    char* d = smem + buf * G1_STG + j * 1024;
    GLDS(sA + (size_t)ks * (128 * 1024), d);
    GLDS(sA + (size_t)ks * (128 * 1024) + 4096, d + 4096);
#pragma unroll
    for (int t = 0; t < NL - 2; ++t) GLDS(sB + (size_t)ks * (216 * 1024) + t * 4096, d + 8192 + t * 4096);
  };
  issue(0, 0); issue(1, 1); issue(2, 2);
  wait_vm<NL * 2>();
  raw_barrier();
  issue(3, 3); issue(4, 4);
  wait_vm<NL * 3>();
  raw_barrier();
  issue(5, 5); issue(6, 6); issue(7, 0);
  static_for<G1_MAIN - 2>([&](auto S_) {
    constexpr int s = decltype(S_)::value + 1;
    constexpr int last = (s + G1_NB - 1 < G1_NS - 1) ? s + G1_NB - 1 : G1_NS - 1;
    wait_vm<NL * (last - (s + 1))>();
    raw_barrier();
    if constexpr (s + G1_NB < G1_NS) issue(s + G1_NB, s % G1_NB);
  });
  wait_vm<0>();
  raw_barrier();
  const char* w2 = ws + OFF_W2 + (size_t)20 * (xcd >> 2) * 1024 + loff;
#pragma unroll
  for (int it = 0; it < 9; ++it) {
    int f = idx * 34 + it * 4 + j; f = f < 1080 ? f : 1079;
    int ks = f / 20, db = f - ks * 20;
    GLDS(w2 + (size_t)(ks * 40 + db) * 1024, smem + (it * 4 + j) * 1024);
  }
  wait_vm<0>();
}
__global__ __launch_bounds__(768, 3) void k4_kernel(char* __restrict__ ws) {
  extern __shared__ __attribute__((aligned(16))) char smem[];
  const int b = blockIdx.x, tid = threadIdx.x, l = tid & 63;
  const int w = __builtin_amdgcn_readfirstlane(tid >> 6);
  const int xcd = b & 7, idx = b >> 3;
  const int mt = 8 * (xcd & 1) + (idx & 7), nt = 4 * (xcd >> 1) + (idx >> 3);
  ST_DECL; ST(0);
  const int nb0 = (27 * nt) >> 1, mb0 = 8 * mt;
  const unsigned loff = l * 16;
  if (w >= 8) {
    if (w < 10) k4_loader<6>(smem, ws, mb0, nb0, loff, w - 8, xcd, idx); else k4_loader<5>(smem, ws, mb0, nb0, loff, w - 8, xcd, idx);
    return;
  }
  f4 acc[7][2];
#pragma unroll
  for (int i = 0; i < 7; ++i) { acc[i][0] = f4{0.f, 0.f, 0.f, 0.f}; acc[i][1] = f4{0.f, 0.f, 0.f, 0.f}; }
  {
    const char* rdA = smem + (2 * (w >> 1)) * 1024 + loff;
    const char* rdB = smem + (8 + 7 * (w & 1)) * 1024 + loff;
    u4 af[2][2], wf[2][7];
    ST(1);
    raw_barrier();
    ST(2);
#pragma unroll
    for (int i = 0; i < 2; ++i) af[0][i] = *(const u4*)(rdA + i * 1024);
#pragma unroll
    for (int i = 0; i < 7; ++i) wf[0][i] = *(const u4*)(rdB + i * 1024);
    static_for<G1_MAIN>([&](auto S_) {
      constexpr int s = decltype(S_)::value;
      constexpr int cur = s & 1, nxt = cur ^ 1;
      constexpr bool more = s + 1 < G1_MAIN;
      if constexpr (more) {
        if constexpr (!K4_BAR2 || (s & 1)) raw_barrier();
        constexpr int bo = ((s + 1) % G1_NB) * G1_STG;
#pragma unroll
        for (int i = 0; i < 2; ++i) af[nxt][i] = *(const u4*)(rdA + bo + i * 1024);
#pragma unroll
        for (int i = 0; i < 7; ++i) wf[nxt][i] = *(const u4*)(rdB + bo + i * 1024);
      }
#pragma unroll
      for (int i = 0; i < 7; ++i)
#pragma unroll
        for (int m = 0; m < 2; ++m) acc[i][m] = mfma_b(wf[cur][i], af[cur][m], acc[i][m]);
      if constexpr (more) {
#pragma unroll
        for (int i = 0; i < 9; ++i) { SGB(0x8, 1); SGB(0x100, 1); }
        SGB(0x8, 14);
      }
      SCHED_FENCE();
      if constexpr (s == 6) ST(3);
      if constexpr (s == 13) ST(4);
    });
  }
  raw_barrier();
  ST(5);
  const int g = l >> 4, r16 = l & 15, wm = w >> 1, wn = w & 1;
  const char* rdA = smem + (2 * wm) * 1024 + loff;
  const char* rdB = smem + (8 + 7 * wn) * 1024 + loff;
  u4 af[G1_TAIL][2], wf[2][G1_TAIL];
#pragma unroll
  for (int t = 0; t < G1_TAIL; ++t) {
    const int bo = ((G1_MAIN + t) % G1_NB) * G1_STG;
    wf[0][t] = *(const u4*)(rdB + bo);
    af[t][0] = *(const u4*)(rdA + bo); af[t][1] = *(const u4*)(rdA + bo + 1024);
  }
  char* apb = ws + OFF_AP + (size_t)(mb0 + 2 * wm) * 1024 + r16 * 16 + 4 * g;
  auto epi = [&](int i) {
    const int B = nb0 + 7 * wn + i;
    char* d = apb + (size_t)(B >> 2) * (128 * 1024) + (B & 3) * 256;
#pragma unroll
    for (int m = 0; m < 2; ++m)
      NTS((unsigned*)(d + m * 1024), pk_bf16(geglu(acc[i][m][0], acc[i][m][1]), geglu(acc[i][m][2], acc[i][m][3])));
  };
  static_for<7>([&](auto I_) {
    constexpr int i = decltype(I_)::value;
    if constexpr (i + 1 < 7) {
#pragma unroll
      for (int t = 0; t < G1_TAIL; ++t) wf[(i + 1) & 1][t] = *(const u4*)(rdB + ((G1_MAIN + t) % G1_NB) * G1_STG + (i + 1) * 1024);
    }
#pragma unroll
    for (int t = 0; t < G1_TAIL; ++t)
#pragma unroll
      for (int m = 0; m < 2; ++m) acc[i][m] = mfma_b(wf[i & 1][t], af[t][m], acc[i][m]);
    if constexpr (i > 0) epi(i - 1);
    if constexpr (i > 0) {
#pragma unroll
      for (int t = 0; t < 8; ++t) { SGB(0x8, 1); if (t < 4 && i + 1 < 7) SGB(0x100, 1); SGB(0x2, 4); SGB(0x400, 1); }
    }
    SCHED_FENCE();
  });
  epi(6);
  ST(6);
  ST_FLUSH(3);
}

constexpr int G2_D = 5, G2_NS = 13;
__global__ __launch_bounds__(256, 1) void k5_kernel(u16* __restrict__ out, char* __restrict__ ws) {
  __shared__ __attribute__((aligned(16))) char smem[60 * 1024];
  const int b = blockIdx.x, tid = threadIdx.x, l = tid & 63;
  const int w = __builtin_amdgcn_readfirstlane(tid >> 6);
  const int xcd = b & 7, idx = b >> 3;
  const int mt = 16 * (xcd & 1) + 8 * ((xcd >> 1) & 1) + (idx & 7), nt = 4 * (xcd >> 2) + (idx >> 3);
  const int mb0 = 4 * mt, db0 = 5 * nt;
  ST_DECL; ST(0);
  const unsigned loff = l * 16;
  const char* pa = ws + OFF_AP + (size_t)(w * 128 + mb0) * 1024;
  const char* pw = ws + OFF_W2 + (size_t)(w * 40 + db0 + 2) * 1024;
  const int g = l >> 4, r16 = l & 15;
  f4 acc[4][5];
#pragma unroll
  for (int m = 0; m < 4; ++m)
#pragma unroll
    for (int n = 0; n < 5; ++n) acc[m][n] = f4{0.f, 0.f, 0.f, 0.f};
  u4 fa[G2_D + 1][4], fw[G2_D + 1][5];
  auto load = [&](int i, u4 (&a)[4], u4 (&wv)[5]) {
    const char* p = pa + (size_t)i * (4 * 128 * 1024) + loff;
    const char* r = pw + (size_t)i * (4 * 40 * 1024) + loff;
#pragma unroll
    for (int m = 0; m < 4; ++m) a[m] = *(const u4*)(p + m * 1024);
#pragma unroll
    for (int n = 0; n < 5; ++n) wv[n] = *(const u4*)(r + (n - 2) * 1024);
  };
  static_for<G2_D>([&](auto I_) { constexpr int i = decltype(I_)::value; load(i, fa[i], fw[i]); SCHED_FENCE(); });
  ST(1);
  const int mp = 16 * (mb0 + w) + r16;
  u16* orow = out + (size_t)srow_of(mp >> 3, (mp >> 1) & 3, mp & 1) * DD + 16 * db0 + 4 * g;
  u2 hv[5];
  static_for<G2_NS>([&](auto S_) {
    constexpr int s = decltype(S_)::value;
    constexpr int cur = s % (G2_D + 1), nx = (s + G2_D) % (G2_D + 1);
    constexpr int nld = s + G2_D < G2_NS ? 9 : (s + G2_D == G2_NS ? 7 : (s + G2_D == G2_NS + 1 ? 5 : 0));
    if constexpr (s + G2_D < G2_NS) load(s + G2_D, fa[nx], fw[nx]);
    if constexpr (s + G2_D == G2_NS) {
      const char* p = ws + OFF_AP + (size_t)((52 + (w >> 1)) * 128 + mb0 + 2 * (w & 1)) * 1024 + loff;
      const char* r = ws + OFF_W2 + (size_t)((52 + (w >> 1)) * 40 + db0 + 2) * 1024 + loff;
      fa[nx][0] = *(const u4*)p; fa[nx][1] = *(const u4*)(p + 1024);
#pragma unroll
      for (int n = 0; n < 5; ++n) fw[nx][n] = *(const u4*)(r + (n - 2) * 1024);
    }
    if constexpr (s + G2_D == G2_NS + 1) {
#pragma unroll
      for (int i = 0; i < 5; ++i) hv[i] = ((const u2*)(ws + OFF_HH))[((size_t)(db0 + i) * 2048 + mp) * 4 + g];
    }
#pragma unroll
    for (int m = 0; m < 4; ++m)
#pragma unroll
      for (int n = 0; n < 5; ++n) acc[m][n] = mfma_b(fw[cur][n], fa[cur][m], acc[m][n]);
#pragma unroll
    for (int i = 0; i < nld; ++i) { SGB(0x8, 1); SGB(0x20, 1); }
    SGB(0x8, 20);
    SCHED_FENCE();
    if constexpr (s == 0) ST(2);
    if constexpr (s == 4) ST(3);
    if constexpr (s == 8) ST(4);
  });
  {
    constexpr int cur = G2_NS % (G2_D + 1);
    if (w & 1) {
#pragma unroll
      for (int m = 0; m < 2; ++m)
#pragma unroll
        for (int n = 0; n < 5; ++n) acc[2 + m][n] = mfma_b(fw[cur][n], fa[cur][m], acc[2 + m][n]);
    } else {
#pragma unroll
      for (int m = 0; m < 2; ++m)
#pragma unroll
        for (int n = 0; n < 5; ++n) acc[m][n] = mfma_b(fw[cur][n], fa[cur][m], acc[m][n]);
    }
  }
  ST(5);
  f4* red = (f4*)smem + l;
  static_for<4>([&](auto M_) {
    constexpr int m = decltype(M_)::value;
    if (w != m) {
      f4* d = red + (m * 3 + ((w - m - 1) & 3)) * 320;
#pragma unroll
      for (int n = 0; n < 5; ++n) d[n * 64] = acc[m][n];
    }
  });
  raw_barrier();
  ST(6);
  const f4* sp = red + w * 3 * 320;
  auto fin = [&](f4 (&own)[5]) {
#pragma unroll
    for (int n = 0; n < 5; ++n) {
      f4 o = (own[n] + sp[n * 64]) + (sp[320 + n * 64] + sp[640 + n * 64]);
      unsigned o01 = pk_bf16(o[0], o[1]), o23 = pk_bf16(o[2], o[3]);
      *(u2*)(orow + 16 * n) = u2{pk_bf16(bf_lo(hv[n][0]) + bf_lo(o01), bf_hi(hv[n][0]) + bf_hi(o01)),
                                 pk_bf16(bf_lo(hv[n][1]) + bf_lo(o23), bf_hi(hv[n][1]) + bf_hi(o23))};
    }
  };
  if (w == 0) fin(acc[0]); else if (w == 1) fin(acc[1]); else if (w == 2) fin(acc[2]); else fin(acc[3]);
  ST_FLUSH(4);
}

#if defined(__HIP_DEVICE_COMPILE__)
#pragma clang attribute pop
#endif

extern "C" void kernel_launch(void* const* d_in, const int* in_sizes, int n_in, void* d_out, int out_size, void* d_ws,
                              size_t ws_size, hipStream_t stream) {
  const u16* x = (const u16*)d_in[0];
  const u16* wr = (const u16*)d_in[1];
  const u16* wi = (const u16*)d_in[2];
  const u16* wg = (const u16*)d_in[4];
  const u16* wu = (const u16*)d_in[5];
  const u16* wd = (const u16*)d_in[6];
  u16* out = (u16*)d_out;
  char* ws = (char*)d_ws;
  (void)hipFuncSetAttribute((const void*)k4_kernel, hipFuncAttributeMaxDynamicSharedMemorySize, G1_STG * G1_NB);
  k1_kernel<<<256 + 128, 512, 0, stream>>>(x, wr, wi, ws);
  k2_kernel<<<328 + 160, 256, 0, stream>>>(wg, wu, ws);
  k3_kernel<<<256 + 56, 512, 0, stream>>>(x, wd, ws);
  k4_kernel<<<256, 768, G1_STG * G1_NB, stream>>>(ws);
  k5_kernel<<<256, 256, 0, stream>>>(out, ws);
}
```

```cpp
#include <hip/hip_runtime.h>
#include <hip/hip_bf16.h>
#include <stdint.h>
#include <utility>

#if defined(__HIP_DEVICE_COMPILE__)
#pragma clang attribute push(__attribute__((target("no-packed-fp32-ops"))), apply_to = function)
#endif

typedef _Float16 h8 __attribute__((ext_vector_type(8)));
typedef _Float16 h2 __attribute__((ext_vector_type(2)));
typedef float f4 __attribute__((ext_vector_type(4)));
typedef float f2 __attribute__((ext_vector_type(2)));
typedef short s8 __attribute__((ext_vector_type(8)));
typedef short s4 __attribute__((ext_vector_type(4)));
typedef unsigned u4 __attribute__((ext_vector_type(4)));
typedef unsigned u4u __attribute__((ext_vector_type(4), aligned(2)));
typedef unsigned u2 __attribute__((ext_vector_type(2)));
typedef unsigned short us2 __attribute__((ext_vector_type(2)));
typedef __bf16 bf2 __attribute__((ext_vector_type(2)));
typedef unsigned short u16;
#define DI __device__ __forceinline__
#define LAS __attribute__((address_space(3)))
#define GAS __attribute__((address_space(1)))

constexpr int DD = 640, FF = 1728;

constexpr size_t OFF_Z1 = 0;
constexpr size_t OFF_Z2 = OFF_Z1 + (size_t)640 * 256 * 16;
constexpr size_t OFF_WT = OFF_Z2 + (size_t)640 * 256 * 16;
constexpr size_t OFF_WM = OFF_WT + (size_t)321 * 512 * 16;
constexpr size_t OFF_HP = OFF_WM + 1024;
constexpr size_t OFF_W1 = OFF_HP + (size_t)20 * 128 * 1024;
constexpr size_t OFF_AP = OFF_W1 + (size_t)20 * 216 * 1024;
constexpr size_t OFF_W2 = OFF_AP + (size_t)54 * 128 * 1024;
constexpr size_t OFF_HH = OFF_W2 + (size_t)54 * 40 * 1024;
constexpr size_t OFF_CNT = OFF_HH + (size_t)40 * 2048 * 32;
constexpr size_t WS_USED = OFF_CNT + 56 * 256;

#define ST_DECL
#define ST(i)
#define ST_FLUSH(kid)
#define SCHED_FENCE() __builtin_amdgcn_sched_barrier(0)
#define NTS(p, v) (*(p) = (v))
#define K2GRID (328 + 160 + 112)
#define WDK 1
#define Z1L 2
#define Z2L 0
#define K4_BAR2 0
#define Z1IDX(q, k) (Z1L == 0 ? (k) * 256 + (q) : Z1L == 1 ? (q) * 640 + (k) : Z1L == 2 ? (((k) >> 2) * 256 + (q)) * 4 + ((k) & 3) : (((k) >> 3) * 256 + (q)) * 8 + ((k) & 7))
#define Z2IDX(q, k) (Z2L == 0 ? (q) * 640 + (k) : Z2L == 1 ? (k) * 256 + (q) : Z2L == 2 ? (((q) >> 2) * 640 + (k)) * 4 + ((q) & 3) : (((q) >> 3) * 640 + (k)) * 8 + ((q) & 7))

template <class Fn, int... I>
DI void static_for_impl(Fn&& f, std::integer_sequence<int, I...>) { (f(std::integral_constant<int, I>{}), ...); }
template <int N, class Fn>
DI void static_for(Fn&& f) { static_for_impl(f, std::make_integer_sequence<int, N>{}); }

DI unsigned pk_bf16(float a, float b) { f2 v = {a, b}; return __builtin_bit_cast(unsigned, __builtin_convertvector(v, bf2)); }
DI unsigned pk_f16(float a, float b) { f2 v = {a, b}; return __builtin_bit_cast(unsigned, __builtin_convertvector(v, h2)); }
DI float bf_lo(unsigned u) { return __builtin_bit_cast(float, u << 16); }
DI float bf_hi(unsigned u) { return __builtin_bit_cast(float, u & 0xffff0000u); }
DI float rbf(float a) { return bf_hi(pk_bf16(0.f, a)); }
DI float asf(unsigned u) { return __builtin_bit_cast(float, u); }
DI unsigned asu(float f) { return __builtin_bit_cast(unsigned, f); }
DI float fsin(float r) { return __builtin_amdgcn_sinf(r); }
DI float fcos(float r) { return __builtin_amdgcn_cosf(r); }
DI f4 mfma_h(u4 a, u4 b, f4 c) { return __builtin_amdgcn_mfma_f32_16x16x32_f16(__builtin_bit_cast(h8, a), __builtin_bit_cast(h8, b), c, 0, 0, 0); }
DI f4 mfma_b(u4 a, u4 b, f4 c) { return __builtin_amdgcn_mfma_f32_16x16x32_bf16(__builtin_bit_cast(s8, a), __builtin_bit_cast(s8, b), c, 0, 0, 0); }
DI u2 tr_read(const char* p) { return __builtin_bit_cast(u2, __builtin_amdgcn_ds_read_tr16_b64_v4i16((LAS s4*)p)); }
DI unsigned perm_lo(unsigned hi, unsigned lo) { return __builtin_amdgcn_perm(hi, lo, 0x05040100u); }
DI unsigned perm_hi(unsigned hi, unsigned lo) { return __builtin_amdgcn_perm(hi, lo, 0x07060302u); }

template <int CTRL> DI unsigned dppu(unsigned v) { return (unsigned)__builtin_amdgcn_update_dpp(0, (int)v, CTRL, 0xf, 0xf, true); }
template <int CTRL> DI float dpp(float v) { return asf(dppu<CTRL>(__builtin_bit_cast(unsigned, v))); }
DI float swap16_sum(float v) {
  unsigned u = __builtin_bit_cast(unsigned, v);
  auto r = __builtin_amdgcn_permlane16_swap(u, u, false, false);
  return asf((unsigned)r[0]) + asf((unsigned)r[1]);
}
DI float swap32_sum(float v) {
  unsigned u = __builtin_bit_cast(unsigned, v);
  auto r = __builtin_amdgcn_permlane32_swap(u, u, false, false);
  return asf((unsigned)r[0]) + asf((unsigned)r[1]);
}
DI float half_sum(float v) {
  v += dpp<0x121>(v); v += dpp<0x122>(v); v += dpp<0x124>(v); v += dpp<0x128>(v);
  return swap16_sum(v);
}
DI float parity_sum(float v) {
  v += dpp<0x122>(v); v += dpp<0x124>(v); v += dpp<0x128>(v);
  v = swap16_sum(v);
  return swap32_sum(v);
}
DI unsigned umax(unsigned a, unsigned b) { return a > b ? a : b; }
DI unsigned wave_umax(unsigned v) {
  v = umax(v, dppu<0x121>(v)); v = umax(v, dppu<0x122>(v)); v = umax(v, dppu<0x124>(v)); v = umax(v, dppu<0x128>(v));
  auto r = __builtin_amdgcn_permlane16_swap(v, v, false, false);
  v = umax((unsigned)r[0], (unsigned)r[1]);
  auto r2 = __builtin_amdgcn_permlane32_swap(v, v, false, false);
  return umax((unsigned)r2[0], (unsigned)r2[1]);
}
DI void raw_barrier() {
  asm volatile("s_waitcnt lgkmcnt(0)" ::: "memory");
  __builtin_amdgcn_s_barrier();
  asm volatile("" ::: "memory");
}
DI int srow_of(int q, int i, int j) { return 32 * (4 * (q >> 4) + i) + (q & 15) + 16 * j; }
DI int blk_of_pos(int pos) {
  if (pos < 128) return ((27 * (pos >> 3)) >> 1) + 7 * ((pos >> 2) & 1) + (pos & 3);
  const int r = pos - 128, pr = r / 11, o = r - 11 * pr;
  const int nt = 2 * pr + (o >= 5), oo = o >= 5 ? o - 5 : o;
  return ((27 * nt) >> 1) + (oo >= 3 ? 8 + oo : 4 + oo);
}
DI unsigned pkmax_abs(unsigned m, unsigned v) {
  us2 a = __builtin_bit_cast(us2, m), b = __builtin_bit_cast(us2, v & 0x7fff7fffu);
  return __builtin_bit_cast(unsigned, __builtin_elementwise_max(a, b));
}

struct RowC { u4 t[9]; };
template <int SGN> DI void rc_F(char* ct, int l, int cb) {
  const float gf = (float)(l >> 4), cf = (float)(16 * cb + (l & 15));
  const float c32 = cf * (SGN / 32.f), a0 = gf * 8.f * c32;
  u4 fr, fi;
#pragma unroll
  for (int jj = 0; jj < 4; ++jj) {
    float r0 = (float)(2 * jj) * c32 + a0, r1 = (float)(2 * jj + 1) * c32 + a0;
    fr[jj] = pk_f16(fcos(r0), fcos(r1));
    fi[jj] = pk_f16(fsin(r0), fsin(r1));
  }
  *(u4*)(ct) = fr; *(u4*)(ct + 1024) = fi;
}
template <int SGN, int OB> DI void rc_G(char* ct, int l) {
  const int r16 = l & 15;
  const float gf = (float)(l >> 4), po4 = (float)(r16 & 1) * 0.25f;
  const float e20 = (float)((r16 >> 1) + 8 * OB) * (-SGN / 20.f), g0 = gf * e20 + po4;
  u4 v;
#pragma unroll
  for (int blk = 0; blk < 4; ++blk) { float r = (float)(4 * blk) * e20 + g0; v[blk] = pk_f16(fcos(r), fsin(r)); }
  *(u4*)(ct + (2 + OB) * 1024) = v;
  float r = 16.f * e20 + g0;
  *(unsigned*)(ct + 5 * 1024 + OB * 4) = pk_f16(fcos(r), fsin(r));
}
template <int SGN, int BLK> DI void rc_T(char* ct, int l, int cb, float tws) {
  const float r = (float)(((l >> 4) + 4 * BLK) * (16 * cb + (l & 15))) * (SGN / 640.f);
  const float tc = fcos(r) * tws, ts = fsin(r) * tws;
  if (BLK < 4) { *(float*)(ct + 6 * 1024 + BLK * 4) = tc; *(float*)(ct + 7 * 1024 + BLK * 4) = ts; }
  else { *(float*)(ct + 5 * 1024 + 12) = tc; *(float*)(ct + 8 * 1024 - l * 12) = ts; }
}
template <int SGN> DI void rc_part(char* ct, int l, int cb, float tws, int part) {
  if (part == 0) rc_F<SGN>(ct, l, cb);
  else if (part == 1) { rc_G<SGN, 0>(ct, l); rc_T<SGN, 0>(ct, l, cb, tws); rc_T<SGN, 1>(ct, l, cb, tws); }
  else if (part == 2) { rc_G<SGN, 1>(ct, l); rc_T<SGN, 2>(ct, l, cb, tws); rc_T<SGN, 3>(ct, l, cb, tws); }
  else { rc_G<SGN, 2>(ct, l); rc_T<SGN, 4>(ct, l, cb, tws); }
}
DI void rc_load(RowC& C, const char* ct, int l) {
#pragma unroll
  for (int j = 0; j < 8; ++j) C.t[j] = *(const u4*)(ct + j * 1024);
  C.t[8][0] = *(const unsigned*)(ct + 8 * 1024 - l * 12);
}
DI void row_stage_a(const RowC& C, const char* lds, int l, int ih, bool ii, u4& T0, u4& T1) {
  const int g = l >> 4, q4 = (l & 15) >> 2, p = l & 3;
  const char* base = lds + (8 * g + q4) * 320 + p * 16 + ih * 8;
  u2 lo[5], hi[5];
#pragma unroll
  for (int blk = 0; blk < 5; ++blk) { lo[blk] = tr_read(base + blk * 64); hi[blk] = tr_read(base + blk * 64 + 4 * 320); }
  T1 = u4{0u, 0u, 0u, 0u};
#pragma unroll
  for (int blk = 0; blk < 5; ++blk) {
    u4 a = {lo[blk][0], lo[blk][1], hi[blk][0], hi[blk][1]};
    f4 z = {0.f, 0.f, 0.f, 0.f};
    f4 pr = mfma_h(a, C.t[0], z);
    f4 pi = mfma_h(a, C.t[1], z);
    float pr0, pr1, pi0, pi1;
    if (ii) { pr0 = pr[2]; pr1 = pr[3]; pi0 = pi[2]; pi1 = pi[3]; } else { pr0 = pr[0]; pr1 = pr[1]; pi0 = pi[0]; pi1 = pi[1]; }
    const float tc = asf(blk < 4 ? C.t[6][blk & 3] : C.t[5][3]), ts = asf(blk < 4 ? C.t[7][blk & 3] : C.t[8][0]);
    float tr = pr0 - pi1, ti = pi0 + pr1;
    unsigned v = pk_f16(tr * tc - ti * ts, ti * tc + tr * ts);
    if (blk < 4) T0[blk & 3] = v; else T1[0] = v;
  }
}

template <bool II> DI void k1_dft(const RowC& C, const char* lin, char* lout, int l, int ih, int cb) {
  u4 T0, T1;
  row_stage_a(C, lin, l, ih, II, T0, T1);
  const int g = l >> 4, cc = 16 * cb + (l & 15);
  char* ob_ = lout + (cc + 64 * g) * 16 + (2 * ih + (int)II) * 4;
#pragma unroll
  for (int ob = 0; ob < 3; ++ob) {
    f4 z = {0.f, 0.f, 0.f, 0.f};
    z = mfma_h(C.t[2 + ob], T0, z);
    u4 ga = {C.t[5][ob], 0u, 0u, 0u};
    z = mfma_h(ga, T1, z);
    *(unsigned*)(ob_ + ob * 4096) = pk_f16(z[0], z[1]);
    *(unsigned*)(ob_ + ob * 4096 + 512) = pk_f16(z[2], z[3]);
  }
}
__global__ __launch_bounds__(512) void k1_kernel(const u16* __restrict__ x, const u16* __restrict__ wr, const u16* __restrict__ wi,
                                                 const u16* __restrict__ wd, char* __restrict__ ws) {
  __shared__ __attribute__((aligned(16))) char smem[32 * 1296];
  const int b = blockIdx.x, tid = threadIdx.x, l = tid & 63;
  const int w = __builtin_amdgcn_readfirstlane(tid >> 6);
  ST_DECL; ST(0);
  if (b >= 384) {
    const int ks = b - 384;
    if (ks >= 54) return;
    u4 a[5];
#pragma unroll
    for (int it = 0; it < 5; ++it) {
      int idx = tid + 512 * it, k = idx / 80, seg = idx - k * 80;
      a[it] = *(const u4*)(wd + (size_t)(8 * blk_of_pos(4 * ks + (k >> 3)) + (k & 7)) * DD + 8 * seg);
    }
    SCHED_FENCE();
#pragma unroll
    for (int it = 0; it < 5; ++it) {
      int idx = tid + 512 * it, k = idx / 80, seg = idx - k * 80;
      *(u4*)(smem + k * 1296 + seg * 16) = a[it];
    }
    raw_barrier();
    u4* W2 = (u4*)(ws + OFF_W2) + (size_t)(ks * 40 + 5 * w) * 64 + l;
    const char* base = smem + (8 * (l >> 4) + ((l & 15) >> 2)) * 1296 + (l & 3) * 8 + w * 160;
#pragma unroll
    for (int t = 0; t < 5; ++t) {
      u2 lo = tr_read(base + t * 32), hi = tr_read(base + t * 32 + 4 * 1296);
      W2[t * 64] = u4{lo[0], lo[1], hi[0], hi[1]};
    }
    ST_FLUSH(0);
    return;
  }
  if (b >= 256) {
    const int task = b - 256, u = task & 7, v = (task >> 3) & 7, hf = task >> 6;
    u4 d[3];
#pragma unroll
    for (int it = 0; it < 3; ++it) {
      int idx = tid + 512 * it; if (idx >= 1280) idx = tid;
      int rho = idx / 20, pc = idx - rho * 20;
      int row = 64 * (4 * v + (rho & 3)) + 8 * u + ((rho >> 2) & 7);
      const u16* src = (rho >= 32 ? wi : wr) + (size_t)row * 321 + 160 * hf + 8 * pc;
      d[it] = *(const u4u*)src;
    }
    unsigned last = 0;
    if (tid < 64) {
      int row = 64 * (4 * v + (tid & 3)) + 8 * u + ((tid >> 2) & 7);
      last = ((tid >= 32 ? wi : wr) + (size_t)row * 321)[320];
    }
    SCHED_FENCE();
    unsigned mx = last & 0x7fffu;
#pragma unroll
    for (int it = 0; it < 3; ++it) {
      int idx = tid + 512 * it; if (idx >= 1280) idx = tid;
      int rho = idx / 20, pc = idx - rho * 20;
      *(u4*)(smem + rho * 336 + pc * 16) = d[it];
#pragma unroll
      for (int e = 0; e < 4; ++e) mx = pkmax_abs(mx, d[it][e]);
    }
    if (tid < 64) *(u16*)(smem + tid * 336 + 320) = (u16)last;
    mx = wave_umax(umax(mx & 0xffffu, mx >> 16));
    if (l == 0) *(unsigned*)(smem + 21504 + w * 4) = mx;
    raw_barrier();
    u4* Wt = (u4*)(ws + OFF_WT);
    const int obase = ((u >> 1) * 2 + (v >> 2)) * 64 + 16 * (v & 3) + 8 * (u & 1);
    const int g = l >> 4, q4 = (l & 15) >> 2, p = l & 3;
#pragma unroll
    for (int rd = 0; rd < 3; ++rd) {
      int job = w + 8 * rd;
      if (job < 20) {
        int cbk = job >> 1, r8 = 4 * (job & 1) + g;
        const char* a = smem + (r8 * 4 + q4) * 336 + (16 * cbk + 4 * p) * 2;
        u2 re = tr_read(a), im = tr_read(a + 32 * 336);
        u4 o = {perm_lo(im[0], re[0]), perm_hi(im[0], re[0]), perm_lo(im[1], re[1]), perm_hi(im[1], re[1])};
        NTS(&Wt[(160 * hf + 16 * cbk + (l & 15)) * 512 + obase + r8], o);
      }
    }
    if (hf == 1 && tid < 8) {
      u4 o;
#pragma unroll
      for (int reg = 0; reg < 4; ++reg)
        o[reg] = (unsigned)*(const u16*)(smem + (tid * 4 + reg) * 336 + 320) | ((unsigned)*(const u16*)(smem + (32 + tid * 4 + reg) * 336 + 320) << 16);
      Wt[320 * 512 + obase + tid] = o;
    }
    if (tid == 0) {
      const unsigned* m = (const unsigned*)(smem + 21504);
      ((unsigned*)(ws + OFF_WM))[task] = umax(umax(umax(m[0], m[1]), umax(m[2], m[3])), umax(umax(m[4], m[5]), umax(m[6], m[7])));
    }
    ST_FLUSH(0);
    return;
  }
  if (b == 0 && tid >= 448 && tid < 504) *(unsigned*)(ws + OFF_CNT + (tid - 448) * 256) = 0u;
  const int q = (b & 7) * 32 + (b >> 3);
  const int ih = (w >> 1) & 1, cb = w & 1;
  char* lin = smem;
  char* lout = smem + 10240;
  char* ct = smem + 22528 + cb * 8448 + l * 16;
  RowC C;
  if (w < 4) {
    const int j = l >> 5, li = l & 31;
    const u2* xr = (const u2*)(x + (size_t)srow_of(q, w, j) * DD);
    u2 xv[5];
#pragma unroll
    for (int c = 0; c < 5; ++c) xv[c] = xr[li + 32 * c];
    float v[20];
#pragma unroll
    for (int c = 0; c < 5; ++c) {
      v[4 * c] = bf_lo(xv[c][0]); v[4 * c + 1] = bf_hi(xv[c][0]);
      v[4 * c + 2] = bf_lo(xv[c][1]); v[4 * c + 3] = bf_hi(xv[c][1]);
    }
    float sa = 0.f, sb = 0.f;
#pragma unroll
    for (int t = 0; t < 10; ++t) { sa += v[2 * t]; sb += v[2 * t + 1]; }
    const float mean = half_sum(sa + sb) * (1.f / 640.f);
    ST(1);
    sa = 0.f; sb = 0.f;
#pragma unroll
    for (int t = 0; t < 10; ++t) { float d0 = v[2 * t] - mean, d1 = v[2 * t + 1] - mean; sa += d0 * d0; sb += d1 * d1; }
    const float var = half_sum(sa + sb) * (1.f / 640.f);
    const float sd = rbf(__builtin_sqrtf(rbf(rbf(var) + 1e-5f)));
    const float rs = 16.f * __builtin_amdgcn_rcpf(sd);
    const float sh = -rbf(mean) * rs;
    char* wp = lin + (4 * li + 2 * j) * 16 + w * 4;
#pragma unroll
    for (int c = 0; c < 5; ++c) {
      unsigned p01 = pk_f16(v[4 * c] * rs + sh, v[4 * c + 1] * rs + sh);
      unsigned p23 = pk_f16(v[4 * c + 2] * rs + sh, v[4 * c + 3] * rs + sh);
      auto r = __builtin_amdgcn_permlane32_swap(p01, p23, false, false);
      unsigned pa = r[0], pb = r[1];
      *(unsigned*)(wp + c * 2048) = perm_lo(pb, pa);
      *(unsigned*)(wp + c * 2048 + 16) = perm_hi(pb, pa);
    }
    ST(2);
    raw_barrier();
    ST(3);
    rc_load(C, ct, l);
    k1_dft<false>(C, lin, lout, l, ih, cb);
  } else {
    rc_part<-1>(ct, l, cb, 1.f / 256.f, 2 * ih);
    rc_part<-1>(ct, l, cb, 1.f / 256.f, 2 * ih + 1);
    raw_barrier();
    rc_load(C, ct, l);
    k1_dft<true>(C, lin, lout, l, ih, cb);
  }
  raw_barrier();
  ST(4);
  u4* Z1 = (u4*)(ws + OFF_Z1);
  Z1[Z1IDX(q, tid)] = *(const u4*)(lout + tid * 16);
  if (tid < 128) Z1[Z1IDX(q, tid + 512)] = *(const u4*)(lout + (tid + 512) * 16);
  ST_FLUSH(0);
}

DI unsigned wmax_exp(u2 m) {
  unsigned eb = wave_umax(umax(m[0], m[1])) >> 7;
  return eb > 251u ? 251u : eb;
}
__global__ __launch_bounds__(256) void k2_kernel(const u16* __restrict__ wg, const u16* __restrict__ wu, const u16* __restrict__ wd,
                                                 char* __restrict__ ws) {
  __shared__ __attribute__((aligned(16))) char smem[32 * 880];
  const int b = blockIdx.x, tid = threadIdx.x, l = tid & 63;
  const int w = __builtin_amdgcn_readfirstlane(tid >> 6);
  const int g = l >> 4, r16 = l & 15;
  ST_DECL; ST(0);
  if (b >= 488) {
    const int task = b - 488, ks = task >> 1, hf = task & 1;
    if (ks >= 54) return;
    u4 a[5];
#pragma unroll
    for (int it = 0; it < 5; ++it) {
      int idx = tid + 256 * it, k = idx / 40, seg = idx - k * 40;
      a[it] = *(const u4*)(wd + (size_t)(8 * blk_of_pos(4 * ks + (k >> 3)) + (k & 7)) * DD + 320 * hf + 8 * seg);
    }
    SCHED_FENCE();
#pragma unroll
    for (int it = 0; it < 5; ++it) {
      int idx = tid + 256 * it, k = idx / 40, seg = idx - k * 40;
      *(u4*)(smem + k * 656 + seg * 16) = a[it];
    }
    raw_barrier();
    u4* W2 = (u4*)(ws + OFF_W2) + (size_t)(ks * 40 + 20 * hf + 5 * w) * 64 + l;
    const char* base = smem + (8 * g + (r16 >> 2)) * 656 + (l & 3) * 8 + w * 160;
#pragma unroll
    for (int t = 0; t < 5; ++t) {
      u2 lo = tr_read(base + t * 32), hi = tr_read(base + t * 32 + 4 * 656);
      W2[t * 64] = u4{lo[0], lo[1], hi[0], hi[1]};
    }
    ST_FLUSH(1);
    return;
  }
  if (b >= 328) {
    const int fg = b & 7, ks = (b - 328) >> 3;
    u4 a[4], c[4];
#pragma unroll
    for (int it = 0; it < 4; ++it) {
      int idx = tid + 256 * it; idx = idx < 864 ? idx : tid;
      int k = idx / 27, seg = idx - k * 27;
      const size_t off = (size_t)(32 * ks + k) * FF + 216 * fg + 8 * seg;
      a[it] = *(const u4*)(wg + off); c[it] = *(const u4*)(wu + off);
    }
    SCHED_FENCE();
#pragma unroll
    for (int it = 0; it < 4; ++it) {
      int idx = tid + 256 * it; idx = idx < 864 ? idx : tid;
      int k = idx / 27, seg = idx - k * 27;
      u4 o0 = {perm_lo(c[it][0], a[it][0]), perm_hi(c[it][0], a[it][0]), perm_lo(c[it][1], a[it][1]), perm_hi(c[it][1], a[it][1])};
      u4 o1 = {perm_lo(c[it][2], a[it][2]), perm_hi(c[it][2], a[it][2]), perm_lo(c[it][3], a[it][3]), perm_hi(c[it][3], a[it][3])};
      *(u4*)(smem + k * 880 + seg * 32) = o0;
      *(u4*)(smem + k * 880 + seg * 32 + 16) = o1;
    }
    raw_barrier();
    u4* W1 = (u4*)(ws + OFF_W1) + (size_t)(ks * 216 + fg * 27 + w) * 64 + l;
    const char* base = smem + (8 * g + (r16 >> 2)) * 880 + (l & 3) * 8 + w * 32;
#pragma unroll
    for (int t = 0; t < 7; ++t) {
      if (t < 6 || w < 3) {
        u2 lo = tr_read(base + t * 128), hi = tr_read(base + t * 128 + 4 * 880);
        NTS(&W1[t * 256], (u4{lo[0], lo[1], hi[0], hi[1]}));
      }
    }
    ST_FLUSH(1);
    return;
  }
  const int xi = b >> 3;
  int kd = 40 * (b & 7) + xi;
  if (xi == 40) { if ((b & 7) != 7) return; kd = 320; }
  const int kdm = kd == 0 ? 0 : 640 - kd;
  const u4* Z1 = (const u4*)(ws + OFF_Z1);
  const u4 z1 = Z1[Z1IDX(tid, kd)], z2 = Z1[Z1IDX(tid, kdm)];
  u4 wv[2];
  {
    const u4* Wt = (const u4*)(ws + OFF_WT) + kd * 512 + 128 * w + l;
    wv[0] = Wt[0]; wv[1] = Wt[64];
  }
  const u2 wmv = ((const u2*)(ws + OFF_WM))[l];
  SCHED_FENCE();
  u4 M1[4], M2[4], N1[4], N2[4];
  float TC[4][4], TS[4][4];
  {
    const float g4 = (float)(4 * g), k1f = (float)(16 * w + r16);
    const float ka = k1f * (1.f / 64.f), ka0 = g4 * ka;
    const bool odd = r16 & 1;
    const unsigned s1 = (r16 & 2) ? 0x80008000u : 0u;
    const unsigned s1b = (((r16 & 3) + 1) & 2) ? 0x80008000u : 0u;
    const unsigned s2 = odd ? 0x80008000u : 0u;
    const unsigned s3 = ((3 * (r16 & 3)) & 2) ? 0x80008000u : 0u, s3b = ((3 * (r16 & 3) + 1) & 2) ? 0x80008000u : 0u;
#pragma unroll
    for (int jj = 0; jj < 4; ++jj) {
      float r = (float)jj * ka + ka0, c = fcos(r), sn = fsin(r);
      const unsigned a = pk_f16(c, sn), bq = pk_f16(-sn, c);
      const unsigned p = odd ? bq : a, q2 = odd ? a : bq;
      M1[0][jj] = a; M2[0][jj] = bq;
      M1[1][jj] = p ^ s1; M2[1][jj] = q2 ^ s1b;
      M1[2][jj] = a ^ s2; M2[2][jj] = bq ^ s2;
      M1[3][jj] = p ^ s3; M2[3][jj] = q2 ^ s3b;
    }
    const float ro = (float)r16 * (1.f / 32.f), ro0 = g4 * ro;
#pragma unroll
    for (int jj = 0; jj < 4; ++jj) {
      float r = (float)jj * ro + ro0, c = fcos(r), sn = fsin(r);
      const unsigned a = pk_f16(c, sn), bq = pk_f16(-sn, c), sj = (jj & 1) ? 0x80008000u : 0u;
      N1[0][jj] = a; N2[0][jj] = bq;
      N1[1][jj] = a ^ s2; N2[1][jj] = bq ^ s2;
      N1[2][jj] = a ^ sj; N2[2][jj] = bq ^ sj;
      N1[3][jj] = a ^ (s2 ^ sj); N2[3][jj] = bq ^ (s2 ^ sj);
    }
    {
      const float kb = k1f * (1.f / 2048.f);
      const float c1 = fcos(kb), d1 = fsin(kb), c16 = fcos(16.f * kb), d16 = fsin(16.f * kb);
      TC[0][0] = fcos(g4 * kb); TS[0][0] = fsin(g4 * kb);
#pragma unroll
      for (int rg = 1; rg < 4; ++rg) {
        TC[0][rg] = TC[0][rg - 1] * c1 - TS[0][rg - 1] * d1; TS[0][rg] = TS[0][rg - 1] * c1 + TC[0][rg - 1] * d1;
      }
#pragma unroll
      for (int rg = 0; rg < 4; ++rg) { TC[1][rg] = TC[0][rg] * c16 - TS[0][rg] * d16; TS[1][rg] = TS[0][rg] * c16 + TC[0][rg] * d16; }
    }
#pragma unroll
    for (int xx = 0; xx < 2; ++xx) {
      const float nu = (float)(16 * xx + r16) * (1.f / 2048.f), nu0 = ((float)(16 * w) + g4) * nu;
      const float c1 = fcos(nu), d1 = fsin(nu);
      TC[2 + xx][0] = fcos(nu0); TS[2 + xx][0] = fsin(nu0);
#pragma unroll
      for (int rg = 1; rg < 4; ++rg) {
        TC[2 + xx][rg] = TC[2 + xx][rg - 1] * c1 - TS[2 + xx][rg - 1] * d1;
        TS[2 + xx][rg] = TS[2 + xx][rg - 1] * c1 + TC[2 + xx][rg - 1] * d1;
      }
    }
  }
  SCHED_FENCE();
  u4* sa = (u4*)smem;
  u4* ex = (u4*)(smem + 8192);
  {
    u4 A0, A1;
#pragma unroll
    for (int jj = 0; jj < 4; ++jj) {
      h2 p = __builtin_bit_cast(h2, (unsigned)z1[jj]), qq = __builtin_bit_cast(h2, (unsigned)z2[jj]);
      h2 qa = {qq[0], -qq[1]};
      h2 pb = {p[1], -p[0]}, qb = {qq[1], qq[0]};
      A0[jj] = __builtin_bit_cast(unsigned, p + qa);
      A1[jj] = __builtin_bit_cast(unsigned, pb + qb);
    }
    sa[tid] = A0;
    sa[256 + tid] = A1;
  }
  ST(1);
  raw_barrier();
  const f4 zero = {0.f, 0.f, 0.f, 0.f};
  f4 Tr[2] = {zero, zero}, Ti[2] = {zero, zero};
#pragma unroll
  for (int t = 0; t < 4; ++t) {
    u4 A0 = sa[64 * t + l], A1 = sa[256 + 64 * t + l];
    Tr[0] = mfma_h(A0, M1[t], Tr[0]); Tr[1] = mfma_h(A1, M1[t], Tr[1]);
    Ti[0] = mfma_h(A0, M2[t], Ti[0]); Ti[1] = mfma_h(A1, M2[t], Ti[1]);
  }
  ST(2);
  const unsigned eb = wmax_exp(wmv);
  const float fs = asf((252u - eb) << 23) * (1.f / 256.f);
  u4 X[2];
#pragma unroll
  for (int rb = 0; rb < 2; ++rb)
#pragma unroll
    for (int rg = 0; rg < 4; ++rg) {
      float a = Tr[rb][rg], c = Ti[rb][rg], tc = TC[rb][rg], ts = TS[rb][rg];
      X[rb][rg] = pk_f16(a * tc + c * ts, c * tc - a * ts);
    }
  u4 Y[2];
#pragma unroll
  for (int ob = 0; ob < 2; ++ob) {
    f4 xr = mfma_h(N1[2 * ob], X[0], zero); xr = mfma_h(N1[2 * ob + 1], X[1], xr);
    f4 xim = mfma_h(N2[2 * ob], X[0], zero); xim = mfma_h(N2[2 * ob + 1], X[1], xim);
#pragma unroll
    for (int rg = 0; rg < 4; ++rg) {
      float wr_ = bf_lo(wv[ob][rg]) * fs, wi_ = bf_hi(wv[ob][rg]) * fs;
      float yr = xr[rg] * wr_ - xim[rg] * wi_;
      float yi = xr[rg] * wi_ + xim[rg] * wr_;
      Y[ob][rg] = pk_f16(yi, yr);
    }
  }
#pragma unroll
  for (int cb = 0; cb < 2; ++cb) {
    f4 ui = mfma_h(Y[0], N1[2 * cb], zero); ui = mfma_h(Y[1], N1[2 * cb + 1], ui);
    f4 ur = mfma_h(Y[0], N2[2 * cb], zero); ur = mfma_h(Y[1], N2[2 * cb + 1], ur);
    u4 U;
#pragma unroll
    for (int rg = 0; rg < 4; ++rg) {
      float tc = TC[2 + cb][rg], ts = TS[2 + cb][rg];
      U[rg] = pk_f16(ui[rg] * tc + ur[rg] * ts, ur[rg] * tc - ui[rg] * ts);
    }
    ex[(w * 2 + cb) * 64 + l] = U;
  }
  ST(3);
  raw_barrier();
  ST(4);
  f4 Vi[2] = {zero, zero}, Vr[2] = {zero, zero};
#pragma unroll
  for (int t = 0; t < 4; ++t)
#pragma unroll
    for (int cb = 0; cb < 2; ++cb) {
      u4 U = ex[(t * 2 + cb) * 64 + l];
      Vi[cb] = mfma_h(M1[t], U, Vi[cb]);
      Vr[cb] = mfma_h(M2[t], U, Vr[cb]);
    }
  u4* Z2 = (u4*)(ws + OFF_Z2);
  const float sg = (kd == 0 || kd == 320) ? 0.f : 1.f;
  u4 o, om;
#pragma unroll
  for (int rg = 0; rg < 4; ++rg) {
    float ia = Vi[0][rg] * sg, ib = Vi[1][rg] * sg;
    o[rg] = pk_f16(Vr[0][rg] - ib, ia + Vr[1][rg]);
    om[rg] = pk_f16(Vr[0][rg] + ib, Vr[1][rg] - ia);
  }
  Z2[Z2IDX(tid, kd)] = o;
  if (kdm != kd) Z2[Z2IDX(tid, kdm)] = om;
  ST_FLUSH(1);
}

#define GLDS(src, dst) __builtin_amdgcn_global_load_lds((const GAS void*)(src), (LAS void*)(dst), 16, 0, 0)
#define LDAUX 16
#define ACQ 0
#define GLDS_SC1(src, dst) __builtin_amdgcn_global_load_lds((const GAS void*)(src), (LAS void*)(dst), 16, 0, LDAUX)
template <int N> DI void wait_vm() { asm volatile("s_waitcnt vmcnt(%0)" ::"n"(N) : "memory"); }
#define SGB(mask, n) __builtin_amdgcn_sched_group_barrier(mask, n, 0)

DI float geglu(float g, float u) {
  float t = g * g;
  float z = g * (t * (-2.f * 1.4426950408889634f * 0.7978845608028654f * 0.044715f) + (-2.f * 1.4426950408889634f * 0.7978845608028654f));
  float e = __builtin_amdgcn_exp2f(z);
  return (g * u) * __builtin_amdgcn_rcpf(1.f + e);
}
DI void poll_ge(const char* ws, int cnt, unsigned want) {
  const GAS unsigned* p = (const GAS unsigned*)(ws + OFF_CNT + cnt * 256);
  for (int spins = 0; spins < (1 << 22); ++spins) {
    unsigned v = __hip_atomic_load(p, __ATOMIC_RELAXED, __HIP_MEMORY_SCOPE_AGENT);
    if (__builtin_amdgcn_readfirstlane(v) >= want) break;
    __builtin_amdgcn_s_sleep(1);
  }
  asm volatile("" ::: "memory");
}
DI void poll_ge16(const char* ws, int cnt) { poll_ge(ws, cnt, 16u); }
DI void signal(char* ws, int cnt) {
  __hip_atomic_fetch_add((GAS unsigned*)(ws + OFF_CNT + cnt * 256), 1u, __ATOMIC_RELAXED, __HIP_MEMORY_SCOPE_AGENT);
}

constexpr int G1_STG = 22 * 1024, G1_NB = 7, G1_NS = 20, G1_TAIL = 4, G1_MAIN = G1_NS - G1_TAIL;
constexpr int LDS_TOTAL = 160 * 1024, PA_OFF = 6 * G1_STG;
constexpr int G2_NS = 14, G2_AD = 6;
#define EARLY_AT 5

DI void arrive_n(char* smem, char* ws, int word, int cnt, int l, unsigned last) {
  if (l == 0) {
    unsigned old = __hip_atomic_fetch_add((LAS unsigned*)(smem + LDS_TOTAL - 64 + word * 4), 1u, __ATOMIC_RELAXED, __HIP_MEMORY_SCOPE_WORKGROUP);
    if (old == last) signal(ws, cnt);
  }
}
template <int NW> DI void k3_loader(char* smem, char* ws, int mt, int nb0, int mt2, int db0, int l, int j) {
  const unsigned loff = l * 16;
  constexpr int NL = NW + 2;
  const char* sA = ws + OFF_HP + (size_t)(8 * mt + j) * 1024 + loff;
  const char* sB = ws + OFF_W1 + (size_t)(nb0 + j) * 1024 + loff;
  auto issueA = [&](int ks) {
    char* d = smem + (ks % G1_NB) * G1_STG + j * 1024;
    GLDS_SC1(sA + (size_t)ks * (128 * 1024), d);
    GLDS_SC1(sA + (size_t)ks * (128 * 1024) + 4096, d + 4096);
  };
  auto issueW = [&](int ks) {
    char* d = smem + (ks % G1_NB) * G1_STG + 8192 + j * 1024;
#pragma unroll
    for (int t = 0; t < NW; ++t) GLDS(sB + (size_t)ks * (216 * 1024) + t * 4096, d + t * 4096);
  };
  int foff[5];
#pragma unroll
  for (int t = 0; t < 5; ++t) { int f = j + 4 * t, kq = f / 5; foff[t] = (kq * 40 + db0 + f - 5 * kq) * 1024; }
  const char* s2 = ws + OFF_W2 + loff;
  auto issue2 = [&](int i) {
    char* d = smem + ((i + 6) % G1_NB) * G1_STG + j * 1024;
#pragma unroll
    for (int t = 0; t < 5; ++t) GLDS(s2 + (size_t)i * (160 * 1024) + foff[t], d + t * 4096);
  };
  raw_barrier();
#pragma unroll
  for (int s = 0; s < 6; ++s) issueW(s);
  raw_barrier(); raw_barrier();
  issueW(6);
  poll_ge16(ws, mt);
  u4 pk[33];
  auto fetch = [&](int e) {
    const int i = 7 + e / 5, t = e % 5;
    pk[e] = *(const u4*)(s2 + (size_t)i * (160 * 1024) + (e == 32 && j >= 2 ? foff[1] : foff[t]));
  };
  auto park = [&](int i) {
    char* d = smem + ((i + 6) % G1_NB) * G1_STG + j * 1024 + loff;
#pragma unroll
    for (int t = 0; t < (i < 13 ? 5 : 3); ++t) *(u4*)(d + t * 4096) = pk[(i - 7) * 5 + t];
  };
#pragma unroll
  for (int s = 0; s < 7; ++s) issueA(s);
  wait_vm<12>();
  raw_barrier();
  static_for<G1_MAIN - 1>([&](auto S_) {
    constexpr int s = decltype(S_)::value;
    constexpr int n = s <= 5 ? 2 * (5 - s) + NL * s : (s <= 13 ? NL * ((s + 6 < 19 ? s + 6 : 19) - (s + 1)) : NL * (19 - (s + 1)) + 5 * (s - 13));
    constexpr int lo = s - 6 > 0 ? s - 6 : 0, hi = s - 1 < 10 ? s - 1 : 10;
    wait_vm<n + (hi >= lo ? 3 * (hi - lo + 1) : 0)>();
    raw_barrier();
    if constexpr (s + G1_NB < G1_NS) { issueA(s + G1_NB); issueW(s + G1_NB); }
    else issue2(s - 13);
    if constexpr (s <= 10) { SCHED_FENCE(); fetch(3 * s); fetch(3 * s + 1); fetch(3 * s + 2); SCHED_FENCE(); }
  });
  wait_vm<10>();
  raw_barrier();
  issue2(2);
  raw_barrier();
  issue2(3); issue2(4); issue2(5); issue2(6);
  wait_vm<0>();
  raw_barrier();
  if (j == 0) poll_ge16(ws, 32 + (mt2 >> 1));
  raw_barrier();
  park(7); park(8); park(9);
  raw_barrier();
  park(10); park(11);
  raw_barrier();
  park(12); park(13);
  raw_barrier();
}
DI void arrive(char* smem, char* ws, int word, int cnt, int l) { arrive_n(smem, ws, word, cnt, l, 7u); }

__global__ __launch_bounds__(768, 3) void k3_kernel(const u16* __restrict__ x, u16* __restrict__ out, char* __restrict__ ws) {
  extern __shared__ __attribute__((aligned(16))) char smem[];
  const int b = blockIdx.x, tid = threadIdx.x, l = tid & 63;
  const int w = __builtin_amdgcn_readfirstlane(tid >> 6);
  const int xcd = b & 7, idx = b >> 3;
  const int mt = 8 * (xcd & 1) + (idx & 7), nt = 4 * (xcd >> 1) + (idx >> 3);
  const int mt2 = 16 * (xcd & 1) + 8 * ((xcd >> 1) & 1) + (idx & 7), nt2 = 4 * (xcd >> 2) + (idx >> 3);
  const int nb0 = (27 * nt) >> 1, mb0 = 8 * mt, db0 = 5 * nt2;
  const unsigned loff = l * 16;
  ST_DECL; ST(0);
  if (w >= 8) {
    if (w < 10) k3_loader<4>(smem, ws, mt, nb0, mt2, db0, l, w - 8); else k3_loader<3>(smem, ws, mt, nb0, mt2, db0, l, w - 8);
    return;
  }
  const int g = l >> 4, r16 = l & 15;
  const auto rsrc = __builtin_amdgcn_make_buffer_rsrc(ws, 0, (int)WS_USED, 0x00020000);
  {
    char* pa = smem + PA_OFF;
    const int q = 16 * mt + nt;
    const int ih = (w >> 1) & 1, cb = w & 1, ii = w >> 2, j = l & 1;
    const u4* Z2 = (const u4*)(ws + OFF_Z2);
    const u4 zv0 = Z2[Z2IDX(q, tid)];
    const u4 zv1 = Z2[Z2IDX(q, 512 + (tid & 127))];
    const int e0 = r16 >> 1;
    const bool v2 = e0 < 4;
    const size_t roff = (size_t)srow_of(q, 2 * ih + ii, j) * DD + 16 * cb + 4 * g + 32 * e0;
    u2 xv[3];
    xv[0] = *(const u2*)(x + roff); xv[1] = *(const u2*)(x + roff + 256); xv[2] = *(const u2*)(x + roff + (v2 ? 512 : 0));
    const u2 wmv = ((const u2*)(ws + OFF_WM))[l];
    SCHED_FENCE();
    char* ct = pa + 10368 + cb * 8448 + l * 16;
    rc_part<1>(ct, l, cb, 1.f / 32.f, w >> 1);
    SCHED_FENCE();
    *(u4*)(pa + tid * 16) = zv0;
    if (tid < 128) *(u4*)(pa + (512 + tid) * 16) = zv1;
    if (tid < 3) *(unsigned*)(smem + LDS_TOTAL - 64 + tid * 4) = 0u;
    raw_barrier();
    RowC C;
    rc_load(C, ct, l);
    u4 T0, T1;
    if (ii) row_stage_a(C, pa, l, ih, true, T0, T1); else row_stage_a(C, pa, l, ih, false, T0, T1);
    const unsigned eb = wmax_exp(wmv);
    const float ysc = asf((eb + 2u) << 23) * 0.05f;
    float h[12];
    float s = 0.f;
    const int mp = 8 * q + 2 * (2 * ih + ii) + j;
    const int hoff = (int)OFF_HH + (((2 * e0 + cb) * 2048 + mp) * 4 + g) * 8;
#pragma unroll
    for (int ob = 0; ob < 3; ++ob) {
      f4 z = {0.f, 0.f, 0.f, 0.f};
      z = mfma_h(T0, C.t[2 + ob], z);
      u4 gb = {C.t[5][ob], 0u, 0u, 0u};
      z = mfma_h(T1, gb, z);
      unsigned y01 = pk_bf16(z[0] * ysc, z[1] * ysc), y23 = pk_bf16(z[2] * ysc, z[3] * ysc);
      unsigned h01 = pk_bf16(bf_lo(xv[ob][0]) + bf_lo(y01), bf_hi(xv[ob][0]) + bf_hi(y01));
      unsigned h23 = pk_bf16(bf_lo(xv[ob][1]) + bf_lo(y23), bf_hi(xv[ob][1]) + bf_hi(y23));
      if (ob == 2) { h01 = v2 ? h01 : 0u; h23 = v2 ? h23 : 0u; }
      if (ob < 2 || v2) __builtin_amdgcn_raw_buffer_store_b64(u2{h01, h23}, rsrc, hoff + ob * (16 * 2048 * 32), 0, 16);
      h[4 * ob] = bf_lo(h01); h[4 * ob + 1] = bf_hi(h01); h[4 * ob + 2] = bf_lo(h23); h[4 * ob + 3] = bf_hi(h23);
      s += (h[4 * ob] + h[4 * ob + 1]) + (h[4 * ob + 2] + h[4 * ob + 3]);
    }
    const float mloc = parity_sum(s) * (1.f / 320.f);
    float s2 = 0.f, s3 = 0.f;
#pragma unroll
    for (int t = 0; t < 8; ++t) { float d = h[t] - mloc; s2 += d * d; }
#pragma unroll
    for (int t = 8; t < 12; ++t) { float d = h[t] - mloc; s3 += d * d; }
    const float m2loc = parity_sum(v2 ? s2 + s3 : s2);
    f2* ex = (f2*)(pa + 10240);
    if (l < 2) ex[w * 2 + l] = f2{mloc, m2loc};
    raw_barrier();
    const f2 o = ex[(w ^ 1) * 2 + j];
    const float mean = 0.5f * (mloc + o[0]);
    const float dl = mloc - o[0];
    const float var = (m2loc + o[1] + dl * dl * 160.f) * (1.f / 640.f);
    const float mb = rbf(mean);
    const float sd = rbf(__builtin_sqrtf(rbf(rbf(var) + 1e-5f)));
    const float rs = __builtin_amdgcn_rcpf(sd);
    const int slot = (mp >> 4) * 64 + (mp & 15) + 16 * (2 * cb + (g >> 1));
    const int poff = (int)OFF_HP + ((e0 * 128 * 64 + slot) * 2 + (g & 1)) * 8;
#pragma unroll
    for (int ob = 0; ob < 3; ++ob) {
      unsigned d01 = pk_bf16(h[4 * ob] - mb, h[4 * ob + 1] - mb);
      unsigned d23 = pk_bf16(h[4 * ob + 2] - mb, h[4 * ob + 3] - mb);
      u2 st = {pk_bf16(bf_lo(d01) * rs, bf_hi(d01) * rs), pk_bf16(bf_lo(d23) * rs, bf_hi(d23) * rs)};
      if (ob < 2 || v2) __builtin_amdgcn_raw_buffer_store_b64(st, rsrc, poff + ob * (8 * 128 * 1024), 0, 16);
    }
    ST(1);
    wait_vm<0>();
    raw_barrier();
    if (tid == 0) signal(ws, mt);
    ST(2);
  }
  {
    int lb = threadIdx.x & 63;
    asm volatile("" : "+v"(lb));
    const unsigned loff = lb * 16;
    const int wm = w >> 1, wn = w & 1;
    const char* rdA = smem + (2 * wm) * 1024 + loff;
    const char* rdB = smem + (8 + 7 * wn) * 1024 + loff;
    f4 acc[7][2];
#pragma unroll
    for (int i = 0; i < 7; ++i) { acc[i][0] = f4{0.f, 0.f, 0.f, 0.f}; acc[i][1] = f4{0.f, 0.f, 0.f, 0.f}; }
    {
      u4 af[2][2], wf[2][7];
      raw_barrier();
      ST(3);
#pragma unroll
      for (int i = 0; i < 2; ++i) af[0][i] = *(const u4*)(rdA + i * 1024);
#pragma unroll
      for (int i = 0; i < 7; ++i) wf[0][i] = *(const u4*)(rdB + i * 1024);
      static_for<G1_MAIN>([&](auto S_) {
        constexpr int s = decltype(S_)::value;
        constexpr int cur = s & 1, nxt = cur ^ 1;
        constexpr bool more = s + 1 < G1_MAIN;
        if constexpr (more) {
          raw_barrier();
          constexpr int bo = ((s + 1) % G1_NB) * G1_STG;
#pragma unroll
          for (int i = 0; i < 2; ++i) af[nxt][i] = *(const u4*)(rdA + bo + i * 1024);
#pragma unroll
          for (int i = 0; i < 7; ++i) wf[nxt][i] = *(const u4*)(rdB + bo + i * 1024);
        }
#pragma unroll
        for (int i = 0; i < 7; ++i)
#pragma unroll
          for (int m = 0; m < 2; ++m) acc[i][m] = mfma_b(wf[cur][i], af[cur][m], acc[i][m]);
        if constexpr (more) {
#pragma unroll
          for (int i = 0; i < 9; ++i) { SGB(0x8, 1); SGB(0x100, 1); }
          SGB(0x8, 14);
        }
        SCHED_FENCE();
      });
    }
    raw_barrier();
    int lz = threadIdx.x & 63;
    asm volatile("" : "+v"(lz));
    const int g = lz >> 4, r16 = lz & 15;
    u4 af[G1_TAIL][2], wf[2][G1_TAIL];
#pragma unroll
    for (int t = 0; t < G1_TAIL; ++t) {
      const int bo = ((G1_MAIN + t) % G1_NB) * G1_STG;
      wf[0][t] = *(const u4*)(rdB + bo);
      af[t][0] = *(const u4*)(rdA + bo); af[t][1] = *(const u4*)(rdA + bo + 1024);
    }
    const int aoff = (int)OFF_AP + (mb0 + 2 * wm) * 1024 + r16 * 16 + 4 * g;
    auto epi = [&](int i) {
      const int pos = i < 4 ? 8 * nt + 4 * wn + i : 128 + 11 * (nt >> 1) + 5 * (nt & 1) + 3 * wn + (i - 4);
      const int so = (pos >> 2) * (128 * 1024) + (pos & 3) * 256;
      if (i == 6 && wn == 1 && !(nt & 1)) return;
#pragma unroll
      for (int m = 0; m < 2; ++m)
        __builtin_amdgcn_raw_buffer_store_b32(pk_bf16(geglu(acc[i][m][0], acc[i][m][1]), geglu(acc[i][m][2], acc[i][m][3])), rsrc,
                                              aoff + m * 1024, so, 16);
    };
    static_for<7>([&](auto I_) {
      constexpr int i = decltype(I_)::value;
      if constexpr (i + 1 < 7) {
#pragma unroll
        for (int t = 0; t < G1_TAIL; ++t) wf[(i + 1) & 1][t] = *(const u4*)(rdB + ((G1_MAIN + t) % G1_NB) * G1_STG + (i + 1) * 1024);
      }
#pragma unroll
      for (int t = 0; t < G1_TAIL; ++t)
#pragma unroll
        for (int m = 0; m < 2; ++m) acc[i][m] = mfma_b(wf[i & 1][t], af[t][m], acc[i][m]);
      if constexpr (i > 0) {
        epi(i - 1);
#pragma unroll
        for (int t = 0; t < 8; ++t) { SGB(0x8, 1); if (t < 4 && i + 1 < 7) SGB(0x100, 1); SGB(0x2, 4); SGB(0x400, 1); }
      }
      SCHED_FENCE();
      if constexpr (i == EARLY_AT) { wait_vm<2 * (EARLY_AT - 4)>(); arrive(smem, ws, 0, 16 + mt, lz); }
    });
    epi(6);
    ST(4);
    raw_barrier();
    wait_vm<0>();
    arrive(smem, ws, 1, 32 + mt, lz);
    if (w == 0) poll_ge16(ws, 16 + (mt2 >> 1));
  }
  {
    int l = threadIdx.x & 63;
    asm volatile("" : "+v"(l));
    const int g = l >> 4, r16 = l & 15;
    const unsigned loff = l * 16;
    const int kq = w & 3, mh = w >> 2;
    const int mb2 = 4 * mt2 + 2 * mh;
    f4 acc[2][5];
#pragma unroll
    for (int m = 0; m < 2; ++m)
#pragma unroll
      for (int n = 0; n < 5; ++n) acc[m][n] = f4{0.f, 0.f, 0.f, 0.f};
    const char* rdW = smem + kq * 5120 + loff;
    const int abase = (int)OFF_AP + (kq * 128 + mb2) * 1024;
    u4 fa[G2_AD][2], wf[2][5];
    auto loadA = [&](int i, u4 (&a)[2]) {
      const int so = abase + (i < 13 ? i : (kq < 2 ? 13 : 12)) * (512 * 1024);
      a[0] = __builtin_amdgcn_raw_buffer_load_b128(rsrc, loff, so, LDAUX);
      a[1] = __builtin_amdgcn_raw_buffer_load_b128(rsrc, loff + 1024, so, LDAUX);
    };
    raw_barrier();
    ST(5);
    static_for<G2_AD>([&](auto I_) { constexpr int i = decltype(I_)::value; loadA(i, fa[i]); SCHED_FENCE(); });
    u2 hv[3];
#pragma unroll
    for (int n = 0; n < 5; ++n) wf[0][n] = *(const u4*)(rdW + 6 * G1_STG + n * 1024);
    SCHED_FENCE();
    static_for<G2_NS - 1>([&](auto I_) {
      constexpr int i = decltype(I_)::value;
      constexpr int cur = i & 1, nxt = cur ^ 1;
      if constexpr (i == 2 || i == 4 || i == 6 || i == 9) raw_barrier();
#pragma unroll
      for (int n = 0; n < 5; ++n) wf[nxt][n] = *(const u4*)(rdW + ((i + 7) % G1_NB) * G1_STG + n * 1024);
#pragma unroll
      for (int m = 0; m < 2; ++m)
#pragma unroll
        for (int n = 0; n < 5; ++n) acc[m][n] = mfma_b(wf[cur][n], fa[i % G2_AD][m], acc[m][n]);
      if constexpr (i + G2_AD < G2_NS) loadA(i + G2_AD, fa[i % G2_AD]);
      if constexpr (i == 9) {
#pragma unroll
        for (int u = 0; u < 3; ++u) {
          int t = kq + 4 * u; t = t < 10 ? t : kq;
          const int m = t >= 5, n = t - 5 * m;
          hv[u] = __builtin_amdgcn_raw_buffer_load_b64(rsrc, r16 * 32 + g * 8, (int)OFF_HH + ((db0 + n) * 2048 + 16 * (mb2 + m)) * 32, 16);
        }
      }
      SGB(0x100, 5); SGB(0x8, 10); SGB(0x20, 3);
      SCHED_FENCE();
    });
    if (kq < 2) {
#pragma unroll
      for (int m = 0; m < 2; ++m)
#pragma unroll
        for (int n = 0; n < 5; ++n) acc[m][n] = mfma_b(wf[1][n], fa[13 % G2_AD][m], acc[m][n]);
    }
    ST(6);
    raw_barrier();
    f4* red = (f4*)smem + mh * (30 * 64) + l;
    static_for<10>([&](auto T_) {
      constexpr int t = decltype(T_)::value;
      if (kq != (t & 3)) red[(t * 3 + ((kq - t - 1) & 3)) * 64] = acc[t / 5][t % 5];
    });
    raw_barrier();
    auto fin = [&](f4 own, int t, int u) {
      const f4* sp = red + t * 192;
      f4 o = (own + sp[0]) + (sp[64] + sp[128]);
      unsigned o01 = pk_bf16(o[0], o[1]), o23 = pk_bf16(o[2], o[3]);
      const int mp = 16 * (mb2 + t / 5) + r16;
      u16* op = out + (size_t)srow_of(mp >> 3, (mp >> 1) & 3, mp & 1) * DD + 16 * (db0 + t % 5) + 4 * g;
      *(u2*)op = u2{pk_bf16(bf_lo(hv[u][0]) + bf_lo(o01), bf_hi(hv[u][0]) + bf_hi(o01)),
                       pk_bf16(bf_lo(hv[u][1]) + bf_lo(o23), bf_hi(hv[u][1]) + bf_hi(o23))};
    };
    static_for<4>([&](auto K_) {
      constexpr int k = decltype(K_)::value;
      if (kq == k) {
        fin(acc[k / 5][k % 5], k, 0);
        fin(acc[(k + 4) / 5][(k + 4) % 5], k + 4, 1);
        if constexpr (k + 8 < 10) fin(acc[1][(k + 8) % 5], k + 8, 2);
      }
    });
  }
  ST_FLUSH(2);
}

#if defined(__HIP_DEVICE_COMPILE__)
#pragma clang attribute pop
#endif

extern "C" void kernel_launch(void* const* d_in, const int* in_sizes, int n_in, void* d_out, int out_size, void* d_ws,
                              size_t ws_size, hipStream_t stream) {
  const u16* x = (const u16*)d_in[0];
  const u16* wr = (const u16*)d_in[1];
  const u16* wi = (const u16*)d_in[2];
  const u16* wg = (const u16*)d_in[4];
  const u16* wu = (const u16*)d_in[5];
  const u16* wd = (const u16*)d_in[6];
  u16* out = (u16*)d_out;
  char* ws = (char*)d_ws;
  (void)hipFuncSetAttribute((const void*)k3_kernel, hipFuncAttributeMaxDynamicSharedMemorySize, LDS_TOTAL);
  k1_kernel<<<256 + 128 + (WDK == 1 ? 56 : 0), 512, 0, stream>>>(x, wr, wi, wd, ws);
  k2_kernel<<<328 + 160 + (WDK == 2 ? 112 : 0), 256, 0, stream>>>(wg, wu, wd, ws);
  k3_kernel<<<256, 768, LDS_TOTAL, stream>>>(x, out, ws);
}
```

```cpp
#include <hip/hip_runtime.h>
#include <hip/hip_bf16.h>
#include <stdint.h>
#include <utility>

#if defined(__HIP_DEVICE_COMPILE__)
#pragma clang attribute push(__attribute__((target("no-packed-fp32-ops"))), apply_to = function)
#endif

typedef _Float16 h8 __attribute__((ext_vector_type(8)));
typedef _Float16 h2 __attribute__((ext_vector_type(2)));
typedef float f4 __attribute__((ext_vector_type(4)));
typedef float f2 __attribute__((ext_vector_type(2)));
typedef short s8 __attribute__((ext_vector_type(8)));
typedef short s4 __attribute__((ext_vector_type(4)));
typedef unsigned u4 __attribute__((ext_vector_type(4)));
typedef unsigned u4u __attribute__((ext_vector_type(4), aligned(2)));
typedef unsigned u2 __attribute__((ext_vector_type(2)));
typedef unsigned short us2 __attribute__((ext_vector_type(2)));
typedef __bf16 bf2 __attribute__((ext_vector_type(2)));
typedef unsigned short u16;
#define DI __device__ __forceinline__
#define LAS __attribute__((address_space(3)))
#define GAS __attribute__((address_space(1)))

constexpr int DD = 640, FF = 1728;

constexpr size_t OFF_Z1 = 0;
constexpr size_t OFF_Z2 = OFF_Z1 + (size_t)640 * 256 * 16;
constexpr size_t OFF_WT = OFF_Z2 + (size_t)640 * 256 * 16;
constexpr size_t OFF_WM = OFF_WT + (size_t)321 * 512 * 16;
constexpr size_t OFF_HP = OFF_WM + 1024;
constexpr size_t OFF_W1 = OFF_HP + (size_t)20 * 128 * 1024;
constexpr size_t OFF_AP = OFF_W1 + (size_t)20 * 216 * 1024;
constexpr size_t OFF_W2 = OFF_AP + (size_t)54 * 128 * 1024;
constexpr size_t OFF_HH = OFF_W2 + (size_t)54 * 40 * 1024;
constexpr size_t OFF_CNT = OFF_HH + (size_t)40 * 2048 * 32;
constexpr size_t WS_USED = OFF_CNT + 56 * 256;

#define ST_DECL
#define ST(i)
#define ST_FLUSH(kid)
#define SCHED_FENCE() __builtin_amdgcn_sched_barrier(0)
#define NTS(p, v) (*(p) = (v))
#define K2GRID (328 + 160 + 112)
#define WDK 1
#define Z1L 2
#define Z2L 0
#define K4_BAR2 0
#define Z1IDX(q, k) (Z1L == 0 ? (k) * 256 + (q) : Z1L == 1 ? (q) * 640 + (k) : Z1L == 2 ? (((k) >> 2) * 256 + (q)) * 4 + ((k) & 3) : (((k) >> 3) * 256 + (q)) * 8 + ((k) & 7))
#define Z2IDX(q, k) (Z2L == 0 ? (q) * 640 + (k) : Z2L == 1 ? (k) * 256 + (q) : Z2L == 2 ? (((q) >> 2) * 640 + (k)) * 4 + ((q) & 3) : (((q) >> 3) * 640 + (k)) * 8 + ((q) & 7))

template <class Fn, int... I>
DI void static_for_impl(Fn&& f, std::integer_sequence<int, I...>) { (f(std::integral_constant<int, I>{}), ...); }
template <int N, class Fn>
DI void static_for(Fn&& f) { static_for_impl(f, std::make_integer_sequence<int, N>{}); }

DI unsigned pk_bf16(float a, float b) { f2 v = {a, b}; return __builtin_bit_cast(unsigned, __builtin_convertvector(v, bf2)); }
DI unsigned pk_f16(float a, float b) { f2 v = {a, b}; return __builtin_bit_cast(unsigned, __builtin_convertvector(v, h2)); }
DI float bf_lo(unsigned u) { return __builtin_bit_cast(float, u << 16); }
DI float bf_hi(unsigned u) { return __builtin_bit_cast(float, u & 0xffff0000u); }
DI float rbf(float a) { return bf_hi(pk_bf16(0.f, a)); }
DI float asf(unsigned u) { return __builtin_bit_cast(float, u); }
DI unsigned asu(float f) { return __builtin_bit_cast(unsigned, f); }
DI float fsin(float r) { return __builtin_amdgcn_sinf(r); }
DI float fcos(float r) { return __builtin_amdgcn_cosf(r); }
DI f4 mfma_h(u4 a, u4 b, f4 c) { return __builtin_amdgcn_mfma_f32_16x16x32_f16(__builtin_bit_cast(h8, a), __builtin_bit_cast(h8, b), c, 0, 0, 0); }
DI f4 mfma_b(u4 a, u4 b, f4 c) { return __builtin_amdgcn_mfma_f32_16x16x32_bf16(__builtin_bit_cast(s8, a), __builtin_bit_cast(s8, b), c, 0, 0, 0); }
DI u2 tr_read(const char* p) { return __builtin_bit_cast(u2, __builtin_amdgcn_ds_read_tr16_b64_v4i16((LAS s4*)p)); }
DI unsigned perm_lo(unsigned hi, unsigned lo) { return __builtin_amdgcn_perm(hi, lo, 0x05040100u); }
DI unsigned perm_hi(unsigned hi, unsigned lo) { return __builtin_amdgcn_perm(hi, lo, 0x07060302u); }

template <int CTRL> DI unsigned dppu(unsigned v) { return (unsigned)__builtin_amdgcn_update_dpp(0, (int)v, CTRL, 0xf, 0xf, true); }
template <int CTRL> DI float dpp(float v) { return asf(dppu<CTRL>(__builtin_bit_cast(unsigned, v))); }
DI float swap16_sum(float v) {
  unsigned u = __builtin_bit_cast(unsigned, v);
  auto r = __builtin_amdgcn_permlane16_swap(u, u, false, false);
  return asf((unsigned)r[0]) + asf((unsigned)r[1]);
}
DI float swap32_sum(float v) {
  unsigned u = __builtin_bit_cast(unsigned, v);
  auto r = __builtin_amdgcn_permlane32_swap(u, u, false, false);
  return asf((unsigned)r[0]) + asf((unsigned)r[1]);
}
DI float half_sum(float v) {
  v += dpp<0x121>(v); v += dpp<0x122>(v); v += dpp<0x124>(v); v += dpp<0x128>(v);
  return swap16_sum(v);
}
DI float parity_sum(float v) {
  v += dpp<0x122>(v); v += dpp<0x124>(v); v += dpp<0x128>(v);
  v = swap16_sum(v);
  return swap32_sum(v);
}
DI unsigned umax(unsigned a, unsigned b) { return a > b ? a : b; }
DI unsigned wave_umax(unsigned v) {
  v = umax(v, dppu<0x121>(v)); v = umax(v, dppu<0x122>(v)); v = umax(v, dppu<0x124>(v)); v = umax(v, dppu<0x128>(v));
  auto r = __builtin_amdgcn_permlane16_swap(v, v, false, false);
  v = umax((unsigned)r[0], (unsigned)r[1]);
  auto r2 = __builtin_amdgcn_permlane32_swap(v, v, false, false);
  return umax((unsigned)r2[0], (unsigned)r2[1]);
}
DI void raw_barrier() {
  asm volatile("s_waitcnt lgkmcnt(0)" ::: "memory");
  __builtin_amdgcn_s_barrier();
  asm volatile("" ::: "memory");
}
DI int srow_of(int q, int i, int j) { return 32 * (4 * (q >> 4) + i) + (q & 15) + 16 * j; }
DI int blk_of_pos(int pos) {
  if (pos < 128) return ((27 * (pos >> 3)) >> 1) + 7 * ((pos >> 2) & 1) + (pos & 3);
  const int r = pos - 128, pr = r / 11, o = r - 11 * pr;
  const int nt = 2 * pr + (o >= 5), oo = o >= 5 ? o - 5 : o;
  return ((27 * nt) >> 1) + (oo >= 3 ? 8 + oo : 4 + oo);
}
DI unsigned pkmax_abs(unsigned m, unsigned v) {
  us2 a = __builtin_bit_cast(us2, m), b = __builtin_bit_cast(us2, v & 0x7fff7fffu);
  return __builtin_bit_cast(unsigned, __builtin_elementwise_max(a, b));
}

struct RowC { u4 t[9]; };
template <int SGN> DI void rc_F(char* ct, int l, int cb) {
  const float gf = (float)(l >> 4), cf = (float)(16 * cb + (l & 15));
  const float c32 = cf * (SGN / 32.f), a0 = gf * 8.f * c32;
  u4 fr, fi;
#pragma unroll
  for (int jj = 0; jj < 4; ++jj) {
    float r0 = (float)(2 * jj) * c32 + a0, r1 = (float)(2 * jj + 1) * c32 + a0;
    fr[jj] = pk_f16(fcos(r0), fcos(r1));
    fi[jj] = pk_f16(fsin(r0), fsin(r1));
  }
  *(u4*)(ct) = fr; *(u4*)(ct + 1024) = fi;
}
template <int SGN, int OB> DI void rc_G(char* ct, int l) {
  const int r16 = l & 15;
  const float gf = (float)(l >> 4), po4 = (float)(r16 & 1) * 0.25f;
  const float e20 = (float)((r16 >> 1) + 8 * OB) * (-SGN / 20.f), g0 = gf * e20 + po4;
  u4 v;
#pragma unroll
  for (int blk = 0; blk < 4; ++blk) { float r = (float)(4 * blk) * e20 + g0; v[blk] = pk_f16(fcos(r), fsin(r)); }
  *(u4*)(ct + (2 + OB) * 1024) = v;
  float r = 16.f * e20 + g0;
  *(unsigned*)(ct + 5 * 1024 + OB * 4) = pk_f16(fcos(r), fsin(r));
}
template <int SGN, int BLK> DI void rc_T(char* ct, int l, int cb, float tws) {
  const float r = (float)(((l >> 4) + 4 * BLK) * (16 * cb + (l & 15))) * (SGN / 640.f);
  const float tc = fcos(r) * tws, ts = fsin(r) * tws;
  if (BLK < 4) { *(float*)(ct + 6 * 1024 + BLK * 4) = tc; *(float*)(ct + 7 * 1024 + BLK * 4) = ts; }
  else { *(float*)(ct + 5 * 1024 + 12) = tc; *(float*)(ct + 8 * 1024 - l * 12) = ts; }
}
template <int SGN> DI void rc_part(char* ct, int l, int cb, float tws, int part) {
  if (part == 0) rc_F<SGN>(ct, l, cb);
  else if (part == 1) { rc_G<SGN, 0>(ct, l); rc_T<SGN, 0>(ct, l, cb, tws); rc_T<SGN, 1>(ct, l, cb, tws); }
  else if (part == 2) { rc_G<SGN, 1>(ct, l); rc_T<SGN, 2>(ct, l, cb, tws); rc_T<SGN, 3>(ct, l, cb, tws); }
  else { rc_G<SGN, 2>(ct, l); rc_T<SGN, 4>(ct, l, cb, tws); }
}
DI void rc_load(RowC& C, const char* ct, int l) {
#pragma unroll
  for (int j = 0; j < 8; ++j) C.t[j] = *(const u4*)(ct + j * 1024);
  C.t[8][0] = *(const unsigned*)(ct + 8 * 1024 - l * 12);
}
DI void row_stage_a(const RowC& C, const char* lds, int l, int ih, bool ii, u4& T0, u4& T1) {
  const int g = l >> 4, q4 = (l & 15) >> 2, p = l & 3;
  const char* base = lds + (8 * g + q4) * 320 + p * 16 + ih * 8;
  u2 lo[5], hi[5];
#pragma unroll
  for (int blk = 0; blk < 5; ++blk) { lo[blk] = tr_read(base + blk * 64); hi[blk] = tr_read(base + blk * 64 + 4 * 320); }
  T1 = u4{0u, 0u, 0u, 0u};
#pragma unroll
  for (int blk = 0; blk < 5; ++blk) {
    u4 a = {lo[blk][0], lo[blk][1], hi[blk][0], hi[blk][1]};
    f4 z = {0.f, 0.f, 0.f, 0.f};
    f4 pr = mfma_h(a, C.t[0], z);
    f4 pi = mfma_h(a, C.t[1], z);
    float pr0, pr1, pi0, pi1;
    if (ii) { pr0 = pr[2]; pr1 = pr[3]; pi0 = pi[2]; pi1 = pi[3]; } else { pr0 = pr[0]; pr1 = pr[1]; pi0 = pi[0]; pi1 = pi[1]; }
    const float tc = asf(blk < 4 ? C.t[6][blk & 3] : C.t[5][3]), ts = asf(blk < 4 ? C.t[7][blk & 3] : C.t[8][0]);
    float tr = pr0 - pi1, ti = pi0 + pr1;
    unsigned v = pk_f16(tr * tc - ti * ts, ti * tc + tr * ts);
    if (blk < 4) T0[blk & 3] = v; else T1[0] = v;
  }
}

template <bool II> DI void k1_dft(const RowC& C, const char* lin, char* lout, int l, int ih, int cb) {
  u4 T0, T1;
  row_stage_a(C, lin, l, ih, II, T0, T1);
  const int g = l >> 4, cc = 16 * cb + (l & 15);
  char* ob_ = lout + (cc + 64 * g) * 16 + (2 * ih + (int)II) * 4;
#pragma unroll
  for (int ob = 0; ob < 3; ++ob) {
    f4 z = {0.f, 0.f, 0.f, 0.f};
    z = mfma_h(C.t[2 + ob], T0, z);
    u4 ga = {C.t[5][ob], 0u, 0u, 0u};
    z = mfma_h(ga, T1, z);
    *(unsigned*)(ob_ + ob * 4096) = pk_f16(z[0], z[1]);
    *(unsigned*)(ob_ + ob * 4096 + 512) = pk_f16(z[2], z[3]);
  }
}
__global__ __launch_bounds__(512) void k1_kernel(const u16* __restrict__ x, const u16* __restrict__ wr, const u16* __restrict__ wi,
                                                 const u16* __restrict__ wd, char* __restrict__ ws) {
  __shared__ __attribute__((aligned(16))) char smem[32 * 1296];
  const int b = blockIdx.x, tid = threadIdx.x, l = tid & 63;
  const int w = __builtin_amdgcn_readfirstlane(tid >> 6);
  ST_DECL; ST(0);
  if (b >= 384) {
    const int ks = b - 384;
    if (ks >= 54) return;
    u4 a[5];
#pragma unroll
    for (int it = 0; it < 5; ++it) {
      int idx = tid + 512 * it, k = idx / 80, seg = idx - k * 80;
      a[it] = *(const u4*)(wd + (size_t)(8 * blk_of_pos(4 * ks + (k >> 3)) + (k & 7)) * DD + 8 * seg);
    }
    SCHED_FENCE();
#pragma unroll
    for (int it = 0; it < 5; ++it) {
      int idx = tid + 512 * it, k = idx / 80, seg = idx - k * 80;
      *(u4*)(smem + k * 1296 + seg * 16) = a[it];
    }
    raw_barrier();
    u4* W2 = (u4*)(ws + OFF_W2) + (size_t)(ks * 40 + 5 * w) * 64 + l;
    const char* base = smem + (8 * (l >> 4) + ((l & 15) >> 2)) * 1296 + (l & 3) * 8 + w * 160;
#pragma unroll
    for (int t = 0; t < 5; ++t) {
      u2 lo = tr_read(base + t * 32), hi = tr_read(base + t * 32 + 4 * 1296);
      W2[t * 64] = u4{lo[0], lo[1], hi[0], hi[1]};
    }
    ST_FLUSH(0);
    return;
  }
  if (b >= 256) {
    const int task = b - 256, u = task & 7, v = (task >> 3) & 7, hf = task >> 6;
    u4 d[3];
#pragma unroll
    for (int it = 0; it < 3; ++it) {
      int idx = tid + 512 * it; if (idx >= 1280) idx = tid;
      int rho = idx / 20, pc = idx - rho * 20;
      int row = 64 * (4 * v + (rho & 3)) + 8 * u + ((rho >> 2) & 7);
      const u16* src = (rho >= 32 ? wi : wr) + (size_t)row * 321 + 160 * hf + 8 * pc;
      d[it] = *(const u4u*)src;
    }
    unsigned last = 0;
    if (tid < 64) {
      int row = 64 * (4 * v + (tid & 3)) + 8 * u + ((tid >> 2) & 7);
      last = ((tid >= 32 ? wi : wr) + (size_t)row * 321)[320];
    }
    SCHED_FENCE();
    unsigned mx = last & 0x7fffu;
#pragma unroll
    for (int it = 0; it < 3; ++it) {
      int idx = tid + 512 * it; if (idx >= 1280) idx = tid;
      int rho = idx / 20, pc = idx - rho * 20;
      *(u4*)(smem + rho * 336 + pc * 16) = d[it];
#pragma unroll
      for (int e = 0; e < 4; ++e) mx = pkmax_abs(mx, d[it][e]);
    }
    if (tid < 64) *(u16*)(smem + tid * 336 + 320) = (u16)last;
    mx = wave_umax(umax(mx & 0xffffu, mx >> 16));
    if (l == 0) *(unsigned*)(smem + 21504 + w * 4) = mx;
    raw_barrier();
    u4* Wt = (u4*)(ws + OFF_WT);
    const int obase = ((u >> 1) * 2 + (v >> 2)) * 64 + 16 * (v & 3) + 8 * (u & 1);
    const int g = l >> 4, q4 = (l & 15) >> 2, p = l & 3;
#pragma unroll
    for (int rd = 0; rd < 3; ++rd) {
      int job = w + 8 * rd;
      if (job < 20) {
        int cbk = job >> 1, r8 = 4 * (job & 1) + g;
        const char* a = smem + (r8 * 4 + q4) * 336 + (16 * cbk + 4 * p) * 2;
        u2 re = tr_read(a), im = tr_read(a + 32 * 336);
        u4 o = {perm_lo(im[0], re[0]), perm_hi(im[0], re[0]), perm_lo(im[1], re[1]), perm_hi(im[1], re[1])};
        NTS(&Wt[(160 * hf + 16 * cbk + (l & 15)) * 512 + obase + r8], o);
      }
    }
    if (hf == 1 && tid < 8) {
      u4 o;
#pragma unroll
      for (int reg = 0; reg < 4; ++reg)
        o[reg] = (unsigned)*(const u16*)(smem + (tid * 4 + reg) * 336 + 320) | ((unsigned)*(const u16*)(smem + (32 + tid * 4 + reg) * 336 + 320) << 16);
      Wt[320 * 512 + obase + tid] = o;
    }
    if (tid == 0) {
      const unsigned* m = (const unsigned*)(smem + 21504);
      ((unsigned*)(ws + OFF_WM))[task] = umax(umax(umax(m[0], m[1]), umax(m[2], m[3])), umax(umax(m[4], m[5]), umax(m[6], m[7])));
    }
    ST_FLUSH(0);
    return;
  }
  if (b == 0 && tid >= 448 && tid < 504) *(unsigned*)(ws + OFF_CNT + (tid - 448) * 256) = 0u;
  const int q = (b & 7) * 32 + (b >> 3);
  const int ih = (w >> 1) & 1, cb = w & 1;
  char* lin = smem;
  char* lout = smem + 10240;
  char* ct = smem + 22528 + cb * 8448 + l * 16;
  RowC C;
  if (w < 4) {
    const int j = l >> 5, li = l & 31;
    const u2* xr = (const u2*)(x + (size_t)srow_of(q, w, j) * DD);
    u2 xv[5];
#pragma unroll
    for (int c = 0; c < 5; ++c) xv[c] = xr[li + 32 * c];
    float v[20];
#pragma unroll
    for (int c = 0; c < 5; ++c) {
      v[4 * c] = bf_lo(xv[c][0]); v[4 * c + 1] = bf_hi(xv[c][0]);
      v[4 * c + 2] = bf_lo(xv[c][1]); v[4 * c + 3] = bf_hi(xv[c][1]);
    }
    float sa = 0.f, sb = 0.f;
#pragma unroll
    for (int t = 0; t < 10; ++t) { sa += v[2 * t]; sb += v[2 * t + 1]; }
    const float mean = half_sum(sa + sb) * (1.f / 640.f);
    ST(1);
    sa = 0.f; sb = 0.f;
#pragma unroll
    for (int t = 0; t < 10; ++t) { float d0 = v[2 * t] - mean, d1 = v[2 * t + 1] - mean; sa += d0 * d0; sb += d1 * d1; }
    const float var = half_sum(sa + sb) * (1.f / 640.f);
    const float sd = rbf(__builtin_sqrtf(rbf(rbf(var) + 1e-5f)));
    const float rs = 16.f * __builtin_amdgcn_rcpf(sd);
    const float sh = -rbf(mean) * rs;
    char* wp = lin + (4 * li + 2 * j) * 16 + w * 4;
#pragma unroll
    for (int c = 0; c < 5; ++c) {
      unsigned p01 = pk_f16(v[4 * c] * rs + sh, v[4 * c + 1] * rs + sh);
      unsigned p23 = pk_f16(v[4 * c + 2] * rs + sh, v[4 * c + 3] * rs + sh);
      auto r = __builtin_amdgcn_permlane32_swap(p01, p23, false, false);
      unsigned pa = r[0], pb = r[1];
      *(unsigned*)(wp + c * 2048) = perm_lo(pb, pa);
      *(unsigned*)(wp + c * 2048 + 16) = perm_hi(pb, pa);
    }
    ST(2);
    raw_barrier();
    ST(3);
    rc_load(C, ct, l);
    k1_dft<false>(C, lin, lout, l, ih, cb);
  } else {
    rc_part<-1>(ct, l, cb, 1.f / 256.f, 2 * ih);
    rc_part<-1>(ct, l, cb, 1.f / 256.f, 2 * ih + 1);
    raw_barrier();
    rc_load(C, ct, l);
    k1_dft<true>(C, lin, lout, l, ih, cb);
  }
  raw_barrier();
  ST(4);
  u4* Z1 = (u4*)(ws + OFF_Z1);
  Z1[Z1IDX(q, tid)] = *(const u4*)(lout + tid * 16);
  if (tid < 128) Z1[Z1IDX(q, tid + 512)] = *(const u4*)(lout + (tid + 512) * 16);
  ST_FLUSH(0);
}

DI unsigned wmax_exp(u2 m) {
  unsigned eb = wave_umax(umax(m[0], m[1])) >> 7;
  return eb > 251u ? 251u : eb;
}
__global__ __launch_bounds__(256) void k2_kernel(const u16* __restrict__ wg, const u16* __restrict__ wu, const u16* __restrict__ wd,
                                                 char* __restrict__ ws) {
  __shared__ __attribute__((aligned(16))) char smem[32 * 880];
  const int b = blockIdx.x, tid = threadIdx.x, l = tid & 63;
  const int w = __builtin_amdgcn_readfirstlane(tid >> 6);
  const int g = l >> 4, r16 = l & 15;
  ST_DECL; ST(0);
  if (b >= 488) {
    const int task = b - 488, ks = task >> 1, hf = task & 1;
    if (ks >= 54) return;
    u4 a[5];
#pragma unroll
    for (int it = 0; it < 5; ++it) {
      int idx = tid + 256 * it, k = idx / 40, seg = idx - k * 40;
      a[it] = *(const u4*)(wd + (size_t)(8 * blk_of_pos(4 * ks + (k >> 3)) + (k & 7)) * DD + 320 * hf + 8 * seg);
    }
    SCHED_FENCE();
#pragma unroll
    for (int it = 0; it < 5; ++it) {
      int idx = tid + 256 * it, k = idx / 40, seg = idx - k * 40;
      *(u4*)(smem + k * 656 + seg * 16) = a[it];
    }
    raw_barrier();
    u4* W2 = (u4*)(ws + OFF_W2) + (size_t)(ks * 40 + 20 * hf + 5 * w) * 64 + l;
    const char* base = smem + (8 * g + (r16 >> 2)) * 656 + (l & 3) * 8 + w * 160;
#pragma unroll
    for (int t = 0; t < 5; ++t) {
      u2 lo = tr_read(base + t * 32), hi = tr_read(base + t * 32 + 4 * 656);
      W2[t * 64] = u4{lo[0], lo[1], hi[0], hi[1]};
    }
    ST_FLUSH(1);
    return;
  }
  if (b >= 328) {
    const int fg = b & 7, ks = (b - 328) >> 3;
    u4 a[4], c[4];
#pragma unroll
    for (int it = 0; it < 4; ++it) {
      int idx = tid + 256 * it; idx = idx < 864 ? idx : tid;
      int k = idx / 27, seg = idx - k * 27;
      const size_t off = (size_t)(32 * ks + k) * FF + 216 * fg + 8 * seg;
      a[it] = *(const u4*)(wg + off); c[it] = *(const u4*)(wu + off);
    }
    SCHED_FENCE();
#pragma unroll
    for (int it = 0; it < 4; ++it) {
      int idx = tid + 256 * it; idx = idx < 864 ? idx : tid;
      int k = idx / 27, seg = idx - k * 27;
      u4 o0 = {perm_lo(c[it][0], a[it][0]), perm_hi(c[it][0], a[it][0]), perm_lo(c[it][1], a[it][1]), perm_hi(c[it][1], a[it][1])};
      u4 o1 = {perm_lo(c[it][2], a[it][2]), perm_hi(c[it][2], a[it][2]), perm_lo(c[it][3], a[it][3]), perm_hi(c[it][3], a[it][3])};
      *(u4*)(smem + k * 880 + seg * 32) = o0;
      *(u4*)(smem + k * 880 + seg * 32 + 16) = o1;
    }
    raw_barrier();
    u4* W1 = (u4*)(ws + OFF_W1) + (size_t)(ks * 216 + fg * 27 + w) * 64 + l;
    const char* base = smem + (8 * g + (r16 >> 2)) * 880 + (l & 3) * 8 + w * 32;
#pragma unroll
    for (int t = 0; t < 7; ++t) {
      if (t < 6 || w < 3) {
        u2 lo = tr_read(base + t * 128), hi = tr_read(base + t * 128 + 4 * 880);
        NTS(&W1[t * 256], (u4{lo[0], lo[1], hi[0], hi[1]}));
      }
    }
    ST_FLUSH(1);
    return;
  }
  const int xi = b >> 3;
  int kd = 40 * (b & 7) + xi;
  if (xi == 40) { if ((b & 7) != 7) return; kd = 320; }
  const int kdm = kd == 0 ? 0 : 640 - kd;
  const u4* Z1 = (const u4*)(ws + OFF_Z1);
  const u4 z1 = Z1[Z1IDX(tid, kd)], z2 = Z1[Z1IDX(tid, kdm)];
  u4 wv[2];
  {
    const u4* Wt = (const u4*)(ws + OFF_WT) + kd * 512 + 128 * w + l;
    wv[0] = Wt[0]; wv[1] = Wt[64];
  }
  const u2 wmv = ((const u2*)(ws + OFF_WM))[l];
  SCHED_FENCE();
  u4 M1[4], M2[4], N1[4], N2[4];
  float TC[4][4], TS[4][4];
  {
    const float g4 = (float)(4 * g), k1f = (float)(16 * w + r16);
    const float ka = k1f * (1.f / 64.f), ka0 = g4 * ka;
    const bool odd = r16 & 1;
    const unsigned s1 = (r16 & 2) ? 0x80008000u : 0u;
    const unsigned s1b = (((r16 & 3) + 1) & 2) ? 0x80008000u : 0u;
    const unsigned s2 = odd ? 0x80008000u : 0u;
    const unsigned s3 = ((3 * (r16 & 3)) & 2) ? 0x80008000u : 0u, s3b = ((3 * (r16 & 3) + 1) & 2) ? 0x80008000u : 0u;
#pragma unroll
    for (int jj = 0; jj < 4; ++jj) {
      float r = (float)jj * ka + ka0, c = fcos(r), sn = fsin(r);
      const unsigned a = pk_f16(c, sn), bq = pk_f16(-sn, c);
      const unsigned p = odd ? bq : a, q2 = odd ? a : bq;
      M1[0][jj] = a; M2[0][jj] = bq;
      M1[1][jj] = p ^ s1; M2[1][jj] = q2 ^ s1b;
      M1[2][jj] = a ^ s2; M2[2][jj] = bq ^ s2;
      M1[3][jj] = p ^ s3; M2[3][jj] = q2 ^ s3b;
    }
    const float ro = (float)r16 * (1.f / 32.f), ro0 = g4 * ro;
#pragma unroll
    for (int jj = 0; jj < 4; ++jj) {
      float r = (float)jj * ro + ro0, c = fcos(r), sn = fsin(r);
      const unsigned a = pk_f16(c, sn), bq = pk_f16(-sn, c), sj = (jj & 1) ? 0x80008000u : 0u;
      N1[0][jj] = a; N2[0][jj] = bq;
      N1[1][jj] = a ^ s2; N2[1][jj] = bq ^ s2;
      N1[2][jj] = a ^ sj; N2[2][jj] = bq ^ sj;
      N1[3][jj] = a ^ (s2 ^ sj); N2[3][jj] = bq ^ (s2 ^ sj);
    }
    {
      const float kb = k1f * (1.f / 2048.f);
      const float c1 = fcos(kb), d1 = fsin(kb), c16 = fcos(16.f * kb), d16 = fsin(16.f * kb);
      TC[0][0] = fcos(g4 * kb); TS[0][0] = fsin(g4 * kb);
#pragma unroll
      for (int rg = 1; rg < 4; ++rg) {
        TC[0][rg] = TC[0][rg - 1] * c1 - TS[0][rg - 1] * d1; TS[0][rg] = TS[0][rg - 1] * c1 + TC[0][rg - 1] * d1;
      }
#pragma unroll
      for (int rg = 0; rg < 4; ++rg) { TC[1][rg] = TC[0][rg] * c16 - TS[0][rg] * d16; TS[1][rg] = TS[0][rg] * c16 + TC[0][rg] * d16; }
    }
#pragma unroll
    for (int xx = 0; xx < 2; ++xx) {
      const float nu = (float)(16 * xx + r16) * (1.f / 2048.f), nu0 = ((float)(16 * w) + g4) * nu;
      const float c1 = fcos(nu), d1 = fsin(nu);
      TC[2 + xx][0] = fcos(nu0); TS[2 + xx][0] = fsin(nu0);
#pragma unroll
      for (int rg = 1; rg < 4; ++rg) {
        TC[2 + xx][rg] = TC[2 + xx][rg - 1] * c1 - TS[2 + xx][rg - 1] * d1;
        TS[2 + xx][rg] = TS[2 + xx][rg - 1] * c1 + TC[2 + xx][rg - 1] * d1;
      }
    }
  }
  SCHED_FENCE();
  u4* sa = (u4*)smem;
  u4* ex = (u4*)(smem + 8192);
  {
    u4 A0, A1;
#pragma unroll
    for (int jj = 0; jj < 4; ++jj) {
      h2 p = __builtin_bit_cast(h2, (unsigned)z1[jj]), qq = __builtin_bit_cast(h2, (unsigned)z2[jj]);
      h2 qa = {qq[0], -qq[1]};
      h2 pb = {p[1], -p[0]}, qb = {qq[1], qq[0]};
      A0[jj] = __builtin_bit_cast(unsigned, p + qa);
      A1[jj] = __builtin_bit_cast(unsigned, pb + qb);
    }
    sa[tid] = A0;
    sa[256 + tid] = A1;
  }
  ST(1);
  raw_barrier();
  const f4 zero = {0.f, 0.f, 0.f, 0.f};
  f4 Tr[2] = {zero, zero}, Ti[2] = {zero, zero};
#pragma unroll
  for (int t = 0; t < 4; ++t) {
    u4 A0 = sa[64 * t + l], A1 = sa[256 + 64 * t + l];
    Tr[0] = mfma_h(A0, M1[t], Tr[0]); Tr[1] = mfma_h(A1, M1[t], Tr[1]);
    Ti[0] = mfma_h(A0, M2[t], Ti[0]); Ti[1] = mfma_h(A1, M2[t], Ti[1]);
  }
  ST(2);
  const unsigned eb = wmax_exp(wmv);
  const float fs = asf((252u - eb) << 23) * (1.f / 256.f);
  u4 X[2];
#pragma unroll
  for (int rb = 0; rb < 2; ++rb)
#pragma unroll
    for (int rg = 0; rg < 4; ++rg) {
      float a = Tr[rb][rg], c = Ti[rb][rg], tc = TC[rb][rg], ts = TS[rb][rg];
      X[rb][rg] = pk_f16(a * tc + c * ts, c * tc - a * ts);
    }
  u4 Y[2];
#pragma unroll
  for (int ob = 0; ob < 2; ++ob) {
    f4 xr = mfma_h(N1[2 * ob], X[0], zero); xr = mfma_h(N1[2 * ob + 1], X[1], xr);
    f4 xim = mfma_h(N2[2 * ob], X[0], zero); xim = mfma_h(N2[2 * ob + 1], X[1], xim);
#pragma unroll
    for (int rg = 0; rg < 4; ++rg) {
      float wr_ = bf_lo(wv[ob][rg]) * fs, wi_ = bf_hi(wv[ob][rg]) * fs;
      float yr = xr[rg] * wr_ - xim[rg] * wi_;
      float yi = xr[rg] * wi_ + xim[rg] * wr_;
      Y[ob][rg] = pk_f16(yi, yr);
    }
  }
#pragma unroll
  for (int cb = 0; cb < 2; ++cb) {
    f4 ui = mfma_h(Y[0], N1[2 * cb], zero); ui = mfma_h(Y[1], N1[2 * cb + 1], ui);
    f4 ur = mfma_h(Y[0], N2[2 * cb], zero); ur = mfma_h(Y[1], N2[2 * cb + 1], ur);
    u4 U;
#pragma unroll
    for (int rg = 0; rg < 4; ++rg) {
      float tc = TC[2 + cb][rg], ts = TS[2 + cb][rg];
      U[rg] = pk_f16(ui[rg] * tc + ur[rg] * ts, ur[rg] * tc - ui[rg] * ts);
    }
    ex[(w * 2 + cb) * 64 + l] = U;
  }
  ST(3);
  raw_barrier();
  ST(4);
  f4 Vi[2] = {zero, zero}, Vr[2] = {zero, zero};
#pragma unroll
  for (int t = 0; t < 4; ++t)
#pragma unroll
    for (int cb = 0; cb < 2; ++cb) {
      u4 U = ex[(t * 2 + cb) * 64 + l];
      Vi[cb] = mfma_h(M1[t], U, Vi[cb]);
      Vr[cb] = mfma_h(M2[t], U, Vr[cb]);
    }
  u4* Z2 = (u4*)(ws + OFF_Z2);
  const float sg = (kd == 0 || kd == 320) ? 0.f : 1.f;
  u4 o, om;
#pragma unroll
  for (int rg = 0; rg < 4; ++rg) {
    float ia = Vi[0][rg] * sg, ib = Vi[1][rg] * sg;
    o[rg] = pk_f16(Vr[0][rg] - ib, ia + Vr[1][rg]);
    om[rg] = pk_f16(Vr[0][rg] + ib, Vr[1][rg] - ia);
  }
  Z2[Z2IDX(tid, kd)] = o;
  if (kdm != kd) Z2[Z2IDX(tid, kdm)] = om;
  ST_FLUSH(1);
}

#define GLDS(src, dst) __builtin_amdgcn_global_load_lds((const GAS void*)(src), (LAS void*)(dst), 16, 0, 0)
#define LDAUX 16
#define ACQ 0
#define GLDS_SC1(src, dst) __builtin_amdgcn_global_load_lds((const GAS void*)(src), (LAS void*)(dst), 16, 0, LDAUX)
template <int N> DI void wait_vm() { asm volatile("s_waitcnt vmcnt(%0)" ::"n"(N) : "memory"); }
#define SGB(mask, n) __builtin_amdgcn_sched_group_barrier(mask, n, 0)

DI float geglu(float g, float u) {
  float t = g * g;
  float z = g * (t * (-2.f * 1.4426950408889634f * 0.7978845608028654f * 0.044715f) + (-2.f * 1.4426950408889634f * 0.7978845608028654f));
  float e = __builtin_amdgcn_exp2f(z);
  return (g * u) * __builtin_amdgcn_rcpf(1.f + e);
}
DI void poll_ge(const char* ws, int cnt, unsigned want) {
  const GAS unsigned* p = (const GAS unsigned*)(ws + OFF_CNT + cnt * 256);
  for (int spins = 0; spins < (1 << 22); ++spins) {
    unsigned v = __hip_atomic_load(p, __ATOMIC_RELAXED, __HIP_MEMORY_SCOPE_AGENT);
    if (__builtin_amdgcn_readfirstlane(v) >= want) break;
    __builtin_amdgcn_s_sleep(1);
  }
  asm volatile("" ::: "memory");
}
DI void poll_ge16(const char* ws, int cnt) { poll_ge(ws, cnt, 16u); }
DI void signal(char* ws, int cnt) {
  __hip_atomic_fetch_add((GAS unsigned*)(ws + OFF_CNT + cnt * 256), 1u, __ATOMIC_RELAXED, __HIP_MEMORY_SCOPE_AGENT);
}

constexpr int G1_STG = 22 * 1024, G1_NB = 7, G1_NS = 20, G1_TAIL = 4, G1_MAIN = G1_NS - G1_TAIL;
constexpr int LDS_TOTAL = 160 * 1024, PA_OFF = 6 * G1_STG;
constexpr int G2_NS = 14, G2_AD = 6;
#define EARLY_AT 5

DI void arrive_n(char* smem, char* ws, int word, int cnt, int l, unsigned last) {
  if (l == 0) {
    unsigned old = __hip_atomic_fetch_add((LAS unsigned*)(smem + LDS_TOTAL - 64 + word * 4), 1u, __ATOMIC_RELAXED, __HIP_MEMORY_SCOPE_WORKGROUP);
    if (old == last) signal(ws, cnt);
  }
}
template <int NW> DI void k3_loader(char* smem, char* ws, int mt, int nb0, int mt2, int db0, int l, int j) {
  const unsigned loff = l * 16;
  constexpr int NL = NW + 2;
  const char* sA = ws + OFF_HP + (size_t)(8 * mt + j) * 1024 + loff;
  const char* sB = ws + OFF_W1 + (size_t)(nb0 + j) * 1024 + loff;
  auto issueA = [&](int ks) {
    char* d = smem + (ks % G1_NB) * G1_STG + j * 1024;
    GLDS_SC1(sA + (size_t)ks * (128 * 1024), d);
    GLDS_SC1(sA + (size_t)ks * (128 * 1024) + 4096, d + 4096);
  };
  auto issueW = [&](int ks) {
    char* d = smem + (ks % G1_NB) * G1_STG + 8192 + j * 1024;
#pragma unroll
    for (int t = 0; t < NW; ++t) GLDS(sB + (size_t)ks * (216 * 1024) + t * 4096, d + t * 4096);
  };
  int foff[5];
#pragma unroll
  for (int t = 0; t < 5; ++t) { int f = j + 4 * t, kq = f / 5; foff[t] = (kq * 40 + db0 + f - 5 * kq) * 1024; }
  const char* s2 = ws + OFF_W2 + loff;
  auto issue2 = [&](int i) {
    char* d = smem + ((i + 6) % G1_NB) * G1_STG + j * 1024;
#pragma unroll
    for (int t = 0; t < 5; ++t) GLDS(s2 + (size_t)i * (160 * 1024) + foff[t], d + t * 4096);
  };
  raw_barrier();
  issueW(0); issueW(1); issueW(2); issueW(5);
  raw_barrier(); raw_barrier(); raw_barrier();
  issueW(3); issueW(4); issueW(6);
  poll_ge16(ws, mt);
  u4 pk[33];
  auto fetch = [&](int e) {
    const int i = 7 + e / 5, t = e % 5;
    pk[e] = *(const u4*)(s2 + (size_t)i * (160 * 1024) + (e == 32 && j >= 2 ? foff[1] : foff[t]));
  };
  auto park = [&](int i) {
    char* d = smem + ((i + 6) % G1_NB) * G1_STG + j * 1024 + loff;
#pragma unroll
    for (int t = 0; t < (i < 13 ? 5 : 3); ++t) *(u4*)(d + t * 4096) = pk[(i - 7) * 5 + t];
  };
#pragma unroll
  for (int s = 0; s < 7; ++s) issueA(s);
  wait_vm<12>();
  raw_barrier();
  static_for<G1_MAIN - 1>([&](auto S_) {
    constexpr int s = decltype(S_)::value;
    constexpr int n = s <= 5 ? 2 * (5 - s) + NL * s : (s <= 13 ? NL * ((s + 6 < 19 ? s + 6 : 19) - (s + 1)) : NL * (19 - (s + 1)) + 5 * (s - 13));
    constexpr int lo = s - 6 > 0 ? s - 6 : 0, hi = s - 1 < 10 ? s - 1 : 10;
    wait_vm<n + (hi >= lo ? 3 * (hi - lo + 1) : 0)>();
    raw_barrier();
    if constexpr (s + G1_NB < G1_NS) { issueA(s + G1_NB); issueW(s + G1_NB); }
    else issue2(s - 13);
    if constexpr (s <= 10) { SCHED_FENCE(); fetch(3 * s); fetch(3 * s + 1); fetch(3 * s + 2); SCHED_FENCE(); }
  });
  wait_vm<10>();
  raw_barrier();
  issue2(2);
  raw_barrier();
  issue2(3); issue2(4); issue2(5); issue2(6);
  wait_vm<0>();
  raw_barrier();
  if (j == 0) poll_ge16(ws, 32 + (mt2 >> 1));
  raw_barrier();
  park(7); park(8); park(9);
  raw_barrier();
  park(10); park(11);
  raw_barrier();
  park(12); park(13);
  raw_barrier();
}
DI void arrive(char* smem, char* ws, int word, int cnt, int l) { arrive_n(smem, ws, word, cnt, l, 7u); }

__global__ __launch_bounds__(768, 3) void k3_kernel(const u16* __restrict__ x, u16* __restrict__ out, char* __restrict__ ws) {
  extern __shared__ __attribute__((aligned(16))) char smem[];
  const int b = blockIdx.x, tid = threadIdx.x, l = tid & 63;
  const int w = __builtin_amdgcn_readfirstlane(tid >> 6);
  const int xcd = b & 7, idx = b >> 3;
  const int mt = 8 * (xcd & 1) + (idx & 7), nt = 4 * (xcd >> 1) + (idx >> 3);
  const int mt2 = 16 * (xcd & 1) + 8 * ((xcd >> 1) & 1) + (idx & 7), nt2 = 4 * (xcd >> 2) + (idx >> 3);
  const int nb0 = (27 * nt) >> 1, mb0 = 8 * mt, db0 = 5 * nt2;
  const unsigned loff = l * 16;
  ST_DECL; ST(0);
  if (w >= 8) {
    if (w < 10) k3_loader<4>(smem, ws, mt, nb0, mt2, db0, l, w - 8); else k3_loader<3>(smem, ws, mt, nb0, mt2, db0, l, w - 8);
    return;
  }
  const int g = l >> 4, r16 = l & 15;
  const auto rsrc = __builtin_amdgcn_make_buffer_rsrc(ws, 0, (int)WS_USED, 0x00020000);
  {
    char* pa = smem + PA_OFF;
    const int q = 16 * mt + nt;
    const int ih = (w >> 1) & 1, cb = w & 1, ii = w >> 2, j = l & 1;
    const u4* Z2 = (const u4*)(ws + OFF_Z2);
    const u4 zv0 = Z2[Z2IDX(q, tid)];
    const u4 zv1 = Z2[Z2IDX(q, 512 + (tid & 127))];
    const int e0 = r16 >> 1;
    const bool v2 = e0 < 4;
    const size_t roff = (size_t)srow_of(q, 2 * ih + ii, j) * DD + 16 * cb + 4 * g + 32 * e0;
    u2 xv[3];
    xv[0] = *(const u2*)(x + roff); xv[1] = *(const u2*)(x + roff + 256); xv[2] = *(const u2*)(x + roff + (v2 ? 512 : 0));
    const u2 wmv = ((const u2*)(ws + OFF_WM))[l];
    SCHED_FENCE();
    char* ct = pa + 10368 + cb * 8448 + l * 16;
    rc_part<1>(ct, l, cb, 1.f / 32.f, w >> 1);
    SCHED_FENCE();
    *(u4*)(pa + tid * 16) = zv0;
    if (tid < 128) *(u4*)(pa + (512 + tid) * 16) = zv1;
    if (tid < 3) *(unsigned*)(smem + LDS_TOTAL - 64 + tid * 4) = 0u;
    raw_barrier();
    RowC C;
    rc_load(C, ct, l);
    u4 T0, T1;
    if (ii) row_stage_a(C, pa, l, ih, true, T0, T1); else row_stage_a(C, pa, l, ih, false, T0, T1);
    const unsigned eb = wmax_exp(wmv);
    const float ysc = asf((eb + 2u) << 23) * 0.05f;
    float h[12];
    float s = 0.f;
    const int mp = 8 * q + 2 * (2 * ih + ii) + j;
    char* hs = smem + 4 * G1_STG - 2048;
    char* ns = smem + 5 * G1_STG - 2048;
    const int r8 = mp & 7;
#pragma unroll
    for (int ob = 0; ob < 3; ++ob) {
      f4 z = {0.f, 0.f, 0.f, 0.f};
      z = mfma_h(T0, C.t[2 + ob], z);
      u4 gb = {C.t[5][ob], 0u, 0u, 0u};
      z = mfma_h(T1, gb, z);
      unsigned y01 = pk_bf16(z[0] * ysc, z[1] * ysc), y23 = pk_bf16(z[2] * ysc, z[3] * ysc);
      unsigned h01 = pk_bf16(bf_lo(xv[ob][0]) + bf_lo(y01), bf_hi(xv[ob][0]) + bf_hi(y01));
      unsigned h23 = pk_bf16(bf_lo(xv[ob][1]) + bf_lo(y23), bf_hi(xv[ob][1]) + bf_hi(y23));
      if (ob == 2) { h01 = v2 ? h01 : 0u; h23 = v2 ? h23 : 0u; }
      if (ob < 2 || v2) *(u2*)(hs + ((2 * e0 + cb + 16 * ob) * 8 + r8) * 32 + g * 8) = u2{h01, h23};
      h[4 * ob] = bf_lo(h01); h[4 * ob + 1] = bf_hi(h01); h[4 * ob + 2] = bf_lo(h23); h[4 * ob + 3] = bf_hi(h23);
      s += (h[4 * ob] + h[4 * ob + 1]) + (h[4 * ob + 2] + h[4 * ob + 3]);
    }
    const float mloc = parity_sum(s) * (1.f / 320.f);
    float s2 = 0.f, s3 = 0.f;
#pragma unroll
    for (int t = 0; t < 8; ++t) { float d = h[t] - mloc; s2 += d * d; }
#pragma unroll
    for (int t = 8; t < 12; ++t) { float d = h[t] - mloc; s3 += d * d; }
    const float m2loc = parity_sum(v2 ? s2 + s3 : s2);
    f2* ex = (f2*)(pa + 10240);
    if (l < 2) ex[w * 2 + l] = f2{mloc, m2loc};
    raw_barrier();
    {
      const int hb = (int)OFF_HH + 8 * q * 32;
      __builtin_amdgcn_raw_buffer_store_b128(*(const u4*)(hs + tid * 16), rsrc, (tid >> 4) * (2048 * 32) + (tid & 15) * 16, hb, 16);
      if (tid < 128) __builtin_amdgcn_raw_buffer_store_b128(*(const u4*)(hs + 8192 + tid * 16), rsrc, (32 + (tid >> 4)) * (2048 * 32) + (tid & 15) * 16, hb, 16);
    }
    const f2 o = ex[(w ^ 1) * 2 + j];
    const float mean = 0.5f * (mloc + o[0]);
    const float dl = mloc - o[0];
    const float var = (m2loc + o[1] + dl * dl * 160.f) * (1.f / 640.f);
    const float mb = rbf(mean);
    const float sd = rbf(__builtin_sqrtf(rbf(rbf(var) + 1e-5f)));
    const float rs = __builtin_amdgcn_rcpf(sd);
#pragma unroll
    for (int ob = 0; ob < 3; ++ob) {
      unsigned d01 = pk_bf16(h[4 * ob] - mb, h[4 * ob + 1] - mb);
      unsigned d23 = pk_bf16(h[4 * ob + 2] - mb, h[4 * ob + 3] - mb);
      u2 st = {pk_bf16(bf_lo(d01) * rs, bf_hi(d01) * rs), pk_bf16(bf_lo(d23) * rs, bf_hi(d23) * rs)};
      if (ob < 2 || v2) *(u2*)(ns + (((8 * ob + e0) * 4 + 2 * cb + (g >> 1)) * 8 + r8) * 16 + (g & 1) * 8) = st;
    }
    raw_barrier();
    {
      const int nbase = (int)OFF_HP + (q >> 1) * 1024 + (q & 1) * 128;
      __builtin_amdgcn_raw_buffer_store_b128(*(const u4*)(ns + tid * 16), rsrc, (tid >> 5) * (128 * 1024) + ((tid >> 3) & 3) * 256 + (tid & 7) * 16, nbase, 16);
      if (tid < 128) __builtin_amdgcn_raw_buffer_store_b128(*(const u4*)(ns + 8192 + tid * 16), rsrc, (16 + (tid >> 5)) * (128 * 1024) + ((tid >> 3) & 3) * 256 + (tid & 7) * 16, nbase, 16);
    }
    ST(1);
    wait_vm<0>();
    raw_barrier();
    if (tid == 0) signal(ws, mt);
    ST(2);
  }
  {
    int lb = threadIdx.x & 63;
    asm volatile("" : "+v"(lb));
    const unsigned loff = lb * 16;
    const int wm = w >> 1, wn = w & 1;
    const char* rdA = smem + (2 * wm) * 1024 + loff;
    const char* rdB = smem + (8 + 7 * wn) * 1024 + loff;
    f4 acc[7][2];
#pragma unroll
    for (int i = 0; i < 7; ++i) { acc[i][0] = f4{0.f, 0.f, 0.f, 0.f}; acc[i][1] = f4{0.f, 0.f, 0.f, 0.f}; }
    {
      u4 af[2][2], wf[2][7];
      raw_barrier();
      ST(3);
#pragma unroll
      for (int i = 0; i < 2; ++i) af[0][i] = *(const u4*)(rdA + i * 1024);
#pragma unroll
      for (int i = 0; i < 7; ++i) wf[0][i] = *(const u4*)(rdB + i * 1024);
      static_for<G1_MAIN>([&](auto S_) {
        constexpr int s = decltype(S_)::value;
        constexpr int cur = s & 1, nxt = cur ^ 1;
        constexpr bool more = s + 1 < G1_MAIN;
        if constexpr (more) {
          raw_barrier();
          constexpr int bo = ((s + 1) % G1_NB) * G1_STG;
#pragma unroll
          for (int i = 0; i < 2; ++i) af[nxt][i] = *(const u4*)(rdA + bo + i * 1024);
#pragma unroll
          for (int i = 0; i < 7; ++i) wf[nxt][i] = *(const u4*)(rdB + bo + i * 1024);
        }
#pragma unroll
        for (int i = 0; i < 7; ++i)
#pragma unroll
          for (int m = 0; m < 2; ++m) acc[i][m] = mfma_b(wf[cur][i], af[cur][m], acc[i][m]);
        if constexpr (more) {
#pragma unroll
          for (int i = 0; i < 9; ++i) { SGB(0x8, 1); SGB(0x100, 1); }
          SGB(0x8, 14);
        }
        SCHED_FENCE();
      });
    }
    raw_barrier();
    int lz = threadIdx.x & 63;
    asm volatile("" : "+v"(lz));
    const int g = lz >> 4, r16 = lz & 15;
    u4 af[G1_TAIL][2], wf[2][G1_TAIL];
#pragma unroll
    for (int t = 0; t < G1_TAIL; ++t) {
      const int bo = ((G1_MAIN + t) % G1_NB) * G1_STG;
      wf[0][t] = *(const u4*)(rdB + bo);
      af[t][0] = *(const u4*)(rdA + bo); af[t][1] = *(const u4*)(rdA + bo + 1024);
    }
    const int aoff = (int)OFF_AP + (mb0 + 2 * wm) * 1024 + r16 * 16 + 4 * g;
    auto epi = [&](int i) {
      const int pos = i < 4 ? 8 * nt + 4 * wn + i : 128 + 11 * (nt >> 1) + 5 * (nt & 1) + 3 * wn + (i - 4);
      const int so = (pos >> 2) * (128 * 1024) + (pos & 3) * 256;
      if (i == 6 && wn == 1 && !(nt & 1)) return;
#pragma unroll
      for (int m = 0; m < 2; ++m)
        __builtin_amdgcn_raw_buffer_store_b32(pk_bf16(geglu(acc[i][m][0], acc[i][m][1]), geglu(acc[i][m][2], acc[i][m][3])), rsrc,
                                              aoff + m * 1024, so, 16);
    };
    static_for<7>([&](auto I_) {
      constexpr int i = decltype(I_)::value;
      if constexpr (i + 1 < 7) {
#pragma unroll
        for (int t = 0; t < G1_TAIL; ++t) wf[(i + 1) & 1][t] = *(const u4*)(rdB + ((G1_MAIN + t) % G1_NB) * G1_STG + (i + 1) * 1024);
      }
#pragma unroll
      for (int t = 0; t < G1_TAIL; ++t)
#pragma unroll
        for (int m = 0; m < 2; ++m) acc[i][m] = mfma_b(wf[i & 1][t], af[t][m], acc[i][m]);
      if constexpr (i > 0) {
        epi(i - 1);
#pragma unroll
        for (int t = 0; t < 8; ++t) { SGB(0x8, 1); if (t < 4 && i + 1 < 7) SGB(0x100, 1); SGB(0x2, 4); SGB(0x400, 1); }
      }
      SCHED_FENCE();
      if constexpr (i == EARLY_AT) { wait_vm<2 * (EARLY_AT - 4)>(); arrive(smem, ws, 0, 16 + mt, lz); }
    });
    epi(6);
    ST(4);
    raw_barrier();
    wait_vm<0>();
    arrive(smem, ws, 1, 32 + mt, lz);
    if (w == 0) poll_ge16(ws, 16 + (mt2 >> 1));
  }
  {
    int l = threadIdx.x & 63;
    asm volatile("" : "+v"(l));
    const int g = l >> 4, r16 = l & 15;
    const unsigned loff = l * 16;
    const int kq = w & 3, mh = w >> 2;
    const int mb2 = 4 * mt2 + 2 * mh;
    f4 acc[2][5];
#pragma unroll
    for (int m = 0; m < 2; ++m)
#pragma unroll
      for (int n = 0; n < 5; ++n) acc[m][n] = f4{0.f, 0.f, 0.f, 0.f};
    const char* rdW = smem + kq * 5120 + loff;
    const int abase = (int)OFF_AP + (kq * 128 + mb2) * 1024;
    u4 fa[G2_AD][2], wf[2][5];
    auto loadA = [&](int i, u4 (&a)[2]) {
      const int so = abase + (i < 13 ? i : (kq < 2 ? 13 : 12)) * (512 * 1024);
      a[0] = __builtin_amdgcn_raw_buffer_load_b128(rsrc, loff, so, LDAUX);
      a[1] = __builtin_amdgcn_raw_buffer_load_b128(rsrc, loff + 1024, so, LDAUX);
    };
    raw_barrier();
    ST(5);
    static_for<G2_AD>([&](auto I_) { constexpr int i = decltype(I_)::value; loadA(i, fa[i]); SCHED_FENCE(); });
    u2 hv[3];
#pragma unroll
    for (int n = 0; n < 5; ++n) wf[0][n] = *(const u4*)(rdW + 6 * G1_STG + n * 1024);
    SCHED_FENCE();
    static_for<G2_NS - 1>([&](auto I_) {
      constexpr int i = decltype(I_)::value;
      constexpr int cur = i & 1, nxt = cur ^ 1;
      if constexpr (i == 2 || i == 4 || i == 6 || i == 9) raw_barrier();
#pragma unroll
      for (int n = 0; n < 5; ++n) wf[nxt][n] = *(const u4*)(rdW + ((i + 7) % G1_NB) * G1_STG + n * 1024);
#pragma unroll
      for (int m = 0; m < 2; ++m)
#pragma unroll
        for (int n = 0; n < 5; ++n) acc[m][n] = mfma_b(wf[cur][n], fa[i % G2_AD][m], acc[m][n]);
      if constexpr (i + G2_AD < G2_NS) loadA(i + G2_AD, fa[i % G2_AD]);
      if constexpr (i == 9) {
#pragma unroll
        for (int u = 0; u < 3; ++u) {
          int t = kq + 4 * u; t = t < 10 ? t : kq;
          const int m = t >= 5, n = t - 5 * m;
          hv[u] = __builtin_amdgcn_raw_buffer_load_b64(rsrc, r16 * 32 + g * 8, (int)OFF_HH + ((db0 + n) * 2048 + 16 * (mb2 + m)) * 32, 16);
        }
      }
      SGB(0x100, 5); SGB(0x8, 10); SGB(0x20, 3);
      SCHED_FENCE();
    });
    if (kq < 2) {
#pragma unroll
      for (int m = 0; m < 2; ++m)
#pragma unroll
        for (int n = 0; n < 5; ++n) acc[m][n] = mfma_b(wf[1][n], fa[13 % G2_AD][m], acc[m][n]);
    }
    ST(6);
    raw_barrier();
    f4* red = (f4*)smem + mh * (30 * 64) + l;
    static_for<10>([&](auto T_) {
      constexpr int t = decltype(T_)::value;
      if (kq != (t & 3)) red[(t * 3 + ((kq - t - 1) & 3)) * 64] = acc[t / 5][t % 5];
    });
    raw_barrier();
    auto fin = [&](f4 own, int t, int u) {
      const f4* sp = red + t * 192;
      f4 o = (own + sp[0]) + (sp[64] + sp[128]);
      unsigned o01 = pk_bf16(o[0], o[1]), o23 = pk_bf16(o[2], o[3]);
      const int mp = 16 * (mb2 + t / 5) + r16;
      u16* op = out + (size_t)srow_of(mp >> 3, (mp >> 1) & 3, mp & 1) * DD + 16 * (db0 + t % 5) + 4 * g;
      *(u2*)op = u2{pk_bf16(bf_lo(hv[u][0]) + bf_lo(o01), bf_hi(hv[u][0]) + bf_hi(o01)),
                       pk_bf16(bf_lo(hv[u][1]) + bf_lo(o23), bf_hi(hv[u][1]) + bf_hi(o23))};
    };
    static_for<4>([&](auto K_) {
      constexpr int k = decltype(K_)::value;
      if (kq == k) {
        fin(acc[k / 5][k % 5], k, 0);
        fin(acc[(k + 4) / 5][(k + 4) % 5], k + 4, 1);
        if constexpr (k + 8 < 10) fin(acc[1][(k + 8) % 5], k + 8, 2);
      }
    });
  }
  ST_FLUSH(2);
}

#if defined(__HIP_DEVICE_COMPILE__)
#pragma clang attribute pop
#endif

extern "C" void kernel_launch(void* const* d_in, const int* in_sizes, int n_in, void* d_out, int out_size, void* d_ws,
                              size_t ws_size, hipStream_t stream) {
  const u16* x = (const u16*)d_in[0];
  const u16* wr = (const u16*)d_in[1];
  const u16* wi = (const u16*)d_in[2];
  const u16* wg = (const u16*)d_in[4];
  const u16* wu = (const u16*)d_in[5];
  const u16* wd = (const u16*)d_in[6];
  u16* out = (u16*)d_out;
  char* ws = (char*)d_ws;
  (void)hipFuncSetAttribute((const void*)k3_kernel, hipFuncAttributeMaxDynamicSharedMemorySize, LDS_TOTAL);
  k1_kernel<<<256 + 128 + (WDK == 1 ? 56 : 0), 512, 0, stream>>>(x, wr, wi, wd, ws);
  k2_kernel<<<328 + 160 + (WDK == 2 ? 112 : 0), 256, 0, stream>>>(wg, wu, wd, ws);
  k3_kernel<<<256, 768, LDS_TOTAL, stream>>>(x, out, ws);
}
```

```cpp
#include <hip/hip_runtime.h>
#include <hip/hip_bf16.h>
#include <stdint.h>
#include <utility>

#if defined(__HIP_DEVICE_COMPILE__)
#pragma clang attribute push(__attribute__((target("no-packed-fp32-ops"))), apply_to = function)
#endif

typedef _Float16 h8 __attribute__((ext_vector_type(8)));
typedef _Float16 h2 __attribute__((ext_vector_type(2)));
typedef float f4 __attribute__((ext_vector_type(4)));
typedef float f2 __attribute__((ext_vector_type(2)));
typedef short s8 __attribute__((ext_vector_type(8)));
typedef short s4 __attribute__((ext_vector_type(4)));
typedef unsigned u4 __attribute__((ext_vector_type(4)));
typedef unsigned u4u __attribute__((ext_vector_type(4), aligned(2)));
typedef unsigned u2 __attribute__((ext_vector_type(2)));
typedef unsigned short us2 __attribute__((ext_vector_type(2)));
typedef __bf16 bf2 __attribute__((ext_vector_type(2)));
typedef unsigned short u16;
#define DI __device__ __forceinline__
#define LAS __attribute__((address_space(3)))
#define GAS __attribute__((address_space(1)))

constexpr int DD = 640, FF = 1728;

constexpr size_t OFF_Z1 = 0;
constexpr size_t OFF_Z2 = OFF_Z1 + (size_t)640 * 256 * 16;
constexpr size_t OFF_WT = OFF_Z2 + (size_t)640 * 256 * 16;
constexpr size_t OFF_WM = OFF_WT + (size_t)321 * 512 * 16;
constexpr size_t OFF_HP = OFF_WM + 1024;
constexpr size_t OFF_W1 = OFF_HP + (size_t)20 * 128 * 1024;
constexpr size_t OFF_AP = OFF_W1 + (size_t)20 * 216 * 1024;
constexpr size_t OFF_W2 = OFF_AP + (size_t)54 * 128 * 1024;
constexpr size_t OFF_HH = OFF_W2 + (size_t)54 * 40 * 1024;
constexpr size_t OFF_CNT = OFF_HH + (size_t)40 * 2048 * 32;
constexpr size_t WS_USED = OFF_CNT + 56 * 256;

#define ST_DECL
#define ST(i)
#define ST_FLUSH(kid)
#define SCHED_FENCE() __builtin_amdgcn_sched_barrier(0)
#define NTS(p, v) (*(p) = (v))
#define K2GRID (328 + 160 + 112)
#define WDK 1
#define CODEWARM 1
#define CW_SPLIT 1
#define Z1L 2
#define Z2L 0
#define K4_BAR2 0
#define Z1IDX(q, k) (Z1L == 0 ? (k) * 256 + (q) : Z1L == 1 ? (q) * 640 + (k) : Z1L == 2 ? (((k) >> 2) * 256 + (q)) * 4 + ((k) & 3) : (((k) >> 3) * 256 + (q)) * 8 + ((k) & 7))
#define Z2IDX(q, k) (Z2L == 0 ? (q) * 640 + (k) : Z2L == 1 ? (k) * 256 + (q) : Z2L == 2 ? (((q) >> 2) * 640 + (k)) * 4 + ((q) & 3) : (((q) >> 3) * 640 + (k)) * 8 + ((q) & 7))

template <class Fn, int... I>
DI void static_for_impl(Fn&& f, std::integer_sequence<int, I...>) { (f(std::integral_constant<int, I>{}), ...); }
template <int N, class Fn>
DI void static_for(Fn&& f) { static_for_impl(f, std::make_integer_sequence<int, N>{}); }

DI unsigned pk_bf16(float a, float b) { f2 v = {a, b}; return __builtin_bit_cast(unsigned, __builtin_convertvector(v, bf2)); }
DI unsigned pk_f16(float a, float b) { f2 v = {a, b}; return __builtin_bit_cast(unsigned, __builtin_convertvector(v, h2)); }
DI float bf_lo(unsigned u) { return __builtin_bit_cast(float, u << 16); }
DI float bf_hi(unsigned u) { return __builtin_bit_cast(float, u & 0xffff0000u); }
DI float rbf(float a) { return bf_hi(pk_bf16(0.f, a)); }
DI float asf(unsigned u) { return __builtin_bit_cast(float, u); }
DI unsigned asu(float f) { return __builtin_bit_cast(unsigned, f); }
DI float fsin(float r) { return __builtin_amdgcn_sinf(r); }
DI float fcos(float r) { return __builtin_amdgcn_cosf(r); }
DI f4 mfma_h(u4 a, u4 b, f4 c) { return __builtin_amdgcn_mfma_f32_16x16x32_f16(__builtin_bit_cast(h8, a), __builtin_bit_cast(h8, b), c, 0, 0, 0); }
DI f4 mfma_b(u4 a, u4 b, f4 c) { return __builtin_amdgcn_mfma_f32_16x16x32_bf16(__builtin_bit_cast(s8, a), __builtin_bit_cast(s8, b), c, 0, 0, 0); }
DI u2 tr_read(const char* p) { return __builtin_bit_cast(u2, __builtin_amdgcn_ds_read_tr16_b64_v4i16((LAS s4*)p)); }
DI unsigned perm_lo(unsigned hi, unsigned lo) { return __builtin_amdgcn_perm(hi, lo, 0x05040100u); }
DI unsigned perm_hi(unsigned hi, unsigned lo) { return __builtin_amdgcn_perm(hi, lo, 0x07060302u); }

template <int CTRL> DI unsigned dppu(unsigned v) { return (unsigned)__builtin_amdgcn_update_dpp(0, (int)v, CTRL, 0xf, 0xf, true); }
template <int CTRL> DI float dpp(float v) { return asf(dppu<CTRL>(__builtin_bit_cast(unsigned, v))); }
DI float swap16_sum(float v) {
  unsigned u = __builtin_bit_cast(unsigned, v);
  auto r = __builtin_amdgcn_permlane16_swap(u, u, false, false);
  return asf((unsigned)r[0]) + asf((unsigned)r[1]);
}
DI float swap32_sum(float v) {
  unsigned u = __builtin_bit_cast(unsigned, v);
  auto r = __builtin_amdgcn_permlane32_swap(u, u, false, false);
  return asf((unsigned)r[0]) + asf((unsigned)r[1]);
}
DI float half_sum(float v) {
  v += dpp<0x121>(v); v += dpp<0x122>(v); v += dpp<0x124>(v); v += dpp<0x128>(v);
  return swap16_sum(v);
}
DI float parity_sum(float v) {
  v += dpp<0x122>(v); v += dpp<0x124>(v); v += dpp<0x128>(v);
  v = swap16_sum(v);
  return swap32_sum(v);
}
DI unsigned umax(unsigned a, unsigned b) { return a > b ? a : b; }
DI unsigned wave_umax(unsigned v) {
  v = umax(v, dppu<0x121>(v)); v = umax(v, dppu<0x122>(v)); v = umax(v, dppu<0x124>(v)); v = umax(v, dppu<0x128>(v));
  auto r = __builtin_amdgcn_permlane16_swap(v, v, false, false);
  v = umax((unsigned)r[0], (unsigned)r[1]);
  auto r2 = __builtin_amdgcn_permlane32_swap(v, v, false, false);
  return umax((unsigned)r2[0], (unsigned)r2[1]);
}
DI void raw_barrier() {
  asm volatile("s_waitcnt lgkmcnt(0)" ::: "memory");
  __builtin_amdgcn_s_barrier();
  asm volatile("" ::: "memory");
}
DI int srow_of(int q, int i, int j) { return 32 * (4 * (q >> 4) + i) + (q & 15) + 16 * j; }
DI int blk_of_pos(int pos) {
  if (pos < 128) return ((27 * (pos >> 3)) >> 1) + 7 * ((pos >> 2) & 1) + (pos & 3);
  const int r = pos - 128, pr = r / 11, o = r - 11 * pr;
  const int nt = 2 * pr + (o >= 5), oo = o >= 5 ? o - 5 : o;
  return ((27 * nt) >> 1) + (oo >= 3 ? 8 + oo : 4 + oo);
}
DI unsigned pkmax_abs(unsigned m, unsigned v) {
  us2 a = __builtin_bit_cast(us2, m), b = __builtin_bit_cast(us2, v & 0x7fff7fffu);
  return __builtin_bit_cast(unsigned, __builtin_elementwise_max(a, b));
}

struct RowC { u4 t[9]; };
template <int SGN> DI void rc_F(char* ct, int l, int cb) {
  const float gf = (float)(l >> 4), cf = (float)(16 * cb + (l & 15));
  const float c32 = cf * (SGN / 32.f), a0 = gf * 8.f * c32;
  u4 fr, fi;
#pragma unroll
  for (int jj = 0; jj < 4; ++jj) {
    float r0 = (float)(2 * jj) * c32 + a0, r1 = (float)(2 * jj + 1) * c32 + a0;
    fr[jj] = pk_f16(fcos(r0), fcos(r1));
    fi[jj] = pk_f16(fsin(r0), fsin(r1));
  }
  *(u4*)(ct) = fr; *(u4*)(ct + 1024) = fi;
}
template <int SGN, int OB> DI void rc_G(char* ct, int l) {
  const int r16 = l & 15;
  const float gf = (float)(l >> 4), po4 = (float)(r16 & 1) * 0.25f;
  const float e20 = (float)((r16 >> 1) + 8 * OB) * (-SGN / 20.f), g0 = gf * e20 + po4;
  u4 v;
#pragma unroll
  for (int blk = 0; blk < 4; ++blk) { float r = (float)(4 * blk) * e20 + g0; v[blk] = pk_f16(fcos(r), fsin(r)); }
  *(u4*)(ct + (2 + OB) * 1024) = v;
  float r = 16.f * e20 + g0;
  *(unsigned*)(ct + 5 * 1024 + OB * 4) = pk_f16(fcos(r), fsin(r));
}
template <int SGN, int BLK> DI void rc_T(char* ct, int l, int cb, float tws) {
  const float r = (float)(((l >> 4) + 4 * BLK) * (16 * cb + (l & 15))) * (SGN / 640.f);
  const float tc = fcos(r) * tws, ts = fsin(r) * tws;
  if (BLK < 4) { *(float*)(ct + 6 * 1024 + BLK * 4) = tc; *(float*)(ct + 7 * 1024 + BLK * 4) = ts; }
  else { *(float*)(ct + 5 * 1024 + 12) = tc; *(float*)(ct + 8 * 1024 - l * 12) = ts; }
}
template <int SGN> DI void rc_part(char* ct, int l, int cb, float tws, int part) {
  if (part == 0) rc_F<SGN>(ct, l, cb);
  else if (part == 1) { rc_G<SGN, 0>(ct, l); rc_T<SGN, 0>(ct, l, cb, tws); rc_T<SGN, 1>(ct, l, cb, tws); }
  else if (part == 2) { rc_G<SGN, 1>(ct, l); rc_T<SGN, 2>(ct, l, cb, tws); rc_T<SGN, 3>(ct, l, cb, tws); }
  else { rc_G<SGN, 2>(ct, l); rc_T<SGN, 4>(ct, l, cb, tws); }
}
DI void rc_load(RowC& C, const char* ct, int l) {
#pragma unroll
  for (int j = 0; j < 8; ++j) C.t[j] = *(const u4*)(ct + j * 1024);
  C.t[8][0] = *(const unsigned*)(ct + 8 * 1024 - l * 12);
}
DI void row_stage_a(const RowC& C, const char* lds, int l, int ih, bool ii, u4& T0, u4& T1) {
  const int g = l >> 4, q4 = (l & 15) >> 2, p = l & 3;
  const char* base = lds + (8 * g + q4) * 320 + p * 16 + ih * 8;
  u2 lo[5], hi[5];
#pragma unroll
  for (int blk = 0; blk < 5; ++blk) { lo[blk] = tr_read(base + blk * 64); hi[blk] = tr_read(base + blk * 64 + 4 * 320); }
  T1 = u4{0u, 0u, 0u, 0u};
#pragma unroll
  for (int blk = 0; blk < 5; ++blk) {
    u4 a = {lo[blk][0], lo[blk][1], hi[blk][0], hi[blk][1]};
    f4 z = {0.f, 0.f, 0.f, 0.f};
    f4 pr = mfma_h(a, C.t[0], z);
    f4 pi = mfma_h(a, C.t[1], z);
    float pr0, pr1, pi0, pi1;
    if (ii) { pr0 = pr[2]; pr1 = pr[3]; pi0 = pi[2]; pi1 = pi[3]; } else { pr0 = pr[0]; pr1 = pr[1]; pi0 = pi[0]; pi1 = pi[1]; }
    const float tc = asf(blk < 4 ? C.t[6][blk & 3] : C.t[5][3]), ts = asf(blk < 4 ? C.t[7][blk & 3] : C.t[8][0]);
    float tr = pr0 - pi1, ti = pi0 + pr1;
    unsigned v = pk_f16(tr * tc - ti * ts, ti * tc + tr * ts);
    if (blk < 4) T0[blk & 3] = v; else T1[0] = v;
  }
}

template <bool II> DI void k1_dft(const RowC& C, const char* lin, char* lout, int l, int ih, int cb) {
  u4 T0, T1;
  row_stage_a(C, lin, l, ih, II, T0, T1);
  const int g = l >> 4, cc = 16 * cb + (l & 15);
  char* ob_ = lout + (cc + 64 * g) * 16 + (2 * ih + (int)II) * 4;
#pragma unroll
  for (int ob = 0; ob < 3; ++ob) {
    f4 z = {0.f, 0.f, 0.f, 0.f};
    z = mfma_h(C.t[2 + ob], T0, z);
    u4 ga = {C.t[5][ob], 0u, 0u, 0u};
    z = mfma_h(ga, T1, z);
    *(unsigned*)(ob_ + ob * 4096) = pk_f16(z[0], z[1]);
    *(unsigned*)(ob_ + ob * 4096 + 512) = pk_f16(z[2], z[3]);
  }
}
extern "C" __global__ __launch_bounds__(512) void gf_k1(const u16* __restrict__ x, const u16* __restrict__ wr, const u16* __restrict__ wi,
                                                 const u16* __restrict__ wd, char* __restrict__ ws) {
  __shared__ __attribute__((aligned(16))) char smem[32 * 1296];
  const int b = blockIdx.x, tid = threadIdx.x, l = tid & 63;
  const int w = __builtin_amdgcn_readfirstlane(tid >> 6);
  ST_DECL; ST(0);
  if (b >= 384) {
    const int ks = b - 384;
    if (ks >= 54) return;
    u4 a[5];
#pragma unroll
    for (int it = 0; it < 5; ++it) {
      int idx = tid + 512 * it, k = idx / 80, seg = idx - k * 80;
      a[it] = *(const u4*)(wd + (size_t)(8 * blk_of_pos(4 * ks + (k >> 3)) + (k & 7)) * DD + 8 * seg);
    }
    SCHED_FENCE();
#pragma unroll
    for (int it = 0; it < 5; ++it) {
      int idx = tid + 512 * it, k = idx / 80, seg = idx - k * 80;
      *(u4*)(smem + k * 1296 + seg * 16) = a[it];
    }
    raw_barrier();
    u4* W2 = (u4*)(ws + OFF_W2) + (size_t)(ks * 40 + 5 * w) * 64 + l;
    const char* base = smem + (8 * (l >> 4) + ((l & 15) >> 2)) * 1296 + (l & 3) * 8 + w * 160;
#pragma unroll
    for (int t = 0; t < 5; ++t) {
      u2 lo = tr_read(base + t * 32), hi = tr_read(base + t * 32 + 4 * 1296);
      W2[t * 64] = u4{lo[0], lo[1], hi[0], hi[1]};
    }
    ST_FLUSH(0);
    return;
  }
  if (b >= 256) {
    const int task = b - 256, u = task & 7, v = (task >> 3) & 7, hf = task >> 6;
    u4 d[3];
#pragma unroll
    for (int it = 0; it < 3; ++it) {
      int idx = tid + 512 * it; if (idx >= 1280) idx = tid;
      int rho = idx / 20, pc = idx - rho * 20;
      int row = 64 * (4 * v + (rho & 3)) + 8 * u + ((rho >> 2) & 7);
      const u16* src = (rho >= 32 ? wi : wr) + (size_t)row * 321 + 160 * hf + 8 * pc;
      d[it] = *(const u4u*)src;
    }
    unsigned last = 0;
    if (tid < 64) {
      int row = 64 * (4 * v + (tid & 3)) + 8 * u + ((tid >> 2) & 7);
      last = ((tid >= 32 ? wi : wr) + (size_t)row * 321)[320];
    }
    SCHED_FENCE();
    unsigned mx = last & 0x7fffu;
#pragma unroll
    for (int it = 0; it < 3; ++it) {
      int idx = tid + 512 * it; if (idx >= 1280) idx = tid;
      int rho = idx / 20, pc = idx - rho * 20;
      *(u4*)(smem + rho * 336 + pc * 16) = d[it];
#pragma unroll
      for (int e = 0; e < 4; ++e) mx = pkmax_abs(mx, d[it][e]);
    }
    if (tid < 64) *(u16*)(smem + tid * 336 + 320) = (u16)last;
    mx = wave_umax(umax(mx & 0xffffu, mx >> 16));
    if (l == 0) *(unsigned*)(smem + 21504 + w * 4) = mx;
    raw_barrier();
    u4* Wt = (u4*)(ws + OFF_WT);
    const int obase = ((u >> 1) * 2 + (v >> 2)) * 64 + 16 * (v & 3) + 8 * (u & 1);
    const int g = l >> 4, q4 = (l & 15) >> 2, p = l & 3;
#pragma unroll
    for (int rd = 0; rd < 3; ++rd) {
      int job = w + 8 * rd;
      if (job < 20) {
        int cbk = job >> 1, r8 = 4 * (job & 1) + g;
        const char* a = smem + (r8 * 4 + q4) * 336 + (16 * cbk + 4 * p) * 2;
        u2 re = tr_read(a), im = tr_read(a + 32 * 336);
        u4 o = {perm_lo(im[0], re[0]), perm_hi(im[0], re[0]), perm_lo(im[1], re[1]), perm_hi(im[1], re[1])};
        NTS(&Wt[(160 * hf + 16 * cbk + (l & 15)) * 512 + obase + r8], o);
      }
    }
    if (hf == 1 && tid < 8) {
      u4 o;
#pragma unroll
      for (int reg = 0; reg < 4; ++reg)
        o[reg] = (unsigned)*(const u16*)(smem + (tid * 4 + reg) * 336 + 320) | ((unsigned)*(const u16*)(smem + (32 + tid * 4 + reg) * 336 + 320) << 16);
      Wt[320 * 512 + obase + tid] = o;
    }
    if (tid == 0) {
      const unsigned* m = (const unsigned*)(smem + 21504);
      ((unsigned*)(ws + OFF_WM))[task] = umax(umax(umax(m[0], m[1]), umax(m[2], m[3])), umax(umax(m[4], m[5]), umax(m[6], m[7])));
    }
    ST_FLUSH(0);
    return;
  }
  if (b == 0 && tid >= 448 && tid < 504) *(unsigned*)(ws + OFF_CNT + (tid - 448) * 256) = 0u;
  const int q = (b & 7) * 32 + (b >> 3);
  const int ih = (w >> 1) & 1, cb = w & 1;
  char* lin = smem;
  char* lout = smem + 10240;
  char* ct = smem + 22528 + cb * 8448 + l * 16;
  RowC C;
  u4 cw[3] = {};
  if (w < 4) {
    const int j = l >> 5, li = l & 31;
    const u2* xr = (const u2*)(x + (size_t)srow_of(q, w, j) * DD);
    u2 xv[5];
#pragma unroll
    for (int c = 0; c < 5; ++c) xv[c] = xr[li + 32 * c];
    float v[20];
#pragma unroll
    for (int c = 0; c < 5; ++c) {
      v[4 * c] = bf_lo(xv[c][0]); v[4 * c + 1] = bf_hi(xv[c][0]);
      v[4 * c + 2] = bf_lo(xv[c][1]); v[4 * c + 3] = bf_hi(xv[c][1]);
    }
    float sa = 0.f, sb = 0.f;
#pragma unroll
    for (int t = 0; t < 10; ++t) { sa += v[2 * t]; sb += v[2 * t + 1]; }
    const float mean = half_sum(sa + sb) * (1.f / 640.f);
    ST(1);
    sa = 0.f; sb = 0.f;
#pragma unroll
    for (int t = 0; t < 10; ++t) { float d0 = v[2 * t] - mean, d1 = v[2 * t + 1] - mean; sa += d0 * d0; sb += d1 * d1; }
    const float var = half_sum(sa + sb) * (1.f / 640.f);
    const float sd = rbf(__builtin_sqrtf(rbf(rbf(var) + 1e-5f)));
    const float rs = 16.f * __builtin_amdgcn_rcpf(sd);
    const float sh = -rbf(mean) * rs;
    char* wp = lin + (4 * li + 2 * j) * 16 + w * 4;
#pragma unroll
    for (int c = 0; c < 5; ++c) {
      unsigned p01 = pk_f16(v[4 * c] * rs + sh, v[4 * c + 1] * rs + sh);
      unsigned p23 = pk_f16(v[4 * c + 2] * rs + sh, v[4 * c + 3] * rs + sh);
      auto r = __builtin_amdgcn_permlane32_swap(p01, p23, false, false);
      unsigned pa = r[0], pb = r[1];
      *(unsigned*)(wp + c * 2048) = perm_lo(pb, pa);
      *(unsigned*)(wp + c * 2048 + 16) = perm_hi(pb, pa);
    }
    ST(2);
    raw_barrier();
    ST(3);
    rc_load(C, ct, l);
    k1_dft<false>(C, lin, lout, l, ih, cb);
  } else {
    if (b < 8 * CW_SPLIT) {
      unsigned long long pc; unsigned o1, len;
      asm volatile("s_getpc_b64 %0\n.Lgfpc%=:\n\ts_mov_b32 %1, .Lgfpc%=-gf_k1\n\ts_mov_b32 %2, gf_end-gf_k1" : "=s"(pc), "=s"(o1), "=s"(len));
      const char* base = (const char*)(pc - o1);
#pragma unroll
      for (int it = 0; it < 3; ++it) {
        unsigned off = (((b >> 3) * 3 + it) * 256 + (tid - 256)) * 64u;
        off = off + 16u <= len ? off : 0u;
        cw[it] = *(const u4*)(base + off);
      }
    }
    rc_part<-1>(ct, l, cb, 1.f / 256.f, 2 * ih);
    rc_part<-1>(ct, l, cb, 1.f / 256.f, 2 * ih + 1);
    raw_barrier();
    rc_load(C, ct, l);
    k1_dft<true>(C, lin, lout, l, ih, cb);
  }
  raw_barrier();
  ST(4);
  u4* Z1 = (u4*)(ws + OFF_Z1);
  Z1[Z1IDX(q, tid)] = *(const u4*)(lout + tid * 16);
  if (tid < 128) Z1[Z1IDX(q, tid + 512)] = *(const u4*)(lout + (tid + 512) * 16);
  asm volatile("" ::"v"(cw[0]), "v"(cw[1]), "v"(cw[2]));
  ST_FLUSH(0);
}

DI unsigned wmax_exp(u2 m) {
  unsigned eb = wave_umax(umax(m[0], m[1])) >> 7;
  return eb > 251u ? 251u : eb;
}
extern "C" __global__ __launch_bounds__(256) void gf_k2(const u16* __restrict__ wg, const u16* __restrict__ wu, const u16* __restrict__ wd,
                                                 char* __restrict__ ws) {
  __shared__ __attribute__((aligned(16))) char smem[32 * 880];
  const int b = blockIdx.x, tid = threadIdx.x, l = tid & 63;
  const int w = __builtin_amdgcn_readfirstlane(tid >> 6);
  const int g = l >> 4, r16 = l & 15;
  ST_DECL; ST(0);
  if (b >= 488) {
    const int task = b - 488, ks = task >> 1, hf = task & 1;
    if (ks >= 54) return;
    u4 a[5];
#pragma unroll
    for (int it = 0; it < 5; ++it) {
      int idx = tid + 256 * it, k = idx / 40, seg = idx - k * 40;
      a[it] = *(const u4*)(wd + (size_t)(8 * blk_of_pos(4 * ks + (k >> 3)) + (k & 7)) * DD + 320 * hf + 8 * seg);
    }
    SCHED_FENCE();
#pragma unroll
    for (int it = 0; it < 5; ++it) {
      int idx = tid + 256 * it, k = idx / 40, seg = idx - k * 40;
      *(u4*)(smem + k * 656 + seg * 16) = a[it];
    }
    raw_barrier();
    u4* W2 = (u4*)(ws + OFF_W2) + (size_t)(ks * 40 + 20 * hf + 5 * w) * 64 + l;
    const char* base = smem + (8 * g + (r16 >> 2)) * 656 + (l & 3) * 8 + w * 160;
#pragma unroll
    for (int t = 0; t < 5; ++t) {
      u2 lo = tr_read(base + t * 32), hi = tr_read(base + t * 32 + 4 * 656);
      W2[t * 64] = u4{lo[0], lo[1], hi[0], hi[1]};
    }
    ST_FLUSH(1);
    return;
  }
  if (b >= 328) {
    const int fg = b & 7, ks = (b - 328) >> 3;
    u4 a[4], c[4];
#pragma unroll
    for (int it = 0; it < 4; ++it) {
      int idx = tid + 256 * it; idx = idx < 864 ? idx : tid;
      int k = idx / 27, seg = idx - k * 27;
      const size_t off = (size_t)(32 * ks + k) * FF + 216 * fg + 8 * seg;
      a[it] = *(const u4*)(wg + off); c[it] = *(const u4*)(wu + off);
    }
    SCHED_FENCE();
#pragma unroll
    for (int it = 0; it < 4; ++it) {
      int idx = tid + 256 * it; idx = idx < 864 ? idx : tid;
      int k = idx / 27, seg = idx - k * 27;
      u4 o0 = {perm_lo(c[it][0], a[it][0]), perm_hi(c[it][0], a[it][0]), perm_lo(c[it][1], a[it][1]), perm_hi(c[it][1], a[it][1])};
      u4 o1 = {perm_lo(c[it][2], a[it][2]), perm_hi(c[it][2], a[it][2]), perm_lo(c[it][3], a[it][3]), perm_hi(c[it][3], a[it][3])};
      *(u4*)(smem + k * 880 + seg * 32) = o0;
      *(u4*)(smem + k * 880 + seg * 32 + 16) = o1;
    }
    raw_barrier();
    u4* W1 = (u4*)(ws + OFF_W1) + (size_t)(ks * 216 + fg * 27 + w) * 64 + l;
    const char* base = smem + (8 * g + (r16 >> 2)) * 880 + (l & 3) * 8 + w * 32;
#pragma unroll
    for (int t = 0; t < 7; ++t) {
      if (t < 6 || w < 3) {
        u2 lo = tr_read(base + t * 128), hi = tr_read(base + t * 128 + 4 * 880);
        NTS(&W1[t * 256], (u4{lo[0], lo[1], hi[0], hi[1]}));
      }
    }
    ST_FLUSH(1);
    return;
  }
  const int xi = b >> 3;
  int kd = 40 * (b & 7) + xi;
  if (xi == 40) { if ((b & 7) != 7) return; kd = 320; }
  const int kdm = kd == 0 ? 0 : 640 - kd;
  const u4* Z1 = (const u4*)(ws + OFF_Z1);
  const u4 z1 = Z1[Z1IDX(tid, kd)], z2 = Z1[Z1IDX(tid, kdm)];
  u4 wv[2];
  {
    const u4* Wt = (const u4*)(ws + OFF_WT) + kd * 512 + 128 * w + l;
    wv[0] = Wt[0]; wv[1] = Wt[64];
  }
  const u2 wmv = ((const u2*)(ws + OFF_WM))[l];
  SCHED_FENCE();
  u4 M1[4], M2[4], N1[4], N2[4];
  float TC[4][4], TS[4][4];
  {
    const float g4 = (float)(4 * g), k1f = (float)(16 * w + r16);
    const float ka = k1f * (1.f / 64.f), ka0 = g4 * ka;
    const bool odd = r16 & 1;
    const unsigned s1 = (r16 & 2) ? 0x80008000u : 0u;
    const unsigned s1b = (((r16 & 3) + 1) & 2) ? 0x80008000u : 0u;
    const unsigned s2 = odd ? 0x80008000u : 0u;
    const unsigned s3 = ((3 * (r16 & 3)) & 2) ? 0x80008000u : 0u, s3b = ((3 * (r16 & 3) + 1) & 2) ? 0x80008000u : 0u;
#pragma unroll
    for (int jj = 0; jj < 4; ++jj) {
      float r = (float)jj * ka + ka0, c = fcos(r), sn = fsin(r);
      const unsigned a = pk_f16(c, sn), bq = pk_f16(-sn, c);
      const unsigned p = odd ? bq : a, q2 = odd ? a : bq;
      M1[0][jj] = a; M2[0][jj] = bq;
      M1[1][jj] = p ^ s1; M2[1][jj] = q2 ^ s1b;
      M1[2][jj] = a ^ s2; M2[2][jj] = bq ^ s2;
      M1[3][jj] = p ^ s3; M2[3][jj] = q2 ^ s3b;
    }
    const float ro = (float)r16 * (1.f / 32.f), ro0 = g4 * ro;
#pragma unroll
    for (int jj = 0; jj < 4; ++jj) {
      float r = (float)jj * ro + ro0, c = fcos(r), sn = fsin(r);
      const unsigned a = pk_f16(c, sn), bq = pk_f16(-sn, c), sj = (jj & 1) ? 0x80008000u : 0u;
      N1[0][jj] = a; N2[0][jj] = bq;
      N1[1][jj] = a ^ s2; N2[1][jj] = bq ^ s2;
      N1[2][jj] = a ^ sj; N2[2][jj] = bq ^ sj;
      N1[3][jj] = a ^ (s2 ^ sj); N2[3][jj] = bq ^ (s2 ^ sj);
    }
    {
      const float kb = k1f * (1.f / 2048.f);
      const float c1 = fcos(kb), d1 = fsin(kb), c16 = fcos(16.f * kb), d16 = fsin(16.f * kb);
      TC[0][0] = fcos(g4 * kb); TS[0][0] = fsin(g4 * kb);
#pragma unroll
      for (int rg = 1; rg < 4; ++rg) {
        TC[0][rg] = TC[0][rg - 1] * c1 - TS[0][rg - 1] * d1; TS[0][rg] = TS[0][rg - 1] * c1 + TC[0][rg - 1] * d1;
      }
#pragma unroll
      for (int rg = 0; rg < 4; ++rg) { TC[1][rg] = TC[0][rg] * c16 - TS[0][rg] * d16; TS[1][rg] = TS[0][rg] * c16 + TC[0][rg] * d16; }
    }
#pragma unroll
    for (int xx = 0; xx < 2; ++xx) {
      const float nu = (float)(16 * xx + r16) * (1.f / 2048.f), nu0 = ((float)(16 * w) + g4) * nu;
      const float c1 = fcos(nu), d1 = fsin(nu);
      TC[2 + xx][0] = fcos(nu0); TS[2 + xx][0] = fsin(nu0);
#pragma unroll
      for (int rg = 1; rg < 4; ++rg) {
        TC[2 + xx][rg] = TC[2 + xx][rg - 1] * c1 - TS[2 + xx][rg - 1] * d1;
        TS[2 + xx][rg] = TS[2 + xx][rg - 1] * c1 + TC[2 + xx][rg - 1] * d1;
      }
    }
  }
  SCHED_FENCE();
  u4* sa = (u4*)smem;
  u4* ex = (u4*)(smem + 8192);
  {
    u4 A0, A1;
#pragma unroll
    for (int jj = 0; jj < 4; ++jj) {
      h2 p = __builtin_bit_cast(h2, (unsigned)z1[jj]), qq = __builtin_bit_cast(h2, (unsigned)z2[jj]);
      h2 qa = {qq[0], -qq[1]};
      h2 pb = {p[1], -p[0]}, qb = {qq[1], qq[0]};
      A0[jj] = __builtin_bit_cast(unsigned, p + qa);
      A1[jj] = __builtin_bit_cast(unsigned, pb + qb);
    }
    sa[tid] = A0;
    sa[256 + tid] = A1;
  }
  ST(1);
  raw_barrier();
  const f4 zero = {0.f, 0.f, 0.f, 0.f};
  f4 Tr[2] = {zero, zero}, Ti[2] = {zero, zero};
#pragma unroll
  for (int t = 0; t < 4; ++t) {
    u4 A0 = sa[64 * t + l], A1 = sa[256 + 64 * t + l];
    Tr[0] = mfma_h(A0, M1[t], Tr[0]); Tr[1] = mfma_h(A1, M1[t], Tr[1]);
    Ti[0] = mfma_h(A0, M2[t], Ti[0]); Ti[1] = mfma_h(A1, M2[t], Ti[1]);
  }
  ST(2);
  const unsigned eb = wmax_exp(wmv);
  const float fs = asf((252u - eb) << 23) * (1.f / 256.f);
  u4 X[2];
#pragma unroll
  for (int rb = 0; rb < 2; ++rb)
#pragma unroll
    for (int rg = 0; rg < 4; ++rg) {
      float a = Tr[rb][rg], c = Ti[rb][rg], tc = TC[rb][rg], ts = TS[rb][rg];
      X[rb][rg] = pk_f16(a * tc + c * ts, c * tc - a * ts);
    }
  u4 Y[2];
#pragma unroll
  for (int ob = 0; ob < 2; ++ob) {
    f4 xr = mfma_h(N1[2 * ob], X[0], zero); xr = mfma_h(N1[2 * ob + 1], X[1], xr);
    f4 xim = mfma_h(N2[2 * ob], X[0], zero); xim = mfma_h(N2[2 * ob + 1], X[1], xim);
#pragma unroll
    for (int rg = 0; rg < 4; ++rg) {
      float wr_ = bf_lo(wv[ob][rg]) * fs, wi_ = bf_hi(wv[ob][rg]) * fs;
      float yr = xr[rg] * wr_ - xim[rg] * wi_;
      float yi = xr[rg] * wi_ + xim[rg] * wr_;
      Y[ob][rg] = pk_f16(yi, yr);
    }
  }
#pragma unroll
  for (int cb = 0; cb < 2; ++cb) {
    f4 ui = mfma_h(Y[0], N1[2 * cb], zero); ui = mfma_h(Y[1], N1[2 * cb + 1], ui);
    f4 ur = mfma_h(Y[0], N2[2 * cb], zero); ur = mfma_h(Y[1], N2[2 * cb + 1], ur);
    u4 U;
#pragma unroll
    for (int rg = 0; rg < 4; ++rg) {
      float tc = TC[2 + cb][rg], ts = TS[2 + cb][rg];
      U[rg] = pk_f16(ui[rg] * tc + ur[rg] * ts, ur[rg] * tc - ui[rg] * ts);
    }
    ex[(w * 2 + cb) * 64 + l] = U;
  }
  ST(3);
  raw_barrier();
  ST(4);
  f4 Vi[2] = {zero, zero}, Vr[2] = {zero, zero};
#pragma unroll
  for (int t = 0; t < 4; ++t)
#pragma unroll
    for (int cb = 0; cb < 2; ++cb) {
      u4 U = ex[(t * 2 + cb) * 64 + l];
      Vi[cb] = mfma_h(M1[t], U, Vi[cb]);
      Vr[cb] = mfma_h(M2[t], U, Vr[cb]);
    }
  u4* Z2 = (u4*)(ws + OFF_Z2);
  const float sg = (kd == 0 || kd == 320) ? 0.f : 1.f;
  u4 o, om;
#pragma unroll
  for (int rg = 0; rg < 4; ++rg) {
    float ia = Vi[0][rg] * sg, ib = Vi[1][rg] * sg;
    o[rg] = pk_f16(Vr[0][rg] - ib, ia + Vr[1][rg]);
    om[rg] = pk_f16(Vr[0][rg] + ib, Vr[1][rg] - ia);
  }
  Z2[Z2IDX(tid, kd)] = o;
  if (kdm != kd) Z2[Z2IDX(tid, kdm)] = om;
  ST_FLUSH(1);
}

#define GLDS(src, dst) __builtin_amdgcn_global_load_lds((const GAS void*)(src), (LAS void*)(dst), 16, 0, 0)
#define LDAUX 16
#define ACQ 0
#define GLDS_SC1(src, dst) __builtin_amdgcn_global_load_lds((const GAS void*)(src), (LAS void*)(dst), 16, 0, LDAUX)
template <int N> DI void wait_vm() { asm volatile("s_waitcnt vmcnt(%0)" ::"n"(N) : "memory"); }
#define SGB(mask, n) __builtin_amdgcn_sched_group_barrier(mask, n, 0)

DI float geglu(float g, float u) {
  float t = g * g;
  float z = g * (t * (-2.f * 1.4426950408889634f * 0.7978845608028654f * 0.044715f) + (-2.f * 1.4426950408889634f * 0.7978845608028654f));
  float e = __builtin_amdgcn_exp2f(z);
  return (g * u) * __builtin_amdgcn_rcpf(1.f + e);
}
DI void poll_ge(const char* ws, int cnt, unsigned want) {
  const GAS unsigned* p = (const GAS unsigned*)(ws + OFF_CNT + cnt * 256);
  for (int spins = 0; spins < (1 << 22); ++spins) {
    unsigned v = __hip_atomic_load(p, __ATOMIC_RELAXED, __HIP_MEMORY_SCOPE_AGENT);
    if (__builtin_amdgcn_readfirstlane(v) >= want) break;
    __builtin_amdgcn_s_sleep(1);
  }
  asm volatile("" ::: "memory");
}
DI void poll_ge16(const char* ws, int cnt) { poll_ge(ws, cnt, 16u); }
DI void signal(char* ws, int cnt) {
  __hip_atomic_fetch_add((GAS unsigned*)(ws + OFF_CNT + cnt * 256), 1u, __ATOMIC_RELAXED, __HIP_MEMORY_SCOPE_AGENT);
}

constexpr int G1_STG = 22 * 1024, G1_NB = 7, G1_NS = 20, G1_TAIL = 4, G1_MAIN = G1_NS - G1_TAIL;
constexpr int LDS_TOTAL = 160 * 1024, PA_OFF = 6 * G1_STG;
constexpr int G2_NS = 14, G2_AD = 6;
#define EARLY_AT 5

DI void arrive_n(char* smem, char* ws, int word, int cnt, int l, unsigned last) {
  if (l == 0) {
    unsigned old = __hip_atomic_fetch_add((LAS unsigned*)(smem + LDS_TOTAL - 64 + word * 4), 1u, __ATOMIC_RELAXED, __HIP_MEMORY_SCOPE_WORKGROUP);
    if (old == last) signal(ws, cnt);
  }
}
template <int NW> DI void k3_loader(char* smem, char* ws, int mt, int nb0, int mt2, int db0, int l, int j) {
  const unsigned loff = l * 16;
  constexpr int NL = NW + 2;
  const char* sA = ws + OFF_HP + (size_t)(8 * mt + j) * 1024 + loff;
  const char* sB = ws + OFF_W1 + (size_t)(nb0 + j) * 1024 + loff;
  auto issueA = [&](int ks) {
    char* d = smem + (ks % G1_NB) * G1_STG + j * 1024;
    GLDS_SC1(sA + (size_t)ks * (128 * 1024), d);
    GLDS_SC1(sA + (size_t)ks * (128 * 1024) + 4096, d + 4096);
  };
  auto issueW = [&](int ks) {
    char* d = smem + (ks % G1_NB) * G1_STG + 8192 + j * 1024;
#pragma unroll
    for (int t = 0; t < NW; ++t) GLDS(sB + (size_t)ks * (216 * 1024) + t * 4096, d + t * 4096);
  };
  int foff[5];
#pragma unroll
  for (int t = 0; t < 5; ++t) { int f = j + 4 * t, kq = f / 5; foff[t] = (kq * 40 + db0 + f - 5 * kq) * 1024; }
  const char* s2 = ws + OFF_W2 + loff;
  auto issue2 = [&](int i) {
    char* d = smem + ((i + 6) % G1_NB) * G1_STG + j * 1024;
#pragma unroll
    for (int t = 0; t < 5; ++t) GLDS(s2 + (size_t)i * (160 * 1024) + foff[t], d + t * 4096);
  };
  raw_barrier();
  issueW(0); issueW(1); issueW(2); issueW(5);
  raw_barrier(); raw_barrier(); raw_barrier();
  issueW(3); issueW(4); issueW(6);
  poll_ge16(ws, mt);
  u4 pk[33];
  auto fetch = [&](int e) {
    const int i = 7 + e / 5, t = e % 5;
    pk[e] = *(const u4*)(s2 + (size_t)i * (160 * 1024) + (e == 32 && j >= 2 ? foff[1] : foff[t]));
  };
  auto park = [&](int i) {
    char* d = smem + ((i + 6) % G1_NB) * G1_STG + j * 1024 + loff;
#pragma unroll
    for (int t = 0; t < (i < 13 ? 5 : 3); ++t) *(u4*)(d + t * 4096) = pk[(i - 7) * 5 + t];
  };
#pragma unroll
  for (int s = 0; s < 7; ++s) issueA(s);
  wait_vm<12>();
  raw_barrier();
  static_for<G1_MAIN - 1>([&](auto S_) {
    constexpr int s = decltype(S_)::value;
    constexpr int n = s <= 5 ? 2 * (5 - s) + NL * s : (s <= 13 ? NL * ((s + 6 < 19 ? s + 6 : 19) - (s + 1)) : NL * (19 - (s + 1)) + 5 * (s - 13));
    constexpr int lo = s - 6 > 0 ? s - 6 : 0, hi = s - 1 < 10 ? s - 1 : 10;
    wait_vm<n + (hi >= lo ? 3 * (hi - lo + 1) : 0)>();
    raw_barrier();
    if constexpr (s + G1_NB < G1_NS) { issueA(s + G1_NB); issueW(s + G1_NB); }
    else issue2(s - 13);
    if constexpr (s <= 10) { SCHED_FENCE(); fetch(3 * s); fetch(3 * s + 1); fetch(3 * s + 2); SCHED_FENCE(); }
  });
  wait_vm<10>();
  raw_barrier();
  issue2(2);
  raw_barrier();
  issue2(3); issue2(4); issue2(5); issue2(6);
  wait_vm<0>();
  raw_barrier();
  if (j == 0) poll_ge16(ws, 32 + (mt2 >> 1));
  raw_barrier();
  park(7); park(8); park(9);
  raw_barrier();
  park(10); park(11);
  raw_barrier();
  park(12); park(13);
  raw_barrier();
}
DI void arrive(char* smem, char* ws, int word, int cnt, int l) { arrive_n(smem, ws, word, cnt, l, 7u); }

extern "C" __global__ __launch_bounds__(768, 3) void gf_k3(const u16* __restrict__ x, u16* __restrict__ out, char* __restrict__ ws) {
  extern __shared__ __attribute__((aligned(16))) char smem[];
  const int b = blockIdx.x, tid = threadIdx.x, l = tid & 63;
  const int w = __builtin_amdgcn_readfirstlane(tid >> 6);
  const int xcd = b & 7, idx = b >> 3;
  const int mt = 8 * (xcd & 1) + (idx & 7), nt = 4 * (xcd >> 1) + (idx >> 3);
  const int mt2 = 16 * (xcd & 1) + 8 * ((xcd >> 1) & 1) + (idx & 7), nt2 = 4 * (xcd >> 2) + (idx >> 3);
  const int nb0 = (27 * nt) >> 1, mb0 = 8 * mt, db0 = 5 * nt2;
  const unsigned loff = l * 16;
  ST_DECL; ST(0);
  if (w >= 8) {
    if (w < 10) k3_loader<4>(smem, ws, mt, nb0, mt2, db0, l, w - 8); else k3_loader<3>(smem, ws, mt, nb0, mt2, db0, l, w - 8);
    return;
  }
  const int g = l >> 4, r16 = l & 15;
  const auto rsrc = __builtin_amdgcn_make_buffer_rsrc(ws, 0, (int)WS_USED, 0x00020000);
  {
    char* pa = smem + PA_OFF;
    const int q = 16 * mt + nt;
    const int ih = (w >> 1) & 1, cb = w & 1, ii = w >> 2, j = l & 1;
    const u4* Z2 = (const u4*)(ws + OFF_Z2);
    const u4 zv0 = Z2[Z2IDX(q, tid)];
    const u4 zv1 = Z2[Z2IDX(q, 512 + (tid & 127))];
    const int e0 = r16 >> 1;
    const bool v2 = e0 < 4;
    const size_t roff = (size_t)srow_of(q, 2 * ih + ii, j) * DD + 16 * cb + 4 * g + 32 * e0;
    u2 xv[3];
    xv[0] = *(const u2*)(x + roff); xv[1] = *(const u2*)(x + roff + 256); xv[2] = *(const u2*)(x + roff + (v2 ? 512 : 0));
    const u2 wmv = ((const u2*)(ws + OFF_WM))[l];
    SCHED_FENCE();
    char* ct = pa + 10368 + cb * 8448 + l * 16;
    rc_part<1>(ct, l, cb, 1.f / 32.f, w >> 1);
    SCHED_FENCE();
    *(u4*)(pa + tid * 16) = zv0;
    if (tid < 128) *(u4*)(pa + (512 + tid) * 16) = zv1;
    if (tid < 3) *(unsigned*)(smem + LDS_TOTAL - 64 + tid * 4) = 0u;
    raw_barrier();
    RowC C;
    rc_load(C, ct, l);
    u4 T0, T1;
    if (ii) row_stage_a(C, pa, l, ih, true, T0, T1); else row_stage_a(C, pa, l, ih, false, T0, T1);
    const unsigned eb = wmax_exp(wmv);
    const float ysc = asf((eb + 2u) << 23) * 0.05f;
    float h[12];
    float s = 0.f;
    const int mp = 8 * q + 2 * (2 * ih + ii) + j;
    char* hs = smem + 4 * G1_STG - 2048;
    char* ns = smem + 5 * G1_STG - 2048;
    const int r8 = mp & 7;
#pragma unroll
    for (int ob = 0; ob < 3; ++ob) {
      f4 z = {0.f, 0.f, 0.f, 0.f};
      z = mfma_h(T0, C.t[2 + ob], z);
      u4 gb = {C.t[5][ob], 0u, 0u, 0u};
      z = mfma_h(T1, gb, z);
      unsigned y01 = pk_bf16(z[0] * ysc, z[1] * ysc), y23 = pk_bf16(z[2] * ysc, z[3] * ysc);
      unsigned h01 = pk_bf16(bf_lo(xv[ob][0]) + bf_lo(y01), bf_hi(xv[ob][0]) + bf_hi(y01));
      unsigned h23 = pk_bf16(bf_lo(xv[ob][1]) + bf_lo(y23), bf_hi(xv[ob][1]) + bf_hi(y23));
      if (ob == 2) { h01 = v2 ? h01 : 0u; h23 = v2 ? h23 : 0u; }
      if (ob < 2 || v2) *(u2*)(hs + ((2 * e0 + cb + 16 * ob) * 8 + r8) * 32 + g * 8) = u2{h01, h23};
      h[4 * ob] = bf_lo(h01); h[4 * ob + 1] = bf_hi(h01); h[4 * ob + 2] = bf_lo(h23); h[4 * ob + 3] = bf_hi(h23);
      s += (h[4 * ob] + h[4 * ob + 1]) + (h[4 * ob + 2] + h[4 * ob + 3]);
    }
    const float mloc = parity_sum(s) * (1.f / 320.f);
    float s2 = 0.f, s3 = 0.f;
#pragma unroll
    for (int t = 0; t < 8; ++t) { float d = h[t] - mloc; s2 += d * d; }
#pragma unroll
    for (int t = 8; t < 12; ++t) { float d = h[t] - mloc; s3 += d * d; }
    const float m2loc = parity_sum(v2 ? s2 + s3 : s2);
    f2* ex = (f2*)(pa + 10240);
    if (l < 2) ex[w * 2 + l] = f2{mloc, m2loc};
    raw_barrier();
    {
      const int hb = (int)OFF_HH + 8 * q * 32;
      __builtin_amdgcn_raw_buffer_store_b128(*(const u4*)(hs + tid * 16), rsrc, (tid >> 4) * (2048 * 32) + (tid & 15) * 16, hb, 16);
      if (tid < 128) __builtin_amdgcn_raw_buffer_store_b128(*(const u4*)(hs + 8192 + tid * 16), rsrc, (32 + (tid >> 4)) * (2048 * 32) + (tid & 15) * 16, hb, 16);
    }
    const f2 o = ex[(w ^ 1) * 2 + j];
    const float mean = 0.5f * (mloc + o[0]);
    const float dl = mloc - o[0];
    const float var = (m2loc + o[1] + dl * dl * 160.f) * (1.f / 640.f);
    const float mb = rbf(mean);
    const float sd = rbf(__builtin_sqrtf(rbf(rbf(var) + 1e-5f)));
    const float rs = __builtin_amdgcn_rcpf(sd);
#pragma unroll
    for (int ob = 0; ob < 3; ++ob) {
      unsigned d01 = pk_bf16(h[4 * ob] - mb, h[4 * ob + 1] - mb);
      unsigned d23 = pk_bf16(h[4 * ob + 2] - mb, h[4 * ob + 3] - mb);
      u2 st = {pk_bf16(bf_lo(d01) * rs, bf_hi(d01) * rs), pk_bf16(bf_lo(d23) * rs, bf_hi(d23) * rs)};
      if (ob < 2 || v2) *(u2*)(ns + (((8 * ob + e0) * 4 + 2 * cb + (g >> 1)) * 8 + r8) * 16 + (g & 1) * 8) = st;
    }
    raw_barrier();
    {
      const int nbase = (int)OFF_HP + (q >> 1) * 1024 + (q & 1) * 128;
      __builtin_amdgcn_raw_buffer_store_b128(*(const u4*)(ns + tid * 16), rsrc, (tid >> 5) * (128 * 1024) + ((tid >> 3) & 3) * 256 + (tid & 7) * 16, nbase, 16);
      if (tid < 128) __builtin_amdgcn_raw_buffer_store_b128(*(const u4*)(ns + 8192 + tid * 16), rsrc, (16 + (tid >> 5)) * (128 * 1024) + ((tid >> 3) & 3) * 256 + (tid & 7) * 16, nbase, 16);
    }
    ST(1);
    wait_vm<0>();
    raw_barrier();
    if (tid == 0) signal(ws, mt);
    ST(2);
  }
  {
    int lb = threadIdx.x & 63;
    asm volatile("" : "+v"(lb));
    const unsigned loff = lb * 16;
    const int wm = w >> 1, wn = w & 1;
    const char* rdA = smem + (2 * wm) * 1024 + loff;
    const char* rdB = smem + (8 + 7 * wn) * 1024 + loff;
    f4 acc[7][2];
#pragma unroll
    for (int i = 0; i < 7; ++i) { acc[i][0] = f4{0.f, 0.f, 0.f, 0.f}; acc[i][1] = f4{0.f, 0.f, 0.f, 0.f}; }
    {
      u4 af[2][2], wf[2][7];
      raw_barrier();
      ST(3);
#pragma unroll
      for (int i = 0; i < 2; ++i) af[0][i] = *(const u4*)(rdA + i * 1024);
#pragma unroll
      for (int i = 0; i < 7; ++i) wf[0][i] = *(const u4*)(rdB + i * 1024);
      static_for<G1_MAIN>([&](auto S_) {
        constexpr int s = decltype(S_)::value;
        constexpr int cur = s & 1, nxt = cur ^ 1;
        constexpr bool more = s + 1 < G1_MAIN;
        if constexpr (more) {
          raw_barrier();
          constexpr int bo = ((s + 1) % G1_NB) * G1_STG;
#pragma unroll
          for (int i = 0; i < 2; ++i) af[nxt][i] = *(const u4*)(rdA + bo + i * 1024);
#pragma unroll
          for (int i = 0; i < 7; ++i) wf[nxt][i] = *(const u4*)(rdB + bo + i * 1024);
        }
#pragma unroll
        for (int i = 0; i < 7; ++i)
#pragma unroll
          for (int m = 0; m < 2; ++m) acc[i][m] = mfma_b(wf[cur][i], af[cur][m], acc[i][m]);
        if constexpr (more) {
#pragma unroll
          for (int i = 0; i < 9; ++i) { SGB(0x8, 1); SGB(0x100, 1); }
          SGB(0x8, 14);
        }
        SCHED_FENCE();
      });
    }
    raw_barrier();
    int lz = threadIdx.x & 63;
    asm volatile("" : "+v"(lz));
    const int g = lz >> 4, r16 = lz & 15;
    u4 af[G1_TAIL][2], wf[2][G1_TAIL];
#pragma unroll
    for (int t = 0; t < G1_TAIL; ++t) {
      const int bo = ((G1_MAIN + t) % G1_NB) * G1_STG;
      wf[0][t] = *(const u4*)(rdB + bo);
      af[t][0] = *(const u4*)(rdA + bo); af[t][1] = *(const u4*)(rdA + bo + 1024);
    }
    const int aoff = (int)OFF_AP + (mb0 + 2 * wm) * 1024 + r16 * 16 + 4 * g;
    auto epi = [&](int i) {
      const int pos = i < 4 ? 8 * nt + 4 * wn + i : 128 + 11 * (nt >> 1) + 5 * (nt & 1) + 3 * wn + (i - 4);
      const int so = (pos >> 2) * (128 * 1024) + (pos & 3) * 256;
      if (i == 6 && wn == 1 && !(nt & 1)) return;
#pragma unroll
      for (int m = 0; m < 2; ++m)
        __builtin_amdgcn_raw_buffer_store_b32(pk_bf16(geglu(acc[i][m][0], acc[i][m][1]), geglu(acc[i][m][2], acc[i][m][3])), rsrc,
                                              aoff + m * 1024, so, 16);
    };
    static_for<7>([&](auto I_) {
      constexpr int i = decltype(I_)::value;
      if constexpr (i + 1 < 7) {
#pragma unroll
        for (int t = 0; t < G1_TAIL; ++t) wf[(i + 1) & 1][t] = *(const u4*)(rdB + ((G1_MAIN + t) % G1_NB) * G1_STG + (i + 1) * 1024);
      }
#pragma unroll
      for (int t = 0; t < G1_TAIL; ++t)
#pragma unroll
        for (int m = 0; m < 2; ++m) acc[i][m] = mfma_b(wf[i & 1][t], af[t][m], acc[i][m]);
      if constexpr (i > 0) {
        epi(i - 1);
#pragma unroll
        for (int t = 0; t < 8; ++t) { SGB(0x8, 1); if (t < 4 && i + 1 < 7) SGB(0x100, 1); SGB(0x2, 4); SGB(0x400, 1); }
      }
      SCHED_FENCE();
      if constexpr (i == EARLY_AT) { wait_vm<2 * (EARLY_AT - 4)>(); arrive(smem, ws, 0, 16 + mt, lz); }
    });
    epi(6);
    ST(4);
    raw_barrier();
    wait_vm<0>();
    arrive(smem, ws, 1, 32 + mt, lz);
    if (w == 0) poll_ge16(ws, 16 + (mt2 >> 1));
  }
  {
    int l = threadIdx.x & 63;
    asm volatile("" : "+v"(l));
    const int g = l >> 4, r16 = l & 15;
    const unsigned loff = l * 16;
    const int kq = w & 3, mh = w >> 2;
    const int mb2 = 4 * mt2 + 2 * mh;
    f4 acc[2][5];
#pragma unroll
    for (int m = 0; m < 2; ++m)
#pragma unroll
      for (int n = 0; n < 5; ++n) acc[m][n] = f4{0.f, 0.f, 0.f, 0.f};
    const char* rdW = smem + kq * 5120 + loff;
    const int abase = (int)OFF_AP + (kq * 128 + mb2) * 1024;
    u4 fa[G2_AD][2], wf[2][5];
    auto loadA = [&](int i, u4 (&a)[2]) {
      const int so = abase + (i < 13 ? i : (kq < 2 ? 13 : 12)) * (512 * 1024);
      a[0] = __builtin_amdgcn_raw_buffer_load_b128(rsrc, loff, so, LDAUX);
      a[1] = __builtin_amdgcn_raw_buffer_load_b128(rsrc, loff + 1024, so, LDAUX);
    };
    raw_barrier();
    ST(5);
    static_for<G2_AD>([&](auto I_) { constexpr int i = decltype(I_)::value; loadA(i, fa[i]); SCHED_FENCE(); });
    u2 hv[3];
#pragma unroll
    for (int n = 0; n < 5; ++n) wf[0][n] = *(const u4*)(rdW + 6 * G1_STG + n * 1024);
    SCHED_FENCE();
    static_for<G2_NS - 1>([&](auto I_) {
      constexpr int i = decltype(I_)::value;
      constexpr int cur = i & 1, nxt = cur ^ 1;
      if constexpr (i == 2 || i == 4 || i == 6 || i == 9) raw_barrier();
#pragma unroll
      for (int n = 0; n < 5; ++n) wf[nxt][n] = *(const u4*)(rdW + ((i + 7) % G1_NB) * G1_STG + n * 1024);
#pragma unroll
      for (int m = 0; m < 2; ++m)
#pragma unroll
        for (int n = 0; n < 5; ++n) acc[m][n] = mfma_b(wf[cur][n], fa[i % G2_AD][m], acc[m][n]);
      if constexpr (i + G2_AD < G2_NS) loadA(i + G2_AD, fa[i % G2_AD]);
      if constexpr (i == 9) {
#pragma unroll
        for (int u = 0; u < 3; ++u) {
          int t = kq + 4 * u; t = t < 10 ? t : kq;
          const int m = t >= 5, n = t - 5 * m;
          hv[u] = __builtin_amdgcn_raw_buffer_load_b64(rsrc, r16 * 32 + g * 8, (int)OFF_HH + ((db0 + n) * 2048 + 16 * (mb2 + m)) * 32, 16);
        }
      }
      SGB(0x100, 5); SGB(0x8, 10); SGB(0x20, 3);
      SCHED_FENCE();
    });
    if (kq < 2) {
#pragma unroll
      for (int m = 0; m < 2; ++m)
#pragma unroll
        for (int n = 0; n < 5; ++n) acc[m][n] = mfma_b(wf[1][n], fa[13 % G2_AD][m], acc[m][n]);
    }
    ST(6);
    raw_barrier();
    f4* red = (f4*)smem + mh * (30 * 64) + l;
    static_for<10>([&](auto T_) {
      constexpr int t = decltype(T_)::value;
      if (kq != (t & 3)) red[(t * 3 + ((kq - t - 1) & 3)) * 64] = acc[t / 5][t % 5];
    });
    raw_barrier();
    auto fin = [&](f4 own, int t, int u) {
      const f4* sp = red + t * 192;
      f4 o = (own + sp[0]) + (sp[64] + sp[128]);
      unsigned o01 = pk_bf16(o[0], o[1]), o23 = pk_bf16(o[2], o[3]);
      const int mp = 16 * (mb2 + t / 5) + r16;
      u16* op = out + (size_t)srow_of(mp >> 3, (mp >> 1) & 3, mp & 1) * DD + 16 * (db0 + t % 5) + 4 * g;
      *(u2*)op = u2{pk_bf16(bf_lo(hv[u][0]) + bf_lo(o01), bf_hi(hv[u][0]) + bf_hi(o01)),
                       pk_bf16(bf_lo(hv[u][1]) + bf_lo(o23), bf_hi(hv[u][1]) + bf_hi(o23))};
    };
    static_for<4>([&](auto K_) {
      constexpr int k = decltype(K_)::value;
      if (kq == k) {
        fin(acc[k / 5][k % 5], k, 0);
        fin(acc[(k + 4) / 5][(k + 4) % 5], k + 4, 1);
        if constexpr (k + 8 < 10) fin(acc[1][(k + 8) % 5], k + 8, 2);
      }
    });
  }
  ST_FLUSH(2);
}

extern "C" __device__ __attribute__((used, noinline)) void gf_end() {}

#if defined(__HIP_DEVICE_COMPILE__)
#pragma clang attribute pop
#endif

extern "C" void kernel_launch(void* const* d_in, const int* in_sizes, int n_in, void* d_out, int out_size, void* d_ws,
                              size_t ws_size, hipStream_t stream) {
  const u16* x = (const u16*)d_in[0];
  const u16* wr = (const u16*)d_in[1];
  const u16* wi = (const u16*)d_in[2];
  const u16* wg = (const u16*)d_in[4];
  const u16* wu = (const u16*)d_in[5];
  const u16* wd = (const u16*)d_in[6];
  u16* out = (u16*)d_out;
  char* ws = (char*)d_ws;
  (void)hipFuncSetAttribute((const void*)gf_k3, hipFuncAttributeMaxDynamicSharedMemorySize, LDS_TOTAL);
  gf_k1<<<256 + 128 + (WDK == 1 ? 56 : 0), 512, 0, stream>>>(x, wr, wi, wd, ws);
  gf_k2<<<328 + 160 + (WDK == 2 ? 112 : 0), 256, 0, stream>>>(wg, wu, wd, ws);
  gf_k3<<<256, 768, LDS_TOTAL, stream>>>(x, out, ws);
}
```

```cpp
#include <hip/hip_runtime.h>
#include <hip/hip_bf16.h>
#include <stdint.h>
#include <utility>

#if defined(__HIP_DEVICE_COMPILE__)
#pragma clang attribute push(__attribute__((target("no-packed-fp32-ops"))), apply_to = function)
#endif

typedef _Float16 h8 __attribute__((ext_vector_type(8)));
typedef _Float16 h2 __attribute__((ext_vector_type(2)));
typedef float f4 __attribute__((ext_vector_type(4)));
typedef float f2 __attribute__((ext_vector_type(2)));
typedef short s8 __attribute__((ext_vector_type(8)));
typedef short s4 __attribute__((ext_vector_type(4)));
typedef unsigned u4 __attribute__((ext_vector_type(4)));
typedef unsigned u4u __attribute__((ext_vector_type(4), aligned(2)));
typedef unsigned u2 __attribute__((ext_vector_type(2)));
typedef unsigned short us2 __attribute__((ext_vector_type(2)));
typedef __bf16 bf2 __attribute__((ext_vector_type(2)));
typedef unsigned short u16;
#define DI __device__ __forceinline__
#define LAS __attribute__((address_space(3)))
#define GAS __attribute__((address_space(1)))

constexpr int DD = 640, FF = 1728;

constexpr size_t OFF_Z1 = 0;
constexpr size_t OFF_Z2 = OFF_Z1 + (size_t)640 * 256 * 16;
constexpr size_t OFF_WT = OFF_Z2 + (size_t)640 * 256 * 16;
constexpr size_t OFF_WM = OFF_WT + (size_t)321 * 512 * 16;
constexpr size_t OFF_HP = OFF_WM + 1024;
constexpr size_t OFF_W1 = OFF_HP + (size_t)20 * 128 * 1024;
constexpr size_t OFF_AP = OFF_W1 + (size_t)20 * 216 * 1024;
constexpr size_t OFF_W2 = OFF_AP + (size_t)54 * 128 * 1024;
constexpr size_t OFF_HH = OFF_W2 + (size_t)54 * 40 * 1024;
constexpr size_t OFF_CNT = OFF_HH + (size_t)40 * 2048 * 32;
constexpr size_t WS_USED = OFF_CNT + 56 * 256;

#define ST_DECL
#define ST(i)
#define ST_FLUSH(kid)
#define SCHED_FENCE() __builtin_amdgcn_sched_barrier(0)
#define NTS(p, v) (*(p) = (v))
#define K2GRID (328 + 160 + 112)
#define WDK 1
#define CODEWARM 1
#define CW_SPLIT 1
#define PSC 5
#define WT16(bit, base, idx, v) do { if (PSC & (bit)) __builtin_amdgcn_raw_buffer_store_b128((v), __builtin_amdgcn_make_buffer_rsrc(ws, 0, (int)WS_USED, 0x00020000), (int)((const char*)&(base)[idx] - ws), 0, 16); else (base)[idx] = (v); } while (0)
#define K2PRIO 0
#define Z1L 2
#define Z2L 0
#define K4_BAR2 0
#define Z1IDX(q, k) (Z1L == 0 ? (k) * 256 + (q) : Z1L == 1 ? (q) * 640 + (k) : Z1L == 2 ? (((k) >> 2) * 256 + (q)) * 4 + ((k) & 3) : (((k) >> 3) * 256 + (q)) * 8 + ((k) & 7))
#define Z2IDX(q, k) (Z2L == 0 ? (q) * 640 + (k) : Z2L == 1 ? (k) * 256 + (q) : Z2L == 2 ? (((q) >> 2) * 640 + (k)) * 4 + ((q) & 3) : (((q) >> 3) * 640 + (k)) * 8 + ((q) & 7))

template <class Fn, int... I>
DI void static_for_impl(Fn&& f, std::integer_sequence<int, I...>) { (f(std::integral_constant<int, I>{}), ...); }
template <int N, class Fn>
DI void static_for(Fn&& f) { static_for_impl(f, std::make_integer_sequence<int, N>{}); }

DI unsigned pk_bf16(float a, float b) { f2 v = {a, b}; return __builtin_bit_cast(unsigned, __builtin_convertvector(v, bf2)); }
DI unsigned pk_f16(float a, float b) { f2 v = {a, b}; return __builtin_bit_cast(unsigned, __builtin_convertvector(v, h2)); }
DI float bf_lo(unsigned u) { return __builtin_bit_cast(float, u << 16); }
DI float bf_hi(unsigned u) { return __builtin_bit_cast(float, u & 0xffff0000u); }
DI float rbf(float a) { return bf_hi(pk_bf16(0.f, a)); }
DI float asf(unsigned u) { return __builtin_bit_cast(float, u); }
DI unsigned asu(float f) { return __builtin_bit_cast(unsigned, f); }
DI float fsin(float r) { return __builtin_amdgcn_sinf(r); }
DI float fcos(float r) { return __builtin_amdgcn_cosf(r); }
DI f4 mfma_h(u4 a, u4 b, f4 c) { return __builtin_amdgcn_mfma_f32_16x16x32_f16(__builtin_bit_cast(h8, a), __builtin_bit_cast(h8, b), c, 0, 0, 0); }
DI f4 mfma_b(u4 a, u4 b, f4 c) { return __builtin_amdgcn_mfma_f32_16x16x32_bf16(__builtin_bit_cast(s8, a), __builtin_bit_cast(s8, b), c, 0, 0, 0); }
DI u2 tr_read(const char* p) { return __builtin_bit_cast(u2, __builtin_amdgcn_ds_read_tr16_b64_v4i16((LAS s4*)p)); }
DI unsigned perm_lo(unsigned hi, unsigned lo) { return __builtin_amdgcn_perm(hi, lo, 0x05040100u); }
DI unsigned perm_hi(unsigned hi, unsigned lo) { return __builtin_amdgcn_perm(hi, lo, 0x07060302u); }

template <int CTRL> DI unsigned dppu(unsigned v) { return (unsigned)__builtin_amdgcn_update_dpp(0, (int)v, CTRL, 0xf, 0xf, true); }
template <int CTRL> DI float dpp(float v) { return asf(dppu<CTRL>(__builtin_bit_cast(unsigned, v))); }
DI float swap16_sum(float v) {
  unsigned u = __builtin_bit_cast(unsigned, v);
  auto r = __builtin_amdgcn_permlane16_swap(u, u, false, false);
  return asf((unsigned)r[0]) + asf((unsigned)r[1]);
}
DI float swap32_sum(float v) {
  unsigned u = __builtin_bit_cast(unsigned, v);
  auto r = __builtin_amdgcn_permlane32_swap(u, u, false, false);
  return asf((unsigned)r[0]) + asf((unsigned)r[1]);
}
DI float half_sum(float v) {
  v += dpp<0x121>(v); v += dpp<0x122>(v); v += dpp<0x124>(v); v += dpp<0x128>(v);
  return swap16_sum(v);
}
DI float parity_sum(float v) {
  v += dpp<0x122>(v); v += dpp<0x124>(v); v += dpp<0x128>(v);
  v = swap16_sum(v);
  return swap32_sum(v);
}
DI unsigned umax(unsigned a, unsigned b) { return a > b ? a : b; }
DI unsigned wave_umax(unsigned v) {
  v = umax(v, dppu<0x121>(v)); v = umax(v, dppu<0x122>(v)); v = umax(v, dppu<0x124>(v)); v = umax(v, dppu<0x128>(v));
  auto r = __builtin_amdgcn_permlane16_swap(v, v, false, false);
  v = umax((unsigned)r[0], (unsigned)r[1]);
  auto r2 = __builtin_amdgcn_permlane32_swap(v, v, false, false);
  return umax((unsigned)r2[0], (unsigned)r2[1]);
}
DI void raw_barrier() {
  asm volatile("s_waitcnt lgkmcnt(0)" ::: "memory");
  __builtin_amdgcn_s_barrier();
  asm volatile("" ::: "memory");
}
DI int srow_of(int q, int i, int j) { return 32 * (4 * (q >> 4) + i) + (q & 15) + 16 * j; }
DI int blk_of_pos(int pos) {
  if (pos < 128) return ((27 * (pos >> 3)) >> 1) + 7 * ((pos >> 2) & 1) + (pos & 3);
  const int r = pos - 128, pr = r / 11, o = r - 11 * pr;
  const int nt = 2 * pr + (o >= 5), oo = o >= 5 ? o - 5 : o;
  return ((27 * nt) >> 1) + (oo >= 3 ? 8 + oo : 4 + oo);
}
DI unsigned pkmax_abs(unsigned m, unsigned v) {
  us2 a = __builtin_bit_cast(us2, m), b = __builtin_bit_cast(us2, v & 0x7fff7fffu);
  return __builtin_bit_cast(unsigned, __builtin_elementwise_max(a, b));
}

struct RowC { u4 t[9]; };
template <int SGN> DI void rc_F(char* ct, int l, int cb) {
  const float gf = (float)(l >> 4), cf = (float)(16 * cb + (l & 15));
  const float c32 = cf * (SGN / 32.f), a0 = gf * 8.f * c32;
  u4 fr, fi;
#pragma unroll
  for (int jj = 0; jj < 4; ++jj) {
    float r0 = (float)(2 * jj) * c32 + a0, r1 = (float)(2 * jj + 1) * c32 + a0;
    fr[jj] = pk_f16(fcos(r0), fcos(r1));
    fi[jj] = pk_f16(fsin(r0), fsin(r1));
  }
  *(u4*)(ct) = fr; *(u4*)(ct + 1024) = fi;
}
template <int SGN, int OB> DI void rc_G(char* ct, int l) {
  const int r16 = l & 15;
  const float gf = (float)(l >> 4), po4 = (float)(r16 & 1) * 0.25f;
  const float e20 = (float)((r16 >> 1) + 8 * OB) * (-SGN / 20.f), g0 = gf * e20 + po4;
  u4 v;
#pragma unroll
  for (int blk = 0; blk < 4; ++blk) { float r = (float)(4 * blk) * e20 + g0; v[blk] = pk_f16(fcos(r), fsin(r)); }
  *(u4*)(ct + (2 + OB) * 1024) = v;
  float r = 16.f * e20 + g0;
  *(unsigned*)(ct + 5 * 1024 + OB * 4) = pk_f16(fcos(r), fsin(r));
}
template <int SGN, int BLK> DI void rc_T(char* ct, int l, int cb, float tws) {
  const float r = (float)(((l >> 4) + 4 * BLK) * (16 * cb + (l & 15))) * (SGN / 640.f);
  const float tc = fcos(r) * tws, ts = fsin(r) * tws;
  if (BLK < 4) { *(float*)(ct + 6 * 1024 + BLK * 4) = tc; *(float*)(ct + 7 * 1024 + BLK * 4) = ts; }
  else { *(float*)(ct + 5 * 1024 + 12) = tc; *(float*)(ct + 8 * 1024 - l * 12) = ts; }
}
template <int SGN> DI void rc_part(char* ct, int l, int cb, float tws, int part) {
  if (part == 0) rc_F<SGN>(ct, l, cb);
  else if (part == 1) { rc_G<SGN, 0>(ct, l); rc_T<SGN, 0>(ct, l, cb, tws); rc_T<SGN, 1>(ct, l, cb, tws); }
  else if (part == 2) { rc_G<SGN, 1>(ct, l); rc_T<SGN, 2>(ct, l, cb, tws); rc_T<SGN, 3>(ct, l, cb, tws); }
  else { rc_G<SGN, 2>(ct, l); rc_T<SGN, 4>(ct, l, cb, tws); }
}
DI void rc_load(RowC& C, const char* ct, int l) {
#pragma unroll
  for (int j = 0; j < 8; ++j) C.t[j] = *(const u4*)(ct + j * 1024);
  C.t[8][0] = *(const unsigned*)(ct + 8 * 1024 - l * 12);
}
DI void row_stage_a(const RowC& C, const char* lds, int l, int ih, bool ii, u4& T0, u4& T1) {
  const int g = l >> 4, q4 = (l & 15) >> 2, p = l & 3;
  const char* base = lds + (8 * g + q4) * 320 + p * 16 + ih * 8;
  u2 lo[5], hi[5];
#pragma unroll
  for (int blk = 0; blk < 5; ++blk) { lo[blk] = tr_read(base + blk * 64); hi[blk] = tr_read(base + blk * 64 + 4 * 320); }
  T1 = u4{0u, 0u, 0u, 0u};
#pragma unroll
  for (int blk = 0; blk < 5; ++blk) {
    u4 a = {lo[blk][0], lo[blk][1], hi[blk][0], hi[blk][1]};
    f4 z = {0.f, 0.f, 0.f, 0.f};
    f4 pr = mfma_h(a, C.t[0], z);
    f4 pi = mfma_h(a, C.t[1], z);
    float pr0, pr1, pi0, pi1;
    if (ii) { pr0 = pr[2]; pr1 = pr[3]; pi0 = pi[2]; pi1 = pi[3]; } else { pr0 = pr[0]; pr1 = pr[1]; pi0 = pi[0]; pi1 = pi[1]; }
    const float tc = asf(blk < 4 ? C.t[6][blk & 3] : C.t[5][3]), ts = asf(blk < 4 ? C.t[7][blk & 3] : C.t[8][0]);
    float tr = pr0 - pi1, ti = pi0 + pr1;
    unsigned v = pk_f16(tr * tc - ti * ts, ti * tc + tr * ts);
    if (blk < 4) T0[blk & 3] = v; else T1[0] = v;
  }
}

template <bool II> DI void k1_dft(const RowC& C, const char* lin, char* lout, int l, int ih, int cb) {
  u4 T0, T1;
  row_stage_a(C, lin, l, ih, II, T0, T1);
  const int g = l >> 4, cc = 16 * cb + (l & 15);
  char* ob_ = lout + (cc + 64 * g) * 16 + (2 * ih + (int)II) * 4;
#pragma unroll
  for (int ob = 0; ob < 3; ++ob) {
    f4 z = {0.f, 0.f, 0.f, 0.f};
    z = mfma_h(C.t[2 + ob], T0, z);
    u4 ga = {C.t[5][ob], 0u, 0u, 0u};
    z = mfma_h(ga, T1, z);
    *(unsigned*)(ob_ + ob * 4096) = pk_f16(z[0], z[1]);
    *(unsigned*)(ob_ + ob * 4096 + 512) = pk_f16(z[2], z[3]);
  }
}
extern "C" __global__ __launch_bounds__(512) void gf_k1(const u16* __restrict__ x, const u16* __restrict__ wr, const u16* __restrict__ wi,
                                                 const u16* __restrict__ wd, char* __restrict__ ws) {
  __shared__ __attribute__((aligned(16))) char smem[32 * 1296];
  const int b = blockIdx.x, tid = threadIdx.x, l = tid & 63;
  const int w = __builtin_amdgcn_readfirstlane(tid >> 6);
  ST_DECL; ST(0);
  if (b >= 440) {
    unsigned long long pc; unsigned o1, len;
    asm volatile("s_getpc_b64 %0\n.Lgfpc%=:\n\ts_mov_b32 %1, .Lgfpc%=-gf_k1\n\ts_mov_b32 %2, gf_end-gf_k1" : "=s"(pc), "=s"(o1), "=s"(len));
    const char* base = (const char*)(pc - o1);
    u4 cw[2];
#pragma unroll
    for (int it = 0; it < 2; ++it) {
      unsigned off = (it * 512 + tid) * 64u;
      off = off + 16u <= len ? off : 0u;
      cw[it] = *(const GAS u4*)(base + off);
    }
    asm volatile("" ::"v"(cw[0]), "v"(cw[1]));
    return;
  }
  if (b >= 384) {
    const int ks = b - 384;
    if (ks >= 54 || WDK != 1) return;
    u4 a[5];
#pragma unroll
    for (int it = 0; it < 5; ++it) {
      int idx = tid + 512 * it, k = idx / 80, seg = idx - k * 80;
      a[it] = *(const u4*)(wd + (size_t)(8 * blk_of_pos(4 * ks + (k >> 3)) + (k & 7)) * DD + 8 * seg);
    }
    SCHED_FENCE();
#pragma unroll
    for (int it = 0; it < 5; ++it) {
      int idx = tid + 512 * it, k = idx / 80, seg = idx - k * 80;
      *(u4*)(smem + k * 1296 + seg * 16) = a[it];
    }
    raw_barrier();
    u4* W2 = (u4*)(ws + OFF_W2) + (size_t)(ks * 40 + 5 * w) * 64 + l;
    const char* base = smem + (8 * (l >> 4) + ((l & 15) >> 2)) * 1296 + (l & 3) * 8 + w * 160;
#pragma unroll
    for (int t = 0; t < 5; ++t) {
      u2 lo = tr_read(base + t * 32), hi = tr_read(base + t * 32 + 4 * 1296);
      WT16(1, W2, t * 64, (u4{lo[0], lo[1], hi[0], hi[1]}));
    }
    ST_FLUSH(0);
    return;
  }
  if (b >= 256) {
    const int task = b - 256, u = task & 7, v = (task >> 3) & 7, hf = task >> 6;
    u4 d[3];
#pragma unroll
    for (int it = 0; it < 3; ++it) {
      int idx = tid + 512 * it; if (idx >= 1280) idx = tid;
      int rho = idx / 20, pc = idx - rho * 20;
      int row = 64 * (4 * v + (rho & 3)) + 8 * u + ((rho >> 2) & 7);
      const u16* src = (rho >= 32 ? wi : wr) + (size_t)row * 321 + 160 * hf + 8 * pc;
      d[it] = *(const u4u*)src;
    }
    unsigned last = 0;
    if (tid < 64) {
      int row = 64 * (4 * v + (tid & 3)) + 8 * u + ((tid >> 2) & 7);
      last = ((tid >= 32 ? wi : wr) + (size_t)row * 321)[320];
    }
    SCHED_FENCE();
    unsigned mx = last & 0x7fffu;
#pragma unroll
    for (int it = 0; it < 3; ++it) {
      int idx = tid + 512 * it; if (idx >= 1280) idx = tid;
      int rho = idx / 20, pc = idx - rho * 20;
      *(u4*)(smem + rho * 336 + pc * 16) = d[it];
#pragma unroll
      for (int e = 0; e < 4; ++e) mx = pkmax_abs(mx, d[it][e]);
    }
    if (tid < 64) *(u16*)(smem + tid * 336 + 320) = (u16)last;
    mx = wave_umax(umax(mx & 0xffffu, mx >> 16));
    if (l == 0) *(unsigned*)(smem + 21504 + w * 4) = mx;
    raw_barrier();
    u4* Wt = (u4*)(ws + OFF_WT);
    const int obase = ((u >> 1) * 2 + (v >> 2)) * 64 + 16 * (v & 3) + 8 * (u & 1);
    const int g = l >> 4, q4 = (l & 15) >> 2, p = l & 3;
#pragma unroll
    for (int rd = 0; rd < 3; ++rd) {
      int job = w + 8 * rd;
      if (job < 20) {
        int cbk = job >> 1, r8 = 4 * (job & 1) + g;
        const char* a = smem + (r8 * 4 + q4) * 336 + (16 * cbk + 4 * p) * 2;
        u2 re = tr_read(a), im = tr_read(a + 32 * 336);
        u4 o = {perm_lo(im[0], re[0]), perm_hi(im[0], re[0]), perm_lo(im[1], re[1]), perm_hi(im[1], re[1])};
        WT16(2, Wt, (160 * hf + 16 * cbk + (l & 15)) * 512 + obase + r8, o);
      }
    }
    if (hf == 1 && tid < 8) {
      u4 o;
#pragma unroll
      for (int reg = 0; reg < 4; ++reg)
        o[reg] = (unsigned)*(const u16*)(smem + (tid * 4 + reg) * 336 + 320) | ((unsigned)*(const u16*)(smem + (32 + tid * 4 + reg) * 336 + 320) << 16);
      Wt[320 * 512 + obase + tid] = o;
    }
    if (tid == 0) {
      const unsigned* m = (const unsigned*)(smem + 21504);
      ((unsigned*)(ws + OFF_WM))[task] = umax(umax(umax(m[0], m[1]), umax(m[2], m[3])), umax(umax(m[4], m[5]), umax(m[6], m[7])));
    }
    ST_FLUSH(0);
    return;
  }
  if (b == 0 && tid >= 448 && tid < 504) *(unsigned*)(ws + OFF_CNT + (tid - 448) * 256) = 0u;
  const int q = (b & 7) * 32 + (b >> 3);
  const int ih = (w >> 1) & 1, cb = w & 1;
  char* lin = smem;
  char* lout = smem + 10240;
  char* ct = smem + 22528 + cb * 8448 + l * 16;
  RowC C;
  if (w < 4) {
    const int j = l >> 5, li = l & 31;
    const u2* xr = (const u2*)(x + (size_t)srow_of(q, w, j) * DD);
    u2 xv[5];
#pragma unroll
    for (int c = 0; c < 5; ++c) xv[c] = xr[li + 32 * c];
    float v[20];
#pragma unroll
    for (int c = 0; c < 5; ++c) {
      v[4 * c] = bf_lo(xv[c][0]); v[4 * c + 1] = bf_hi(xv[c][0]);
      v[4 * c + 2] = bf_lo(xv[c][1]); v[4 * c + 3] = bf_hi(xv[c][1]);
    }
    float sa = 0.f, sb = 0.f;
#pragma unroll
    for (int t = 0; t < 10; ++t) { sa += v[2 * t]; sb += v[2 * t + 1]; }
    const float mean = half_sum(sa + sb) * (1.f / 640.f);
    ST(1);
    sa = 0.f; sb = 0.f;
#pragma unroll
    for (int t = 0; t < 10; ++t) { float d0 = v[2 * t] - mean, d1 = v[2 * t + 1] - mean; sa += d0 * d0; sb += d1 * d1; }
    const float var = half_sum(sa + sb) * (1.f / 640.f);
    const float sd = rbf(__builtin_sqrtf(rbf(rbf(var) + 1e-5f)));
    const float rs = 16.f * __builtin_amdgcn_rcpf(sd);
    const float sh = -rbf(mean) * rs;
    char* wp = lin + (4 * li + 2 * j) * 16 + w * 4;
#pragma unroll
    for (int c = 0; c < 5; ++c) {
      unsigned p01 = pk_f16(v[4 * c] * rs + sh, v[4 * c + 1] * rs + sh);
      unsigned p23 = pk_f16(v[4 * c + 2] * rs + sh, v[4 * c + 3] * rs + sh);
      auto r = __builtin_amdgcn_permlane32_swap(p01, p23, false, false);
      unsigned pa = r[0], pb = r[1];
      *(unsigned*)(wp + c * 2048) = perm_lo(pb, pa);
      *(unsigned*)(wp + c * 2048 + 16) = perm_hi(pb, pa);
    }
    ST(2);
    raw_barrier();
    ST(3);
    rc_load(C, ct, l);
    k1_dft<false>(C, lin, lout, l, ih, cb);
  } else {
    rc_part<-1>(ct, l, cb, 1.f / 256.f, 2 * ih);
    rc_part<-1>(ct, l, cb, 1.f / 256.f, 2 * ih + 1);
    raw_barrier();
    rc_load(C, ct, l);
    k1_dft<true>(C, lin, lout, l, ih, cb);
  }
  raw_barrier();
  ST(4);
  u4* Z1 = (u4*)(ws + OFF_Z1);
  WT16(8, Z1, Z1IDX(q, tid), *(const u4*)(lout + tid * 16));
  if (tid < 128) WT16(8, Z1, Z1IDX(q, tid + 512), *(const u4*)(lout + (tid + 512) * 16));
  ST_FLUSH(0);
}

DI unsigned wmax_exp(u2 m) {
  unsigned eb = wave_umax(umax(m[0], m[1])) >> 7;
  return eb > 251u ? 251u : eb;
}
extern "C" __global__ __launch_bounds__(256) void gf_k2(const u16* __restrict__ wg, const u16* __restrict__ wu, const u16* __restrict__ wd,
                                                 char* __restrict__ ws) {
  __shared__ __attribute__((aligned(16))) char smem[32 * 880];
  const int b = blockIdx.x, tid = threadIdx.x, l = tid & 63;
  const int w = __builtin_amdgcn_readfirstlane(tid >> 6);
  const int g = l >> 4, r16 = l & 15;
  ST_DECL; ST(0);
  if (b >= 488) {
    const int task = b - 488, ks = task >> 1, hf = task & 1;
    if (ks >= 54) return;
    u4 a[5];
#pragma unroll
    for (int it = 0; it < 5; ++it) {
      int idx = tid + 256 * it, k = idx / 40, seg = idx - k * 40;
      a[it] = *(const u4*)(wd + (size_t)(8 * blk_of_pos(4 * ks + (k >> 3)) + (k & 7)) * DD + 320 * hf + 8 * seg);
    }
    SCHED_FENCE();
#pragma unroll
    for (int it = 0; it < 5; ++it) {
      int idx = tid + 256 * it, k = idx / 40, seg = idx - k * 40;
      *(u4*)(smem + k * 656 + seg * 16) = a[it];
    }
    raw_barrier();
    u4* W2 = (u4*)(ws + OFF_W2) + (size_t)(ks * 40 + 20 * hf + 5 * w) * 64 + l;
    const char* base = smem + (8 * g + (r16 >> 2)) * 656 + (l & 3) * 8 + w * 160;
#pragma unroll
    for (int t = 0; t < 5; ++t) {
      u2 lo = tr_read(base + t * 32), hi = tr_read(base + t * 32 + 4 * 656);
      WT16(1, W2, t * 64, (u4{lo[0], lo[1], hi[0], hi[1]}));
    }
    ST_FLUSH(1);
    return;
  }
  if (b >= 328) {
    const int fg = b & 7, ks = (b - 328) >> 3;
    u4 a[4], c[4];
#pragma unroll
    for (int it = 0; it < 4; ++it) {
      int idx = tid + 256 * it; idx = idx < 864 ? idx : tid;
      int k = idx / 27, seg = idx - k * 27;
      const size_t off = (size_t)(32 * ks + k) * FF + 216 * fg + 8 * seg;
      a[it] = *(const u4*)(wg + off); c[it] = *(const u4*)(wu + off);
    }
    SCHED_FENCE();
#pragma unroll
    for (int it = 0; it < 4; ++it) {
      int idx = tid + 256 * it; idx = idx < 864 ? idx : tid;
      int k = idx / 27, seg = idx - k * 27;
      u4 o0 = {perm_lo(c[it][0], a[it][0]), perm_hi(c[it][0], a[it][0]), perm_lo(c[it][1], a[it][1]), perm_hi(c[it][1], a[it][1])};
      u4 o1 = {perm_lo(c[it][2], a[it][2]), perm_hi(c[it][2], a[it][2]), perm_lo(c[it][3], a[it][3]), perm_hi(c[it][3], a[it][3])};
      *(u4*)(smem + k * 880 + seg * 32) = o0;
      *(u4*)(smem + k * 880 + seg * 32 + 16) = o1;
    }
    raw_barrier();
    u4* W1 = (u4*)(ws + OFF_W1) + (size_t)(ks * 216 + fg * 27 + w) * 64 + l;
    const char* base = smem + (8 * g + (r16 >> 2)) * 880 + (l & 3) * 8 + w * 32;
#pragma unroll
    for (int t = 0; t < 7; ++t) {
      if (t < 6 || w < 3) {
        u2 lo = tr_read(base + t * 128), hi = tr_read(base + t * 128 + 4 * 880);
        WT16(4, W1, t * 256, (u4{lo[0], lo[1], hi[0], hi[1]}));
      }
    }
    ST_FLUSH(1);
    return;
  }
  const int xi = b >> 3;
  int kd = 40 * (b & 7) + xi;
  if (xi == 40) { if ((b & 7) != 7) return; kd = 320; }
  const int kdm = kd == 0 ? 0 : 640 - kd;
  const u4* Z1 = (const u4*)(ws + OFF_Z1);
  const u4 z1 = Z1[Z1IDX(tid, kd)], z2 = Z1[Z1IDX(tid, kdm)];
  u4 wv[2];
  {
    const u4* Wt = (const u4*)(ws + OFF_WT) + kd * 512 + 128 * w + l;
    wv[0] = Wt[0]; wv[1] = Wt[64];
  }
  const u2 wmv = ((const u2*)(ws + OFF_WM))[l];
  SCHED_FENCE();
  u4 M1[4], M2[4], N1[4], N2[4];
  float TC[4][4], TS[4][4];
  {
    const float g4 = (float)(4 * g), k1f = (float)(16 * w + r16);
    const float ka = k1f * (1.f / 64.f), ka0 = g4 * ka;
    const bool odd = r16 & 1;
    const unsigned s1 = (r16 & 2) ? 0x80008000u : 0u;
    const unsigned s1b = (((r16 & 3) + 1) & 2) ? 0x80008000u : 0u;
    const unsigned s2 = odd ? 0x80008000u : 0u;
    const unsigned s3 = ((3 * (r16 & 3)) & 2) ? 0x80008000u : 0u, s3b = ((3 * (r16 & 3) + 1) & 2) ? 0x80008000u : 0u;
#pragma unroll
    for (int jj = 0; jj < 4; ++jj) {
      float r = (float)jj * ka + ka0, c = fcos(r), sn = fsin(r);
      const unsigned a = pk_f16(c, sn), bq = pk_f16(-sn, c);
      const unsigned p = odd ? bq : a, q2 = odd ? a : bq;
      M1[0][jj] = a; M2[0][jj] = bq;
      M1[1][jj] = p ^ s1; M2[1][jj] = q2 ^ s1b;
      M1[2][jj] = a ^ s2; M2[2][jj] = bq ^ s2;
      M1[3][jj] = p ^ s3; M2[3][jj] = q2 ^ s3b;
    }
    const float ro = (float)r16 * (1.f / 32.f), ro0 = g4 * ro;
#pragma unroll
    for (int jj = 0; jj < 4; ++jj) {
      float r = (float)jj * ro + ro0, c = fcos(r), sn = fsin(r);
      const unsigned a = pk_f16(c, sn), bq = pk_f16(-sn, c), sj = (jj & 1) ? 0x80008000u : 0u;
      N1[0][jj] = a; N2[0][jj] = bq;
      N1[1][jj] = a ^ s2; N2[1][jj] = bq ^ s2;
      N1[2][jj] = a ^ sj; N2[2][jj] = bq ^ sj;
      N1[3][jj] = a ^ (s2 ^ sj); N2[3][jj] = bq ^ (s2 ^ sj);
    }
    {
      const float kb = k1f * (1.f / 2048.f);
      const float c1 = fcos(kb), d1 = fsin(kb), c16 = fcos(16.f * kb), d16 = fsin(16.f * kb);
      TC[0][0] = fcos(g4 * kb); TS[0][0] = fsin(g4 * kb);
#pragma unroll
      for (int rg = 1; rg < 4; ++rg) {
        TC[0][rg] = TC[0][rg - 1] * c1 - TS[0][rg - 1] * d1; TS[0][rg] = TS[0][rg - 1] * c1 + TC[0][rg - 1] * d1;
      }
#pragma unroll
      for (int rg = 0; rg < 4; ++rg) { TC[1][rg] = TC[0][rg] * c16 - TS[0][rg] * d16; TS[1][rg] = TS[0][rg] * c16 + TC[0][rg] * d16; }
    }
#pragma unroll
    for (int xx = 0; xx < 2; ++xx) {
      const float nu = (float)(16 * xx + r16) * (1.f / 2048.f), nu0 = ((float)(16 * w) + g4) * nu;
      const float c1 = fcos(nu), d1 = fsin(nu);
      TC[2 + xx][0] = fcos(nu0); TS[2 + xx][0] = fsin(nu0);
#pragma unroll
      for (int rg = 1; rg < 4; ++rg) {
        TC[2 + xx][rg] = TC[2 + xx][rg - 1] * c1 - TS[2 + xx][rg - 1] * d1;
        TS[2 + xx][rg] = TS[2 + xx][rg - 1] * c1 + TC[2 + xx][rg - 1] * d1;
      }
    }
  }
  SCHED_FENCE();
  u4* sa = (u4*)smem;
  u4* ex = (u4*)(smem + 8192);
  {
    u4 A0, A1;
#pragma unroll
    for (int jj = 0; jj < 4; ++jj) {
      h2 p = __builtin_bit_cast(h2, (unsigned)z1[jj]), qq = __builtin_bit_cast(h2, (unsigned)z2[jj]);
      h2 qa = {qq[0], -qq[1]};
      h2 pb = {p[1], -p[0]}, qb = {qq[1], qq[0]};
      A0[jj] = __builtin_bit_cast(unsigned, p + qa);
      A1[jj] = __builtin_bit_cast(unsigned, pb + qb);
    }
    sa[tid] = A0;
    sa[256 + tid] = A1;
  }
  ST(1);
  raw_barrier();
  const f4 zero = {0.f, 0.f, 0.f, 0.f};
  f4 Tr[2] = {zero, zero}, Ti[2] = {zero, zero};
#pragma unroll
  for (int t = 0; t < 4; ++t) {
    u4 A0 = sa[64 * t + l], A1 = sa[256 + 64 * t + l];
    Tr[0] = mfma_h(A0, M1[t], Tr[0]); Tr[1] = mfma_h(A1, M1[t], Tr[1]);
    Ti[0] = mfma_h(A0, M2[t], Ti[0]); Ti[1] = mfma_h(A1, M2[t], Ti[1]);
  }
  ST(2);
  const unsigned eb = wmax_exp(wmv);
  const float fs = asf((252u - eb) << 23) * (1.f / 256.f);
  u4 X[2];
#pragma unroll
  for (int rb = 0; rb < 2; ++rb)
#pragma unroll
    for (int rg = 0; rg < 4; ++rg) {
      float a = Tr[rb][rg], c = Ti[rb][rg], tc = TC[rb][rg], ts = TS[rb][rg];
      X[rb][rg] = pk_f16(a * tc + c * ts, c * tc - a * ts);
    }
  u4 Y[2];
#pragma unroll
  for (int ob = 0; ob < 2; ++ob) {
    f4 xr = mfma_h(N1[2 * ob], X[0], zero); xr = mfma_h(N1[2 * ob + 1], X[1], xr);
    f4 xim = mfma_h(N2[2 * ob], X[0], zero); xim = mfma_h(N2[2 * ob + 1], X[1], xim);
#pragma unroll
    for (int rg = 0; rg < 4; ++rg) {
      float wr_ = bf_lo(wv[ob][rg]) * fs, wi_ = bf_hi(wv[ob][rg]) * fs;
      float yr = xr[rg] * wr_ - xim[rg] * wi_;
      float yi = xr[rg] * wi_ + xim[rg] * wr_;
      Y[ob][rg] = pk_f16(yi, yr);
    }
  }
#pragma unroll
  for (int cb = 0; cb < 2; ++cb) {
    f4 ui = mfma_h(Y[0], N1[2 * cb], zero); ui = mfma_h(Y[1], N1[2 * cb + 1], ui);
    f4 ur = mfma_h(Y[0], N2[2 * cb], zero); ur = mfma_h(Y[1], N2[2 * cb + 1], ur);
    u4 U;
#pragma unroll
    for (int rg = 0; rg < 4; ++rg) {
      float tc = TC[2 + cb][rg], ts = TS[2 + cb][rg];
      U[rg] = pk_f16(ui[rg] * tc + ur[rg] * ts, ur[rg] * tc - ui[rg] * ts);
    }
    ex[(w * 2 + cb) * 64 + l] = U;
  }
  ST(3);
  raw_barrier();
  ST(4);
  f4 Vi[2] = {zero, zero}, Vr[2] = {zero, zero};
#pragma unroll
  for (int t = 0; t < 4; ++t)
#pragma unroll
    for (int cb = 0; cb < 2; ++cb) {
      u4 U = ex[(t * 2 + cb) * 64 + l];
      Vi[cb] = mfma_h(M1[t], U, Vi[cb]);
      Vr[cb] = mfma_h(M2[t], U, Vr[cb]);
    }
  u4* Z2 = (u4*)(ws + OFF_Z2);
  const float sg = (kd == 0 || kd == 320) ? 0.f : 1.f;
  u4 o, om;
#pragma unroll
  for (int rg = 0; rg < 4; ++rg) {
    float ia = Vi[0][rg] * sg, ib = Vi[1][rg] * sg;
    o[rg] = pk_f16(Vr[0][rg] - ib, ia + Vr[1][rg]);
    om[rg] = pk_f16(Vr[0][rg] + ib, Vr[1][rg] - ia);
  }
  WT16(16, Z2, Z2IDX(tid, kd), o);
  if (kdm != kd) WT16(16, Z2, Z2IDX(tid, kdm), om);
  ST_FLUSH(1);
}

#define GLDS(src, dst) __builtin_amdgcn_global_load_lds((const GAS void*)(src), (LAS void*)(dst), 16, 0, 0)
#define LDAUX 16
#define ACQ 0
#define GLDS_SC1(src, dst) __builtin_amdgcn_global_load_lds((const GAS void*)(src), (LAS void*)(dst), 16, 0, LDAUX)
template <int N> DI void wait_vm() { asm volatile("s_waitcnt vmcnt(%0)" ::"n"(N) : "memory"); }
#define SGB(mask, n) __builtin_amdgcn_sched_group_barrier(mask, n, 0)

DI float geglu(float g, float u) {
  float t = g * g;
  float z = g * (t * (-2.f * 1.4426950408889634f * 0.7978845608028654f * 0.044715f) + (-2.f * 1.4426950408889634f * 0.7978845608028654f));
  float e = __builtin_amdgcn_exp2f(z);
  return (g * u) * __builtin_amdgcn_rcpf(1.f + e);
}
DI void poll_ge(const char* ws, int cnt, unsigned want) {
  const GAS unsigned* p = (const GAS unsigned*)(ws + OFF_CNT + cnt * 256);
  for (int spins = 0; spins < (1 << 22); ++spins) {
    unsigned v = __hip_atomic_load(p, __ATOMIC_RELAXED, __HIP_MEMORY_SCOPE_AGENT);
    if (__builtin_amdgcn_readfirstlane(v) >= want) break;
    __builtin_amdgcn_s_sleep(1);
  }
  asm volatile("" ::: "memory");
}
DI void poll_ge16(const char* ws, int cnt) { poll_ge(ws, cnt, 16u); }
DI void signal(char* ws, int cnt) {
  __hip_atomic_fetch_add((GAS unsigned*)(ws + OFF_CNT + cnt * 256), 1u, __ATOMIC_RELAXED, __HIP_MEMORY_SCOPE_AGENT);
}

constexpr int G1_STG = 22 * 1024, G1_NB = 7, G1_NS = 20, G1_TAIL = 4, G1_MAIN = G1_NS - G1_TAIL;
constexpr int LDS_TOTAL = 160 * 1024, PA_OFF = 6 * G1_STG;
constexpr int G2_NS = 14, G2_AD = 6;
#define EARLY_AT 5

DI void arrive_n(char* smem, char* ws, int word, int cnt, int l, unsigned last) {
  if (l == 0) {
    unsigned old = __hip_atomic_fetch_add((LAS unsigned*)(smem + LDS_TOTAL - 64 + word * 4), 1u, __ATOMIC_RELAXED, __HIP_MEMORY_SCOPE_WORKGROUP);
    if (old == last) signal(ws, cnt);
  }
}
template <int NW> DI void k3_loader(char* smem, char* ws, int mt, int nb0, int mt2, int db0, int l, int j) {
  const unsigned loff = l * 16;
  constexpr int NL = NW + 2;
  const char* sA = ws + OFF_HP + (size_t)(8 * mt + j) * 1024 + loff;
  const char* sB = ws + OFF_W1 + (size_t)(nb0 + j) * 1024 + loff;
  auto issueA = [&](int ks) {
    char* d = smem + (ks % G1_NB) * G1_STG + j * 1024;
    GLDS_SC1(sA + (size_t)ks * (128 * 1024), d);
    GLDS_SC1(sA + (size_t)ks * (128 * 1024) + 4096, d + 4096);
  };
  auto issueW = [&](int ks) {
    char* d = smem + (ks % G1_NB) * G1_STG + 8192 + j * 1024;
#pragma unroll
    for (int t = 0; t < NW; ++t) GLDS(sB + (size_t)ks * (216 * 1024) + t * 4096, d + t * 4096);
  };
  int foff[5];
#pragma unroll
  for (int t = 0; t < 5; ++t) { int f = j + 4 * t, kq = f / 5; foff[t] = (kq * 40 + db0 + f - 5 * kq) * 1024; }
  const char* s2 = ws + OFF_W2 + loff;
  auto issue2 = [&](int i) {
    char* d = smem + ((i + 6) % G1_NB) * G1_STG + j * 1024;
#pragma unroll
    for (int t = 0; t < 5; ++t) GLDS(s2 + (size_t)i * (160 * 1024) + foff[t], d + t * 4096);
  };
  raw_barrier();
  issueW(0); issueW(1); issueW(2); issueW(5);
  raw_barrier(); raw_barrier(); raw_barrier();
  issueW(3); issueW(4); issueW(6);
  poll_ge16(ws, mt);
  u4 pk[33];
  auto fetch = [&](int e) {
    const int i = 7 + e / 5, t = e % 5;
    pk[e] = *(const u4*)(s2 + (size_t)i * (160 * 1024) + (e == 32 && j >= 2 ? foff[1] : foff[t]));
  };
  auto park = [&](int i) {
    char* d = smem + ((i + 6) % G1_NB) * G1_STG + j * 1024 + loff;
#pragma unroll
    for (int t = 0; t < (i < 13 ? 5 : 3); ++t) *(u4*)(d + t * 4096) = pk[(i - 7) * 5 + t];
  };
#pragma unroll
  for (int s = 0; s < 7; ++s) issueA(s);
  wait_vm<12>();
  raw_barrier();
  static_for<G1_MAIN - 1>([&](auto S_) {
    constexpr int s = decltype(S_)::value;
    constexpr int n = s <= 5 ? 2 * (5 - s) + NL * s : (s <= 13 ? NL * ((s + 6 < 19 ? s + 6 : 19) - (s + 1)) : NL * (19 - (s + 1)) + 5 * (s - 13));
    constexpr int lo = s - 6 > 0 ? s - 6 : 0, hi = s - 1 < 10 ? s - 1 : 10;
    wait_vm<n + (hi >= lo ? 3 * (hi - lo + 1) : 0)>();
    raw_barrier();
    if constexpr (s + G1_NB < G1_NS) { issueA(s + G1_NB); issueW(s + G1_NB); }
    else issue2(s - 13);
    if constexpr (s <= 10) { SCHED_FENCE(); fetch(3 * s); fetch(3 * s + 1); fetch(3 * s + 2); SCHED_FENCE(); }
  });
  wait_vm<10>();
  raw_barrier();
  issue2(2);
  raw_barrier();
  issue2(3); issue2(4); issue2(5); issue2(6);
  wait_vm<0>();
  raw_barrier();
  if (j == 0) poll_ge16(ws, 32 + (mt2 >> 1));
  raw_barrier();
  park(7); park(8); park(9);
  raw_barrier();
  park(10); park(11);
  raw_barrier();
  park(12); park(13);
  raw_barrier();
}
DI void arrive(char* smem, char* ws, int word, int cnt, int l) { arrive_n(smem, ws, word, cnt, l, 7u); }

extern "C" __global__ __launch_bounds__(768, 3) void gf_k3(const u16* __restrict__ x, u16* __restrict__ out, char* __restrict__ ws) {
  extern __shared__ __attribute__((aligned(16))) char smem[];
  const int b = blockIdx.x, tid = threadIdx.x, l = tid & 63;
  const int w = __builtin_amdgcn_readfirstlane(tid >> 6);
  const int xcd = b & 7, idx = b >> 3;
  const int mt = 8 * (xcd & 1) + (idx & 7), nt = 4 * (xcd >> 1) + (idx >> 3);
  const int mt2 = 16 * (xcd & 1) + 8 * ((xcd >> 1) & 1) + (idx & 7), nt2 = 4 * (xcd >> 2) + (idx >> 3);
  const int nb0 = (27 * nt) >> 1, mb0 = 8 * mt, db0 = 5 * nt2;
  const unsigned loff = l * 16;
  ST_DECL; ST(0);
  if (w >= 8) {
    if (w < 10) k3_loader<4>(smem, ws, mt, nb0, mt2, db0, l, w - 8); else k3_loader<3>(smem, ws, mt, nb0, mt2, db0, l, w - 8);
    return;
  }
  const int g = l >> 4, r16 = l & 15;
  const auto rsrc = __builtin_amdgcn_make_buffer_rsrc(ws, 0, (int)WS_USED, 0x00020000);
  {
    char* pa = smem + PA_OFF;
    const int q = 16 * mt + nt;
    const int ih = (w >> 1) & 1, cb = w & 1, ii = w >> 2, j = l & 1;
    const u4* Z2 = (const u4*)(ws + OFF_Z2);
    const u4 zv0 = Z2[Z2IDX(q, tid)];
    const u4 zv1 = Z2[Z2IDX(q, 512 + (tid & 127))];
    const int e0 = r16 >> 1;
    const bool v2 = e0 < 4;
    const size_t roff = (size_t)srow_of(q, 2 * ih + ii, j) * DD + 16 * cb + 4 * g + 32 * e0;
    u2 xv[3];
    xv[0] = *(const u2*)(x + roff); xv[1] = *(const u2*)(x + roff + 256); xv[2] = *(const u2*)(x + roff + (v2 ? 512 : 0));
    const u2 wmv = ((const u2*)(ws + OFF_WM))[l];
    SCHED_FENCE();
    char* ct = pa + 10368 + cb * 8448 + l * 16;
    rc_part<1>(ct, l, cb, 1.f / 32.f, w >> 1);
    SCHED_FENCE();
    *(u4*)(pa + tid * 16) = zv0;
    if (tid < 128) *(u4*)(pa + (512 + tid) * 16) = zv1;
    if (tid < 3) *(unsigned*)(smem + LDS_TOTAL - 64 + tid * 4) = 0u;
    raw_barrier();
    RowC C;
    rc_load(C, ct, l);
    u4 T0, T1;
    if (ii) row_stage_a(C, pa, l, ih, true, T0, T1); else row_stage_a(C, pa, l, ih, false, T0, T1);
    const unsigned eb = wmax_exp(wmv);
    const float ysc = asf((eb + 2u) << 23) * 0.05f;
    float h[12];
    float s = 0.f;
    const int mp = 8 * q + 2 * (2 * ih + ii) + j;
    char* hs = smem + 4 * G1_STG - 2048;
    char* ns = smem + 5 * G1_STG - 2048;
    const int r8 = mp & 7;
#pragma unroll
    for (int ob = 0; ob < 3; ++ob) {
      f4 z = {0.f, 0.f, 0.f, 0.f};
      z = mfma_h(T0, C.t[2 + ob], z);
      u4 gb = {C.t[5][ob], 0u, 0u, 0u};
      z = mfma_h(T1, gb, z);
      unsigned y01 = pk_bf16(z[0] * ysc, z[1] * ysc), y23 = pk_bf16(z[2] * ysc, z[3] * ysc);
      unsigned h01 = pk_bf16(bf_lo(xv[ob][0]) + bf_lo(y01), bf_hi(xv[ob][0]) + bf_hi(y01));
      unsigned h23 = pk_bf16(bf_lo(xv[ob][1]) + bf_lo(y23), bf_hi(xv[ob][1]) + bf_hi(y23));
      if (ob == 2) { h01 = v2 ? h01 : 0u; h23 = v2 ? h23 : 0u; }
      if (ob < 2 || v2) *(u2*)(hs + ((2 * e0 + cb + 16 * ob) * 8 + r8) * 32 + g * 8) = u2{h01, h23};
      h[4 * ob] = bf_lo(h01); h[4 * ob + 1] = bf_hi(h01); h[4 * ob + 2] = bf_lo(h23); h[4 * ob + 3] = bf_hi(h23);
      s += (h[4 * ob] + h[4 * ob + 1]) + (h[4 * ob + 2] + h[4 * ob + 3]);
    }
    const float mloc = parity_sum(s) * (1.f / 320.f);
    float s2 = 0.f, s3 = 0.f;
#pragma unroll
    for (int t = 0; t < 8; ++t) { float d = h[t] - mloc; s2 += d * d; }
#pragma unroll
    for (int t = 8; t < 12; ++t) { float d = h[t] - mloc; s3 += d * d; }
    const float m2loc = parity_sum(v2 ? s2 + s3 : s2);
    f2* ex = (f2*)(pa + 10240);
    if (l < 2) ex[w * 2 + l] = f2{mloc, m2loc};
    raw_barrier();
    {
      const int hb = (int)OFF_HH + 8 * q * 32;
      __builtin_amdgcn_raw_buffer_store_b128(*(const u4*)(hs + tid * 16), rsrc, (tid >> 4) * (2048 * 32) + (tid & 15) * 16, hb, 16);
      if (tid < 128) __builtin_amdgcn_raw_buffer_store_b128(*(const u4*)(hs + 8192 + tid * 16), rsrc, (32 + (tid >> 4)) * (2048 * 32) + (tid & 15) * 16, hb, 16);
    }
    const f2 o = ex[(w ^ 1) * 2 + j];
    const float mean = 0.5f * (mloc + o[0]);
    const float dl = mloc - o[0];
    const float var = (m2loc + o[1] + dl * dl * 160.f) * (1.f / 640.f);
    const float mb = rbf(mean);
    const float sd = rbf(__builtin_sqrtf(rbf(rbf(var) + 1e-5f)));
    const float rs = __builtin_amdgcn_rcpf(sd);
#pragma unroll
    for (int ob = 0; ob < 3; ++ob) {
      unsigned d01 = pk_bf16(h[4 * ob] - mb, h[4 * ob + 1] - mb);
      unsigned d23 = pk_bf16(h[4 * ob + 2] - mb, h[4 * ob + 3] - mb);
      u2 st = {pk_bf16(bf_lo(d01) * rs, bf_hi(d01) * rs), pk_bf16(bf_lo(d23) * rs, bf_hi(d23) * rs)};
      if (ob < 2 || v2) *(u2*)(ns + (((8 * ob + e0) * 4 + 2 * cb + (g >> 1)) * 8 + r8) * 16 + (g & 1) * 8) = st;
    }
    raw_barrier();
    {
      const int nbase = (int)OFF_HP + (q >> 1) * 1024 + (q & 1) * 128;
      __builtin_amdgcn_raw_buffer_store_b128(*(const u4*)(ns + tid * 16), rsrc, (tid >> 5) * (128 * 1024) + ((tid >> 3) & 3) * 256 + (tid & 7) * 16, nbase, 16);
      if (tid < 128) __builtin_amdgcn_raw_buffer_store_b128(*(const u4*)(ns + 8192 + tid * 16), rsrc, (16 + (tid >> 5)) * (128 * 1024) + ((tid >> 3) & 3) * 256 + (tid & 7) * 16, nbase, 16);
    }
    ST(1);
    wait_vm<0>();
    raw_barrier();
    if (tid == 0) signal(ws, mt);
    ST(2);
  }
  {
    int lb = threadIdx.x & 63;
    asm volatile("" : "+v"(lb));
    const unsigned loff = lb * 16;
    const int wm = w >> 1, wn = w & 1;
    const char* rdA = smem + (2 * wm) * 1024 + loff;
    const char* rdB = smem + (8 + 7 * wn) * 1024 + loff;
    f4 acc[7][2];
#pragma unroll
    for (int i = 0; i < 7; ++i) { acc[i][0] = f4{0.f, 0.f, 0.f, 0.f}; acc[i][1] = f4{0.f, 0.f, 0.f, 0.f}; }
    {
      u4 af[2][2], wf[2][7];
      raw_barrier();
      ST(3);
#pragma unroll
      for (int i = 0; i < 2; ++i) af[0][i] = *(const u4*)(rdA + i * 1024);
#pragma unroll
      for (int i = 0; i < 7; ++i) wf[0][i] = *(const u4*)(rdB + i * 1024);
      static_for<G1_MAIN>([&](auto S_) {
        constexpr int s = decltype(S_)::value;
        constexpr int cur = s & 1, nxt = cur ^ 1;
        constexpr bool more = s + 1 < G1_MAIN;
        if constexpr (more) {
          raw_barrier();
          constexpr int bo = ((s + 1) % G1_NB) * G1_STG;
#pragma unroll
          for (int i = 0; i < 2; ++i) af[nxt][i] = *(const u4*)(rdA + bo + i * 1024);
#pragma unroll
          for (int i = 0; i < 7; ++i) wf[nxt][i] = *(const u4*)(rdB + bo + i * 1024);
        }
#pragma unroll
        for (int i = 0; i < 7; ++i)
#pragma unroll
          for (int m = 0; m < 2; ++m) acc[i][m] = mfma_b(wf[cur][i], af[cur][m], acc[i][m]);
        if constexpr (more) {
#pragma unroll
          for (int i = 0; i < 9; ++i) { SGB(0x8, 1); SGB(0x100, 1); }
          SGB(0x8, 14);
        }
        SCHED_FENCE();
      });
    }
    raw_barrier();
    int lz = threadIdx.x & 63;
    asm volatile("" : "+v"(lz));
    const int g = lz >> 4, r16 = lz & 15;
    u4 af[G1_TAIL][2], wf[2][G1_TAIL];
#pragma unroll
    for (int t = 0; t < G1_TAIL; ++t) {
      const int bo = ((G1_MAIN + t) % G1_NB) * G1_STG;
      wf[0][t] = *(const u4*)(rdB + bo);
      af[t][0] = *(const u4*)(rdA + bo); af[t][1] = *(const u4*)(rdA + bo + 1024);
    }
    const int aoff = (int)OFF_AP + (mb0 + 2 * wm) * 1024 + r16 * 16 + 4 * g;
    auto epi = [&](int i) {
      const int pos = i < 4 ? 8 * nt + 4 * wn + i : 128 + 11 * (nt >> 1) + 5 * (nt & 1) + 3 * wn + (i - 4);
      const int so = (pos >> 2) * (128 * 1024) + (pos & 3) * 256;
      if (i == 6 && wn == 1 && !(nt & 1)) return;
#pragma unroll
      for (int m = 0; m < 2; ++m)
        __builtin_amdgcn_raw_buffer_store_b32(pk_bf16(geglu(acc[i][m][0], acc[i][m][1]), geglu(acc[i][m][2], acc[i][m][3])), rsrc,
                                              aoff + m * 1024, so, 16);
    };
    static_for<7>([&](auto I_) {
      constexpr int i = decltype(I_)::value;
      if constexpr (i + 1 < 7) {
#pragma unroll
        for (int t = 0; t < G1_TAIL; ++t) wf[(i + 1) & 1][t] = *(const u4*)(rdB + ((G1_MAIN + t) % G1_NB) * G1_STG + (i + 1) * 1024);
      }
#pragma unroll
      for (int t = 0; t < G1_TAIL; ++t)
#pragma unroll
        for (int m = 0; m < 2; ++m) acc[i][m] = mfma_b(wf[i & 1][t], af[t][m], acc[i][m]);
      if constexpr (i > 0) {
        epi(i - 1);
#pragma unroll
        for (int t = 0; t < 8; ++t) { SGB(0x8, 1); if (t < 4 && i + 1 < 7) SGB(0x100, 1); SGB(0x2, 4); SGB(0x400, 1); }
      }
      SCHED_FENCE();
      if constexpr (i == EARLY_AT) { wait_vm<2 * (EARLY_AT - 4)>(); arrive(smem, ws, 0, 16 + mt, lz); }
    });
    epi(6);
    ST(4);
    raw_barrier();
    wait_vm<0>();
    arrive(smem, ws, 1, 32 + mt, lz);
    if (w == 0) poll_ge16(ws, 16 + (mt2 >> 1));
  }
  {
    int l = threadIdx.x & 63;
    asm volatile("" : "+v"(l));
    const int g = l >> 4, r16 = l & 15;
    const unsigned loff = l * 16;
    const int kq = w & 3, mh = w >> 2;
    const int mb2 = 4 * mt2 + 2 * mh;
    f4 acc[2][5];
#pragma unroll
    for (int m = 0; m < 2; ++m)
#pragma unroll
      for (int n = 0; n < 5; ++n) acc[m][n] = f4{0.f, 0.f, 0.f, 0.f};
    const char* rdW = smem + kq * 5120 + loff;
    const int abase = (int)OFF_AP + (kq * 128 + mb2) * 1024;
    u4 fa[G2_AD][2], wf[2][5];
    auto loadA = [&](int i, u4 (&a)[2]) {
      const int so = abase + (i < 13 ? i : (kq < 2 ? 13 : 12)) * (512 * 1024);
      a[0] = __builtin_amdgcn_raw_buffer_load_b128(rsrc, loff, so, LDAUX);
      a[1] = __builtin_amdgcn_raw_buffer_load_b128(rsrc, loff + 1024, so, LDAUX);
    };
    raw_barrier();
    ST(5);
    static_for<G2_AD>([&](auto I_) { constexpr int i = decltype(I_)::value; loadA(i, fa[i]); SCHED_FENCE(); });
    u2 hv[3];
#pragma unroll
    for (int n = 0; n < 5; ++n) wf[0][n] = *(const u4*)(rdW + 6 * G1_STG + n * 1024);
    SCHED_FENCE();
    static_for<G2_NS - 1>([&](auto I_) {
      constexpr int i = decltype(I_)::value;
      constexpr int cur = i & 1, nxt = cur ^ 1;
      if constexpr (i == 2 || i == 4 || i == 6 || i == 9) raw_barrier();
#pragma unroll
      for (int n = 0; n < 5; ++n) wf[nxt][n] = *(const u4*)(rdW + ((i + 7) % G1_NB) * G1_STG + n * 1024);
#pragma unroll
      for (int m = 0; m < 2; ++m)
#pragma unroll
        for (int n = 0; n < 5; ++n) acc[m][n] = mfma_b(wf[cur][n], fa[i % G2_AD][m], acc[m][n]);
      if constexpr (i + G2_AD < G2_NS) loadA(i + G2_AD, fa[i % G2_AD]);
      if constexpr (i == 9) {
#pragma unroll
        for (int u = 0; u < 3; ++u) {
          int t = kq + 4 * u; t = t < 10 ? t : kq;
          const int m = t >= 5, n = t - 5 * m;
          hv[u] = __builtin_amdgcn_raw_buffer_load_b64(rsrc, r16 * 32 + g * 8, (int)OFF_HH + ((db0 + n) * 2048 + 16 * (mb2 + m)) * 32, 16);
        }
      }
      SGB(0x100, 5); SGB(0x8, 10); SGB(0x20, 3);
      SCHED_FENCE();
    });
    if (kq < 2) {
#pragma unroll
      for (int m = 0; m < 2; ++m)
#pragma unroll
        for (int n = 0; n < 5; ++n) acc[m][n] = mfma_b(wf[1][n], fa[13 % G2_AD][m], acc[m][n]);
    }
    ST(6);
    raw_barrier();
    f4* red = (f4*)smem + mh * (40 * 64) + l;
#pragma unroll
    for (int t = 0; t < 10; ++t) red[(t * 4 + kq) * 64] = acc[t / 5][t % 5];
    raw_barrier();
#pragma unroll
    for (int u = 0; u < 3; ++u) {
      const int t = kq + 4 * u < 10 ? kq + 4 * u : kq, m = t >= 5, n = t - 5 * m;
      const f4* sp = red + t * 256;
      f4 o = (sp[0] + sp[64]) + (sp[128] + sp[192]);
      unsigned o01 = pk_bf16(o[0], o[1]), o23 = pk_bf16(o[2], o[3]);
      const int mp = 16 * (mb2 + m) + r16;
      u16* op = out + (size_t)srow_of(mp >> 3, (mp >> 1) & 3, mp & 1) * DD + 16 * (db0 + n) + 4 * g;
      *(u2*)op = u2{pk_bf16(bf_lo(hv[u][0]) + bf_lo(o01), bf_hi(hv[u][0]) + bf_hi(o01)),
                    pk_bf16(bf_lo(hv[u][1]) + bf_lo(o23), bf_hi(hv[u][1]) + bf_hi(o23))};
    }
  }
  ST_FLUSH(2);
}

extern "C" __device__ __attribute__((used, noinline)) void gf_end() {}

#if defined(__HIP_DEVICE_COMPILE__)
#pragma clang attribute pop
#endif

extern "C" void kernel_launch(void* const* d_in, const int* in_sizes, int n_in, void* d_out, int out_size, void* d_ws,
                              size_t ws_size, hipStream_t stream) {
  const u16* x = (const u16*)d_in[0];
  const u16* wr = (const u16*)d_in[1];
  const u16* wi = (const u16*)d_in[2];
  const u16* wg = (const u16*)d_in[4];
  const u16* wu = (const u16*)d_in[5];
  const u16* wd = (const u16*)d_in[6];
  u16* out = (u16*)d_out;
  char* ws = (char*)d_ws;
  (void)hipFuncSetAttribute((const void*)gf_k3, hipFuncAttributeMaxDynamicSharedMemorySize, LDS_TOTAL);
  gf_k1<<<448, 512, 0, stream>>>(x, wr, wi, wd, ws);
  gf_k2<<<328 + 160 + (WDK == 2 ? 112 : 0), 256, 0, stream>>>(wg, wu, wd, ws);
  gf_k3<<<256, 768, LDS_TOTAL, stream>>>(x, out, ws);
}
```

```cpp
#include <hip/hip_runtime.h>
#include <hip/hip_bf16.h>
#include <stdint.h>
#include <utility>

#if defined(__HIP_DEVICE_COMPILE__)
#pragma clang attribute push(__attribute__((target("no-packed-fp32-ops"))), apply_to = function)
#endif

typedef _Float16 h8 __attribute__((ext_vector_type(8)));
typedef _Float16 h2 __attribute__((ext_vector_type(2)));
typedef float f4 __attribute__((ext_vector_type(4)));
typedef float f2 __attribute__((ext_vector_type(2)));
typedef short s8 __attribute__((ext_vector_type(8)));
typedef short s4 __attribute__((ext_vector_type(4)));
typedef unsigned u4 __attribute__((ext_vector_type(4)));
typedef unsigned u4u __attribute__((ext_vector_type(4), aligned(2)));
typedef unsigned u2 __attribute__((ext_vector_type(2)));
typedef unsigned short us2 __attribute__((ext_vector_type(2)));
typedef __bf16 bf2 __attribute__((ext_vector_type(2)));
typedef unsigned short u16;
#define DI __device__ __forceinline__
#define LAS __attribute__((address_space(3)))
#define GAS __attribute__((address_space(1)))

constexpr int DD = 640, FF = 1728;

constexpr size_t OFF_Z1 = 0;
constexpr size_t OFF_Z2 = OFF_Z1 + (size_t)640 * 256 * 16;
constexpr size_t OFF_WT = OFF_Z2 + (size_t)640 * 256 * 16;
constexpr size_t OFF_WM = OFF_WT + (size_t)321 * 512 * 16;
constexpr size_t OFF_HP = OFF_WM + 1024;
constexpr size_t OFF_W1 = OFF_HP + (size_t)20 * 128 * 1024;
constexpr size_t OFF_AP = OFF_W1 + (size_t)20 * 216 * 1024;
constexpr size_t OFF_W2 = OFF_AP + (size_t)54 * 128 * 1024;
constexpr size_t OFF_HH = OFF_W2 + (size_t)54 * 40 * 1024;
constexpr size_t OFF_CNT = OFF_HH + (size_t)40 * 2048 * 32;
constexpr size_t WS_USED = OFF_CNT + 56 * 256;

#define ST_DECL
#define ST(i)
#define ST_FLUSH(kid)
#define SCHED_FENCE() __builtin_amdgcn_sched_barrier(0)
#define NTS(p, v) (*(p) = (v))
#define K2GRID (328 + 160 + 112)
#define WDK 1
#define CODEWARM 1
#define CW_SPLIT 1
#define PSC 5
#define WT16(bit, base, idx, v) do { if (PSC & (bit)) __builtin_amdgcn_raw_buffer_store_b128((v), __builtin_amdgcn_make_buffer_rsrc(ws, 0, (int)WS_USED, 0x00020000), (int)((const char*)&(base)[idx] - ws), 0, 16); else (base)[idx] = (v); } while (0)
#define K2PRIO 0
#define FLAGW 1
#define OAUX 2
#define Z1L 2
#define Z2L 0
#define K4_BAR2 0
#define Z1IDX(q, k) (Z1L == 0 ? (k) * 256 + (q) : Z1L == 1 ? (q) * 640 + (k) : Z1L == 2 ? (((k) >> 2) * 256 + (q)) * 4 + ((k) & 3) : (((k) >> 3) * 256 + (q)) * 8 + ((k) & 7))
#define Z2IDX(q, k) (Z2L == 0 ? (q) * 640 + (k) : Z2L == 1 ? (k) * 256 + (q) : Z2L == 2 ? (((q) >> 2) * 640 + (k)) * 4 + ((q) & 3) : (((q) >> 3) * 640 + (k)) * 8 + ((q) & 7))

template <class Fn, int... I>
DI void static_for_impl(Fn&& f, std::integer_sequence<int, I...>) { (f(std::integral_constant<int, I>{}), ...); }
template <int N, class Fn>
DI void static_for(Fn&& f) { static_for_impl(f, std::make_integer_sequence<int, N>{}); }

DI unsigned pk_bf16(float a, float b) { f2 v = {a, b}; return __builtin_bit_cast(unsigned, __builtin_convertvector(v, bf2)); }
DI unsigned pk_f16(float a, float b) { f2 v = {a, b}; return __builtin_bit_cast(unsigned, __builtin_convertvector(v, h2)); }
DI float bf_lo(unsigned u) { return __builtin_bit_cast(float, u << 16); }
DI float bf_hi(unsigned u) { return __builtin_bit_cast(float, u & 0xffff0000u); }
DI float rbf(float a) { return bf_hi(pk_bf16(0.f, a)); }
DI float asf(unsigned u) { return __builtin_bit_cast(float, u); }
DI unsigned asu(float f) { return __builtin_bit_cast(unsigned, f); }
DI float fsin(float r) { return __builtin_amdgcn_sinf(r); }
DI float fcos(float r) { return __builtin_amdgcn_cosf(r); }
DI f4 mfma_h(u4 a, u4 b, f4 c) { return __builtin_amdgcn_mfma_f32_16x16x32_f16(__builtin_bit_cast(h8, a), __builtin_bit_cast(h8, b), c, 0, 0, 0); }
DI f4 mfma_b(u4 a, u4 b, f4 c) { return __builtin_amdgcn_mfma_f32_16x16x32_bf16(__builtin_bit_cast(s8, a), __builtin_bit_cast(s8, b), c, 0, 0, 0); }
DI u2 tr_read(const char* p) { return __builtin_bit_cast(u2, __builtin_amdgcn_ds_read_tr16_b64_v4i16((LAS s4*)p)); }
DI unsigned perm_lo(unsigned hi, unsigned lo) { return __builtin_amdgcn_perm(hi, lo, 0x05040100u); }
DI unsigned perm_hi(unsigned hi, unsigned lo) { return __builtin_amdgcn_perm(hi, lo, 0x07060302u); }

template <int CTRL> DI unsigned dppu(unsigned v) { return (unsigned)__builtin_amdgcn_update_dpp(0, (int)v, CTRL, 0xf, 0xf, true); }
template <int CTRL> DI float dpp(float v) { return asf(dppu<CTRL>(__builtin_bit_cast(unsigned, v))); }
DI float swap16_sum(float v) {
  unsigned u = __builtin_bit_cast(unsigned, v);
  auto r = __builtin_amdgcn_permlane16_swap(u, u, false, false);
  return asf((unsigned)r[0]) + asf((unsigned)r[1]);
}
DI float swap32_sum(float v) {
  unsigned u = __builtin_bit_cast(unsigned, v);
  auto r = __builtin_amdgcn_permlane32_swap(u, u, false, false);
  return asf((unsigned)r[0]) + asf((unsigned)r[1]);
}
DI float half_sum(float v) {
  v += dpp<0x121>(v); v += dpp<0x122>(v); v += dpp<0x124>(v); v += dpp<0x128>(v);
  return swap16_sum(v);
}
DI float parity_sum(float v) {
  v += dpp<0x122>(v); v += dpp<0x124>(v); v += dpp<0x128>(v);
  v = swap16_sum(v);
  return swap32_sum(v);
}
DI unsigned umax(unsigned a, unsigned b) { return a > b ? a : b; }
DI unsigned wave_umax(unsigned v) {
  v = umax(v, dppu<0x121>(v)); v = umax(v, dppu<0x122>(v)); v = umax(v, dppu<0x124>(v)); v = umax(v, dppu<0x128>(v));
  auto r = __builtin_amdgcn_permlane16_swap(v, v, false, false);
  v = umax((unsigned)r[0], (unsigned)r[1]);
  auto r2 = __builtin_amdgcn_permlane32_swap(v, v, false, false);
  return umax((unsigned)r2[0], (unsigned)r2[1]);
}
DI void raw_barrier() {
  asm volatile("s_waitcnt lgkmcnt(0)" ::: "memory");
  __builtin_amdgcn_s_barrier();
  asm volatile("" ::: "memory");
}
DI int srow_of(int q, int i, int j) { return 32 * (4 * (q >> 4) + i) + (q & 15) + 16 * j; }
DI int blk_of_pos(int pos) {
  if (pos < 128) return ((27 * (pos >> 3)) >> 1) + 7 * ((pos >> 2) & 1) + (pos & 3);
  const int r = pos - 128, pr = r / 11, o = r - 11 * pr;
  const int nt = 2 * pr + (o >= 5), oo = o >= 5 ? o - 5 : o;
  return ((27 * nt) >> 1) + (oo >= 3 ? 8 + oo : 4 + oo);
}
DI unsigned pkmax_abs(unsigned m, unsigned v) {
  us2 a = __builtin_bit_cast(us2, m), b = __builtin_bit_cast(us2, v & 0x7fff7fffu);
  return __builtin_bit_cast(unsigned, __builtin_elementwise_max(a, b));
}

struct RowC { u4 t[9]; };
template <int SGN> DI void rc_F(char* ct, int l, int cb) {
  const float gf = (float)(l >> 4), cf = (float)(16 * cb + (l & 15));
  const float c32 = cf * (SGN / 32.f), a0 = gf * 8.f * c32;
  u4 fr, fi;
#pragma unroll
  for (int jj = 0; jj < 4; ++jj) {
    float r0 = (float)(2 * jj) * c32 + a0, r1 = (float)(2 * jj + 1) * c32 + a0;
    fr[jj] = pk_f16(fcos(r0), fcos(r1));
    fi[jj] = pk_f16(fsin(r0), fsin(r1));
  }
  *(u4*)(ct) = fr; *(u4*)(ct + 1024) = fi;
}
template <int SGN, int OB> DI void rc_G(char* ct, int l) {
  const int r16 = l & 15;
  const float gf = (float)(l >> 4), po4 = (float)(r16 & 1) * 0.25f;
  const float e20 = (float)((r16 >> 1) + 8 * OB) * (-SGN / 20.f), g0 = gf * e20 + po4;
  u4 v;
#pragma unroll
  for (int blk = 0; blk < 4; ++blk) { float r = (float)(4 * blk) * e20 + g0; v[blk] = pk_f16(fcos(r), fsin(r)); }
  *(u4*)(ct + (2 + OB) * 1024) = v;
  float r = 16.f * e20 + g0;
  *(unsigned*)(ct + 5 * 1024 + OB * 4) = pk_f16(fcos(r), fsin(r));
}
template <int SGN, int BLK> DI void rc_T(char* ct, int l, int cb, float tws) {
  const float r = (float)(((l >> 4) + 4 * BLK) * (16 * cb + (l & 15))) * (SGN / 640.f);
  const float tc = fcos(r) * tws, ts = fsin(r) * tws;
  if (BLK < 4) { *(float*)(ct + 6 * 1024 + BLK * 4) = tc; *(float*)(ct + 7 * 1024 + BLK * 4) = ts; }
  else { *(float*)(ct + 5 * 1024 + 12) = tc; *(float*)(ct + 8 * 1024 - l * 12) = ts; }
}
template <int SGN> DI void rc_part(char* ct, int l, int cb, float tws, int part) {
  if (part == 0) rc_F<SGN>(ct, l, cb);
  else if (part == 1) { rc_G<SGN, 0>(ct, l); rc_T<SGN, 0>(ct, l, cb, tws); rc_T<SGN, 1>(ct, l, cb, tws); }
  else if (part == 2) { rc_G<SGN, 1>(ct, l); rc_T<SGN, 2>(ct, l, cb, tws); rc_T<SGN, 3>(ct, l, cb, tws); }
  else { rc_G<SGN, 2>(ct, l); rc_T<SGN, 4>(ct, l, cb, tws); }
}
DI void rc_load(RowC& C, const char* ct, int l) {
#pragma unroll
  for (int j = 0; j < 8; ++j) C.t[j] = *(const u4*)(ct + j * 1024);
  C.t[8][0] = *(const unsigned*)(ct + 8 * 1024 - l * 12);
}
DI void row_stage_a(const RowC& C, const char* lds, int l, int ih, bool ii, u4& T0, u4& T1) {
  const int g = l >> 4, q4 = (l & 15) >> 2, p = l & 3;
  const char* base = lds + (8 * g + q4) * 320 + p * 16 + ih * 8;
  u2 lo[5], hi[5];
#pragma unroll
  for (int blk = 0; blk < 5; ++blk) { lo[blk] = tr_read(base + blk * 64); hi[blk] = tr_read(base + blk * 64 + 4 * 320); }
  T1 = u4{0u, 0u, 0u, 0u};
#pragma unroll
  for (int blk = 0; blk < 5; ++blk) {
    u4 a = {lo[blk][0], lo[blk][1], hi[blk][0], hi[blk][1]};
    f4 z = {0.f, 0.f, 0.f, 0.f};
    f4 pr = mfma_h(a, C.t[0], z);
    f4 pi = mfma_h(a, C.t[1], z);
    float pr0, pr1, pi0, pi1;
    if (ii) { pr0 = pr[2]; pr1 = pr[3]; pi0 = pi[2]; pi1 = pi[3]; } else { pr0 = pr[0]; pr1 = pr[1]; pi0 = pi[0]; pi1 = pi[1]; }
    const float tc = asf(blk < 4 ? C.t[6][blk & 3] : C.t[5][3]), ts = asf(blk < 4 ? C.t[7][blk & 3] : C.t[8][0]);
    float tr = pr0 - pi1, ti = pi0 + pr1;
    unsigned v = pk_f16(tr * tc - ti * ts, ti * tc + tr * ts);
    if (blk < 4) T0[blk & 3] = v; else T1[0] = v;
  }
}

template <bool II> DI void k1_dft(const RowC& C, const char* lin, char* lout, int l, int ih, int cb) {
  u4 T0, T1;
  row_stage_a(C, lin, l, ih, II, T0, T1);
  const int g = l >> 4, cc = 16 * cb + (l & 15);
  char* ob_ = lout + (cc + 64 * g) * 16 + (2 * ih + (int)II) * 4;
#pragma unroll
  for (int ob = 0; ob < 3; ++ob) {
    f4 z = {0.f, 0.f, 0.f, 0.f};
    z = mfma_h(C.t[2 + ob], T0, z);
    u4 ga = {C.t[5][ob], 0u, 0u, 0u};
    z = mfma_h(ga, T1, z);
    *(unsigned*)(ob_ + ob * 4096) = pk_f16(z[0], z[1]);
    *(unsigned*)(ob_ + ob * 4096 + 512) = pk_f16(z[2], z[3]);
  }
}
extern "C" __global__ __launch_bounds__(512) void gf_k1(const u16* __restrict__ x, const u16* __restrict__ wr, const u16* __restrict__ wi,
                                                 const u16* __restrict__ wd, char* __restrict__ ws) {
  __shared__ __attribute__((aligned(16))) char smem[32 * 1296];
  const int b = blockIdx.x, tid = threadIdx.x, l = tid & 63;
  const int w = __builtin_amdgcn_readfirstlane(tid >> 6);
  ST_DECL; ST(0);
  if (b >= 440) {
    unsigned long long pc; unsigned o1, len;
    asm volatile("s_getpc_b64 %0\n.Lgfpc%=:\n\ts_mov_b32 %1, .Lgfpc%=-gf_k1\n\ts_mov_b32 %2, gf_end-gf_k1" : "=s"(pc), "=s"(o1), "=s"(len));
    const char* base = (const char*)(pc - o1);
    u4 cw[2];
#pragma unroll
    for (int it = 0; it < 2; ++it) {
      unsigned off = (it * 512 + tid) * 64u;
      off = off + 16u <= len ? off : 0u;
      cw[it] = *(const GAS u4*)(base + off);
    }
    asm volatile("" ::"v"(cw[0]), "v"(cw[1]));
    return;
  }
  if (b >= 384) {
    const int ks = b - 384;
    if (ks >= 54 || WDK != 1) return;
    u4 a[5];
#pragma unroll
    for (int it = 0; it < 5; ++it) {
      int idx = tid + 512 * it, k = idx / 80, seg = idx - k * 80;
      a[it] = *(const u4*)(wd + (size_t)(8 * blk_of_pos(4 * ks + (k >> 3)) + (k & 7)) * DD + 8 * seg);
    }
    SCHED_FENCE();
#pragma unroll
    for (int it = 0; it < 5; ++it) {
      int idx = tid + 512 * it, k = idx / 80, seg = idx - k * 80;
      *(u4*)(smem + k * 1296 + seg * 16) = a[it];
    }
    raw_barrier();
    u4* W2 = (u4*)(ws + OFF_W2) + (size_t)(ks * 40 + 5 * w) * 64 + l;
    const char* base = smem + (8 * (l >> 4) + ((l & 15) >> 2)) * 1296 + (l & 3) * 8 + w * 160;
#pragma unroll
    for (int t = 0; t < 5; ++t) {
      u2 lo = tr_read(base + t * 32), hi = tr_read(base + t * 32 + 4 * 1296);
      WT16(1, W2, t * 64, (u4{lo[0], lo[1], hi[0], hi[1]}));
    }
    ST_FLUSH(0);
    return;
  }
  if (b >= 256) {
    const int task = b - 256, u = task & 7, v = (task >> 3) & 7, hf = task >> 6;
    u4 d[3];
#pragma unroll
    for (int it = 0; it < 3; ++it) {
      int idx = tid + 512 * it; if (idx >= 1280) idx = tid;
      int rho = idx / 20, pc = idx - rho * 20;
      int row = 64 * (4 * v + (rho & 3)) + 8 * u + ((rho >> 2) & 7);
      const u16* src = (rho >= 32 ? wi : wr) + (size_t)row * 321 + 160 * hf + 8 * pc;
      d[it] = *(const u4u*)src;
    }
    unsigned last = 0;
    if (tid < 64) {
      int row = 64 * (4 * v + (tid & 3)) + 8 * u + ((tid >> 2) & 7);
      last = ((tid >= 32 ? wi : wr) + (size_t)row * 321)[320];
    }
    SCHED_FENCE();
    unsigned mx = last & 0x7fffu;
#pragma unroll
    for (int it = 0; it < 3; ++it) {
      int idx = tid + 512 * it; if (idx >= 1280) idx = tid;
      int rho = idx / 20, pc = idx - rho * 20;
      *(u4*)(smem + rho * 336 + pc * 16) = d[it];
#pragma unroll
      for (int e = 0; e < 4; ++e) mx = pkmax_abs(mx, d[it][e]);
    }
    if (tid < 64) *(u16*)(smem + tid * 336 + 320) = (u16)last;
    mx = wave_umax(umax(mx & 0xffffu, mx >> 16));
    if (l == 0) *(unsigned*)(smem + 21504 + w * 4) = mx;
    raw_barrier();
    u4* Wt = (u4*)(ws + OFF_WT);
    const int obase = ((u >> 1) * 2 + (v >> 2)) * 64 + 16 * (v & 3) + 8 * (u & 1);
    const int g = l >> 4, q4 = (l & 15) >> 2, p = l & 3;
#pragma unroll
    for (int rd = 0; rd < 3; ++rd) {
      int job = w + 8 * rd;
      if (job < 20) {
        int cbk = job >> 1, r8 = 4 * (job & 1) + g;
        const char* a = smem + (r8 * 4 + q4) * 336 + (16 * cbk + 4 * p) * 2;
        u2 re = tr_read(a), im = tr_read(a + 32 * 336);
        u4 o = {perm_lo(im[0], re[0]), perm_hi(im[0], re[0]), perm_lo(im[1], re[1]), perm_hi(im[1], re[1])};
        WT16(2, Wt, (160 * hf + 16 * cbk + (l & 15)) * 512 + obase + r8, o);
      }
    }
    if (hf == 1 && tid < 8) {
      u4 o;
#pragma unroll
      for (int reg = 0; reg < 4; ++reg)
        o[reg] = (unsigned)*(const u16*)(smem + (tid * 4 + reg) * 336 + 320) | ((unsigned)*(const u16*)(smem + (32 + tid * 4 + reg) * 336 + 320) << 16);
      Wt[320 * 512 + obase + tid] = o;
    }
    if (tid == 0) {
      const unsigned* m = (const unsigned*)(smem + 21504);
      ((unsigned*)(ws + OFF_WM))[task] = umax(umax(umax(m[0], m[1]), umax(m[2], m[3])), umax(umax(m[4], m[5]), umax(m[6], m[7])));
    }
    ST_FLUSH(0);
    return;
  }
  if (b < 3 && tid >= 256) *(unsigned*)(ws + OFF_CNT + (b * 16 + ((tid - 256) >> 4)) * 256 + (tid & 15) * 4) = 0u;
  const int q = (b & 7) * 32 + (b >> 3);
  const int ih = (w >> 1) & 1, cb = w & 1;
  char* lin = smem;
  char* lout = smem + 10240;
  char* ct = smem + 22528 + cb * 8448 + l * 16;
  RowC C;
  if (w < 4) {
    const int j = l >> 5, li = l & 31;
    const u2* xr = (const u2*)(x + (size_t)srow_of(q, w, j) * DD);
    u2 xv[5];
#pragma unroll
    for (int c = 0; c < 5; ++c) xv[c] = xr[li + 32 * c];
    float v[20];
#pragma unroll
    for (int c = 0; c < 5; ++c) {
      v[4 * c] = bf_lo(xv[c][0]); v[4 * c + 1] = bf_hi(xv[c][0]);
      v[4 * c + 2] = bf_lo(xv[c][1]); v[4 * c + 3] = bf_hi(xv[c][1]);
    }
    float sa = 0.f, sb = 0.f;
#pragma unroll
    for (int t = 0; t < 10; ++t) { sa += v[2 * t]; sb += v[2 * t + 1]; }
    const float mean = half_sum(sa + sb) * (1.f / 640.f);
    ST(1);
    sa = 0.f; sb = 0.f;
#pragma unroll
    for (int t = 0; t < 10; ++t) { float d0 = v[2 * t] - mean, d1 = v[2 * t + 1] - mean; sa += d0 * d0; sb += d1 * d1; }
    const float var = half_sum(sa + sb) * (1.f / 640.f);
    const float sd = rbf(__builtin_sqrtf(rbf(rbf(var) + 1e-5f)));
    const float rs = 16.f * __builtin_amdgcn_rcpf(sd);
    const float sh = -rbf(mean) * rs;
    char* wp = lin + (4 * li + 2 * j) * 16 + w * 4;
#pragma unroll
    for (int c = 0; c < 5; ++c) {
      unsigned p01 = pk_f16(v[4 * c] * rs + sh, v[4 * c + 1] * rs + sh);
      unsigned p23 = pk_f16(v[4 * c + 2] * rs + sh, v[4 * c + 3] * rs + sh);
      auto r = __builtin_amdgcn_permlane32_swap(p01, p23, false, false);
      unsigned pa = r[0], pb = r[1];
      *(unsigned*)(wp + c * 2048) = perm_lo(pb, pa);
      *(unsigned*)(wp + c * 2048 + 16) = perm_hi(pb, pa);
    }
    ST(2);
    raw_barrier();
    ST(3);
    rc_load(C, ct, l);
    k1_dft<false>(C, lin, lout, l, ih, cb);
  } else {
    rc_part<-1>(ct, l, cb, 1.f / 256.f, 2 * ih);
    rc_part<-1>(ct, l, cb, 1.f / 256.f, 2 * ih + 1);
    raw_barrier();
    rc_load(C, ct, l);
    k1_dft<true>(C, lin, lout, l, ih, cb);
  }
  raw_barrier();
  ST(4);
  u4* Z1 = (u4*)(ws + OFF_Z1);
  WT16(8, Z1, Z1IDX(q, tid), *(const u4*)(lout + tid * 16));
  if (tid < 128) WT16(8, Z1, Z1IDX(q, tid + 512), *(const u4*)(lout + (tid + 512) * 16));
  ST_FLUSH(0);
}

DI unsigned wmax_exp(u2 m) {
  unsigned eb = wave_umax(umax(m[0], m[1])) >> 7;
  return eb > 251u ? 251u : eb;
}
extern "C" __global__ __launch_bounds__(256) void gf_k2(const u16* __restrict__ wg, const u16* __restrict__ wu, const u16* __restrict__ wd,
                                                 char* __restrict__ ws) {
  __shared__ __attribute__((aligned(16))) char smem[32 * 880];
  const int b = blockIdx.x, tid = threadIdx.x, l = tid & 63;
  const int w = __builtin_amdgcn_readfirstlane(tid >> 6);
  const int g = l >> 4, r16 = l & 15;
  ST_DECL; ST(0);
  if (b >= 488) {
    const int task = b - 488, ks = task >> 1, hf = task & 1;
    if (ks >= 54) return;
    u4 a[5];
#pragma unroll
    for (int it = 0; it < 5; ++it) {
      int idx = tid + 256 * it, k = idx / 40, seg = idx - k * 40;
      a[it] = *(const u4*)(wd + (size_t)(8 * blk_of_pos(4 * ks + (k >> 3)) + (k & 7)) * DD + 320 * hf + 8 * seg);
    }
    SCHED_FENCE();
#pragma unroll
    for (int it = 0; it < 5; ++it) {
      int idx = tid + 256 * it, k = idx / 40, seg = idx - k * 40;
      *(u4*)(smem + k * 656 + seg * 16) = a[it];
    }
    raw_barrier();
    u4* W2 = (u4*)(ws + OFF_W2) + (size_t)(ks * 40 + 20 * hf + 5 * w) * 64 + l;
    const char* base = smem + (8 * g + (r16 >> 2)) * 656 + (l & 3) * 8 + w * 160;
#pragma unroll
    for (int t = 0; t < 5; ++t) {
      u2 lo = tr_read(base + t * 32), hi = tr_read(base + t * 32 + 4 * 656);
      WT16(1, W2, t * 64, (u4{lo[0], lo[1], hi[0], hi[1]}));
    }
    ST_FLUSH(1);
    return;
  }
  if (b >= 328) {
    const int fg = b & 7, ks = (b - 328) >> 3;
    u4 a[4], c[4];
#pragma unroll
    for (int it = 0; it < 4; ++it) {
      int idx = tid + 256 * it; idx = idx < 864 ? idx : tid;
      int k = idx / 27, seg = idx - k * 27;
      const size_t off = (size_t)(32 * ks + k) * FF + 216 * fg + 8 * seg;
      a[it] = *(const u4*)(wg + off); c[it] = *(const u4*)(wu + off);
    }
    SCHED_FENCE();
#pragma unroll
    for (int it = 0; it < 4; ++it) {
      int idx = tid + 256 * it; idx = idx < 864 ? idx : tid;
      int k = idx / 27, seg = idx - k * 27;
      u4 o0 = {perm_lo(c[it][0], a[it][0]), perm_hi(c[it][0], a[it][0]), perm_lo(c[it][1], a[it][1]), perm_hi(c[it][1], a[it][1])};
      u4 o1 = {perm_lo(c[it][2], a[it][2]), perm_hi(c[it][2], a[it][2]), perm_lo(c[it][3], a[it][3]), perm_hi(c[it][3], a[it][3])};
      *(u4*)(smem + k * 880 + seg * 32) = o0;
      *(u4*)(smem + k * 880 + seg * 32 + 16) = o1;
    }
    raw_barrier();
    u4* W1 = (u4*)(ws + OFF_W1) + (size_t)(ks * 216 + fg * 27 + w) * 64 + l;
    const char* base = smem + (8 * g + (r16 >> 2)) * 880 + (l & 3) * 8 + w * 32;
#pragma unroll
    for (int t = 0; t < 7; ++t) {
      if (t < 6 || w < 3) {
        u2 lo = tr_read(base + t * 128), hi = tr_read(base + t * 128 + 4 * 880);
        WT16(4, W1, t * 256, (u4{lo[0], lo[1], hi[0], hi[1]}));
      }
    }
    ST_FLUSH(1);
    return;
  }
  const int xi = b >> 3;
  int kd = 40 * (b & 7) + xi;
  if (xi == 40) { if ((b & 7) != 7) return; kd = 320; }
  const int kdm = kd == 0 ? 0 : 640 - kd;
  const u4* Z1 = (const u4*)(ws + OFF_Z1);
  const u4 z1 = Z1[Z1IDX(tid, kd)], z2 = Z1[Z1IDX(tid, kdm)];
  u4 wv[2];
  {
    const u4* Wt = (const u4*)(ws + OFF_WT) + kd * 512 + 128 * w + l;
    wv[0] = Wt[0]; wv[1] = Wt[64];
  }
  const u2 wmv = ((const u2*)(ws + OFF_WM))[l];
  SCHED_FENCE();
  u4 M1[4], M2[4], N1[4], N2[4];
  float TC[4][4], TS[4][4];
  {
    const float g4 = (float)(4 * g), k1f = (float)(16 * w + r16);
    const float ka = k1f * (1.f / 64.f), ka0 = g4 * ka;
    const bool odd = r16 & 1;
    const unsigned s1 = (r16 & 2) ? 0x80008000u : 0u;
    const unsigned s1b = (((r16 & 3) + 1) & 2) ? 0x80008000u : 0u;
    const unsigned s2 = odd ? 0x80008000u : 0u;
    const unsigned s3 = ((3 * (r16 & 3)) & 2) ? 0x80008000u : 0u, s3b = ((3 * (r16 & 3) + 1) & 2) ? 0x80008000u : 0u;
#pragma unroll
    for (int jj = 0; jj < 4; ++jj) {
      float r = (float)jj * ka + ka0, c = fcos(r), sn = fsin(r);
      const unsigned a = pk_f16(c, sn), bq = pk_f16(-sn, c);
      const unsigned p = odd ? bq : a, q2 = odd ? a : bq;
      M1[0][jj] = a; M2[0][jj] = bq;
      M1[1][jj] = p ^ s1; M2[1][jj] = q2 ^ s1b;
      M1[2][jj] = a ^ s2; M2[2][jj] = bq ^ s2;
      M1[3][jj] = p ^ s3; M2[3][jj] = q2 ^ s3b;
    }
    const float ro = (float)r16 * (1.f / 32.f), ro0 = g4 * ro;
#pragma unroll
    for (int jj = 0; jj < 4; ++jj) {
      float r = (float)jj * ro + ro0, c = fcos(r), sn = fsin(r);
      const unsigned a = pk_f16(c, sn), bq = pk_f16(-sn, c), sj = (jj & 1) ? 0x80008000u : 0u;
      N1[0][jj] = a; N2[0][jj] = bq;
      N1[1][jj] = a ^ s2; N2[1][jj] = bq ^ s2;
      N1[2][jj] = a ^ sj; N2[2][jj] = bq ^ sj;
      N1[3][jj] = a ^ (s2 ^ sj); N2[3][jj] = bq ^ (s2 ^ sj);
    }
    {
      const float kb = k1f * (1.f / 2048.f);
      const float c1 = fcos(kb), d1 = fsin(kb), c16 = fcos(16.f * kb), d16 = fsin(16.f * kb);
      TC[0][0] = fcos(g4 * kb); TS[0][0] = fsin(g4 * kb);
#pragma unroll
      for (int rg = 1; rg < 4; ++rg) {
        TC[0][rg] = TC[0][rg - 1] * c1 - TS[0][rg - 1] * d1; TS[0][rg] = TS[0][rg - 1] * c1 + TC[0][rg - 1] * d1;
      }
#pragma unroll
      for (int rg = 0; rg < 4; ++rg) { TC[1][rg] = TC[0][rg] * c16 - TS[0][rg] * d16; TS[1][rg] = TS[0][rg] * c16 + TC[0][rg] * d16; }
    }
#pragma unroll
    for (int xx = 0; xx < 2; ++xx) {
      const float nu = (float)(16 * xx + r16) * (1.f / 2048.f), nu0 = ((float)(16 * w) + g4) * nu;
      const float c1 = fcos(nu), d1 = fsin(nu);
      TC[2 + xx][0] = fcos(nu0); TS[2 + xx][0] = fsin(nu0);
#pragma unroll
      for (int rg = 1; rg < 4; ++rg) {
        TC[2 + xx][rg] = TC[2 + xx][rg - 1] * c1 - TS[2 + xx][rg - 1] * d1;
        TS[2 + xx][rg] = TS[2 + xx][rg - 1] * c1 + TC[2 + xx][rg - 1] * d1;
      }
    }
  }
  SCHED_FENCE();
  u4* sa = (u4*)smem;
  u4* ex = (u4*)(smem + 8192);
  {
    u4 A0, A1;
#pragma unroll
    for (int jj = 0; jj < 4; ++jj) {
      h2 p = __builtin_bit_cast(h2, (unsigned)z1[jj]), qq = __builtin_bit_cast(h2, (unsigned)z2[jj]);
      h2 qa = {qq[0], -qq[1]};
      h2 pb = {p[1], -p[0]}, qb = {qq[1], qq[0]};
      A0[jj] = __builtin_bit_cast(unsigned, p + qa);
      A1[jj] = __builtin_bit_cast(unsigned, pb + qb);
    }
    sa[tid] = A0;
    sa[256 + tid] = A1;
  }
  ST(1);
  raw_barrier();
  const f4 zero = {0.f, 0.f, 0.f, 0.f};
  f4 Tr[2] = {zero, zero}, Ti[2] = {zero, zero};
#pragma unroll
  for (int t = 0; t < 4; ++t) {
    u4 A0 = sa[64 * t + l], A1 = sa[256 + 64 * t + l];
    Tr[0] = mfma_h(A0, M1[t], Tr[0]); Tr[1] = mfma_h(A1, M1[t], Tr[1]);
    Ti[0] = mfma_h(A0, M2[t], Ti[0]); Ti[1] = mfma_h(A1, M2[t], Ti[1]);
  }
  ST(2);
  const unsigned eb = wmax_exp(wmv);
  const float fs = asf((252u - eb) << 23) * (1.f / 256.f);
  u4 X[2];
#pragma unroll
  for (int rb = 0; rb < 2; ++rb)
#pragma unroll
    for (int rg = 0; rg < 4; ++rg) {
      float a = Tr[rb][rg], c = Ti[rb][rg], tc = TC[rb][rg], ts = TS[rb][rg];
      X[rb][rg] = pk_f16(a * tc + c * ts, c * tc - a * ts);
    }
  u4 Y[2];
#pragma unroll
  for (int ob = 0; ob < 2; ++ob) {
    f4 xr = mfma_h(N1[2 * ob], X[0], zero); xr = mfma_h(N1[2 * ob + 1], X[1], xr);
    f4 xim = mfma_h(N2[2 * ob], X[0], zero); xim = mfma_h(N2[2 * ob + 1], X[1], xim);
#pragma unroll
    for (int rg = 0; rg < 4; ++rg) {
      float wr_ = bf_lo(wv[ob][rg]) * fs, wi_ = bf_hi(wv[ob][rg]) * fs;
      float yr = xr[rg] * wr_ - xim[rg] * wi_;
      float yi = xr[rg] * wi_ + xim[rg] * wr_;
      Y[ob][rg] = pk_f16(yi, yr);
    }
  }
#pragma unroll
  for (int cb = 0; cb < 2; ++cb) {
    f4 ui = mfma_h(Y[0], N1[2 * cb], zero); ui = mfma_h(Y[1], N1[2 * cb + 1], ui);
    f4 ur = mfma_h(Y[0], N2[2 * cb], zero); ur = mfma_h(Y[1], N2[2 * cb + 1], ur);
    u4 U;
#pragma unroll
    for (int rg = 0; rg < 4; ++rg) {
      float tc = TC[2 + cb][rg], ts = TS[2 + cb][rg];
      U[rg] = pk_f16(ui[rg] * tc + ur[rg] * ts, ur[rg] * tc - ui[rg] * ts);
    }
    ex[(w * 2 + cb) * 64 + l] = U;
  }
  ST(3);
  raw_barrier();
  ST(4);
  f4 Vi[2] = {zero, zero}, Vr[2] = {zero, zero};
#pragma unroll
  for (int t = 0; t < 4; ++t)
#pragma unroll
    for (int cb = 0; cb < 2; ++cb) {
      u4 U = ex[(t * 2 + cb) * 64 + l];
      Vi[cb] = mfma_h(M1[t], U, Vi[cb]);
      Vr[cb] = mfma_h(M2[t], U, Vr[cb]);
    }
  u4* Z2 = (u4*)(ws + OFF_Z2);
  const float sg = (kd == 0 || kd == 320) ? 0.f : 1.f;
  u4 o, om;
#pragma unroll
  for (int rg = 0; rg < 4; ++rg) {
    float ia = Vi[0][rg] * sg, ib = Vi[1][rg] * sg;
    o[rg] = pk_f16(Vr[0][rg] - ib, ia + Vr[1][rg]);
    om[rg] = pk_f16(Vr[0][rg] + ib, Vr[1][rg] - ia);
  }
  WT16(16, Z2, Z2IDX(tid, kd), o);
  if (kdm != kd) WT16(16, Z2, Z2IDX(tid, kdm), om);
  ST_FLUSH(1);
}

#define GLDS(src, dst) __builtin_amdgcn_global_load_lds((const GAS void*)(src), (LAS void*)(dst), 16, 0, 0)
#define LDAUX 16
#define ACQ 0
#define GLDS_SC1(src, dst) __builtin_amdgcn_global_load_lds((const GAS void*)(src), (LAS void*)(dst), 16, 0, LDAUX)
template <int N> DI void wait_vm() { asm volatile("s_waitcnt vmcnt(%0)" ::"n"(N) : "memory"); }
#define SGB(mask, n) __builtin_amdgcn_sched_group_barrier(mask, n, 0)

DI float geglu(float g, float u) {
  float t = g * g;
  float z = g * (t * (-2.f * 1.4426950408889634f * 0.7978845608028654f * 0.044715f) + (-2.f * 1.4426950408889634f * 0.7978845608028654f));
  float e = __builtin_amdgcn_exp2f(z);
  return (g * u) * __builtin_amdgcn_rcpf(1.f + e);
}
DI void poll_ge16(const char* ws, int cnt) {
  const GAS unsigned* p = (const GAS unsigned*)(ws + OFF_CNT + cnt * 256) + (threadIdx.x & 15);
  for (int spins = 0; spins < (1 << 20); ++spins) {
    unsigned v = __hip_atomic_load(p, __ATOMIC_RELAXED, __HIP_MEMORY_SCOPE_AGENT);
    if (__builtin_amdgcn_ballot_w64(v == 0u) == 0ull) break;
    __builtin_amdgcn_s_sleep(1);
  }
  asm volatile("" ::: "memory");
}
DI void signal(char* ws, int cnt, int slot) {
  __hip_atomic_store((GAS unsigned*)(ws + OFF_CNT + cnt * 256) + slot, 1u, __ATOMIC_RELAXED, __HIP_MEMORY_SCOPE_AGENT);
}

constexpr int G1_STG = 22 * 1024, G1_NB = 7, G1_NS = 20, G1_TAIL = 4, G1_MAIN = G1_NS - G1_TAIL;
constexpr int LDS_TOTAL = 160 * 1024, PA_OFF = 6 * G1_STG;
constexpr int G2_NS = 14, G2_AD = 6;
#define EARLY_AT 5

DI void arrive_n(char* smem, char* ws, int word, int cnt, int l, unsigned last, int slot) {
  if (l == 0) {
    unsigned old = __hip_atomic_fetch_add((LAS unsigned*)(smem + LDS_TOTAL - 64 + word * 4), 1u, __ATOMIC_RELAXED, __HIP_MEMORY_SCOPE_WORKGROUP);
    if (old == last) signal(ws, cnt, slot);
  }
}
template <int NW> DI void k3_loader(char* smem, char* ws, int mt, int nb0, int mt2, int db0, int l, int j) {
  const unsigned loff = l * 16;
  constexpr int NL = NW + 2;
  const char* sA = ws + OFF_HP + (size_t)(8 * mt + j) * 1024 + loff;
  const char* sB = ws + OFF_W1 + (size_t)(nb0 + j) * 1024 + loff;
  auto issueA = [&](int ks) {
    char* d = smem + (ks % G1_NB) * G1_STG + j * 1024;
    GLDS_SC1(sA + (size_t)ks * (128 * 1024), d);
    GLDS_SC1(sA + (size_t)ks * (128 * 1024) + 4096, d + 4096);
  };
  auto issueW = [&](int ks) {
    char* d = smem + (ks % G1_NB) * G1_STG + 8192 + j * 1024;
#pragma unroll
    for (int t = 0; t < NW; ++t) GLDS(sB + (size_t)ks * (216 * 1024) + t * 4096, d + t * 4096);
  };
  int foff[5];
#pragma unroll
  for (int t = 0; t < 5; ++t) { int f = j + 4 * t, kq = f / 5; foff[t] = (kq * 40 + db0 + f - 5 * kq) * 1024; }
  const char* s2 = ws + OFF_W2 + loff;
  auto issue2 = [&](int i) {
    char* d = smem + ((i + 6) % G1_NB) * G1_STG + j * 1024;
#pragma unroll
    for (int t = 0; t < 5; ++t) GLDS(s2 + (size_t)i * (160 * 1024) + foff[t], d + t * 4096);
  };
  raw_barrier();
  issueW(0); issueW(1); issueW(2); issueW(5);
  raw_barrier(); raw_barrier(); raw_barrier();
  issueW(3); issueW(4); issueW(6);
  poll_ge16(ws, mt);
  u4 pk[33];
  auto fetch = [&](int e) {
    const int i = 7 + e / 5, t = e % 5;
    pk[e] = *(const u4*)(s2 + (size_t)i * (160 * 1024) + (e == 32 && j >= 2 ? foff[1] : foff[t]));
  };
  auto park = [&](int i) {
    char* d = smem + ((i + 6) % G1_NB) * G1_STG + j * 1024 + loff;
#pragma unroll
    for (int t = 0; t < (i < 13 ? 5 : 3); ++t) *(u4*)(d + t * 4096) = pk[(i - 7) * 5 + t];
  };
#pragma unroll
  for (int s = 0; s < 7; ++s) issueA(s);
  wait_vm<12>();
  raw_barrier();
  static_for<G1_MAIN - 1>([&](auto S_) {
    constexpr int s = decltype(S_)::value;
    constexpr int n = s <= 5 ? 2 * (5 - s) + NL * s : (s <= 13 ? NL * ((s + 6 < 19 ? s + 6 : 19) - (s + 1)) : NL * (19 - (s + 1)) + 5 * (s - 13));
    constexpr int lo = s - 6 > 0 ? s - 6 : 0, hi = s - 1 < 10 ? s - 1 : 10;
    wait_vm<n + (hi >= lo ? 3 * (hi - lo + 1) : 0)>();
    raw_barrier();
    if constexpr (s + G1_NB < G1_NS) { issueA(s + G1_NB); issueW(s + G1_NB); }
    else issue2(s - 13);
    if constexpr (s <= 10) { SCHED_FENCE(); fetch(3 * s); fetch(3 * s + 1); fetch(3 * s + 2); SCHED_FENCE(); }
  });
  wait_vm<10>();
  raw_barrier();
  issue2(2);
  raw_barrier();
  issue2(3); issue2(4); issue2(5); issue2(6);
  wait_vm<0>();
  raw_barrier();
  if (j == 0) poll_ge16(ws, 32 + (mt2 >> 1));
  raw_barrier();
  park(7); park(8); park(9);
  raw_barrier();
  park(10); park(11);
  raw_barrier();
  park(12); park(13);
  raw_barrier();
}
DI void arrive(char* smem, char* ws, int word, int cnt, int l, int slot) { arrive_n(smem, ws, word, cnt, l, 7u, slot); }

extern "C" __global__ __launch_bounds__(768, 3) void gf_k3(const u16* __restrict__ x, u16* __restrict__ out, char* __restrict__ ws) {
  extern __shared__ __attribute__((aligned(16))) char smem[];
  const int b = blockIdx.x, tid = threadIdx.x, l = tid & 63;
  const int w = __builtin_amdgcn_readfirstlane(tid >> 6);
  const int xcd = b & 7, idx = b >> 3;
  const int mt = 8 * (xcd & 1) + (idx & 7), nt = 4 * (xcd >> 1) + (idx >> 3);
  const int mt2 = 16 * (xcd & 1) + 8 * ((xcd >> 1) & 1) + (idx & 7), nt2 = 4 * (xcd >> 2) + (idx >> 3);
  const int nb0 = (27 * nt) >> 1, mb0 = 8 * mt, db0 = 5 * nt2;
  const unsigned loff = l * 16;
  ST_DECL; ST(0);
  if (w >= 8) {
    if (w < 10) k3_loader<4>(smem, ws, mt, nb0, mt2, db0, l, w - 8); else k3_loader<3>(smem, ws, mt, nb0, mt2, db0, l, w - 8);
    return;
  }
  const int g = l >> 4, r16 = l & 15;
  const auto rsrc = __builtin_amdgcn_make_buffer_rsrc(ws, 0, (int)WS_USED, 0x00020000);
  {
    char* pa = smem + PA_OFF;
    const int q = 16 * mt + nt;
    const int ih = (w >> 1) & 1, cb = w & 1, ii = w >> 2, j = l & 1;
    const u4* Z2 = (const u4*)(ws + OFF_Z2);
    const u4 zv0 = Z2[Z2IDX(q, tid)];
    const u4 zv1 = Z2[Z2IDX(q, 512 + (tid & 127))];
    const int e0 = r16 >> 1;
    const bool v2 = e0 < 4;
    const size_t roff = (size_t)srow_of(q, 2 * ih + ii, j) * DD + 16 * cb + 4 * g + 32 * e0;
    u2 xv[3];
    xv[0] = *(const u2*)(x + roff); xv[1] = *(const u2*)(x + roff + 256); xv[2] = *(const u2*)(x + roff + (v2 ? 512 : 0));
    const u2 wmv = ((const u2*)(ws + OFF_WM))[l];
    SCHED_FENCE();
    char* ct = pa + 10368 + cb * 8448 + l * 16;
    rc_part<1>(ct, l, cb, 1.f / 32.f, w >> 1);
    SCHED_FENCE();
    *(u4*)(pa + tid * 16) = zv0;
    if (tid < 128) *(u4*)(pa + (512 + tid) * 16) = zv1;
    if (tid < 3) *(unsigned*)(smem + LDS_TOTAL - 64 + tid * 4) = 0u;
    raw_barrier();
    RowC C;
    rc_load(C, ct, l);
    u4 T0, T1;
    if (ii) row_stage_a(C, pa, l, ih, true, T0, T1); else row_stage_a(C, pa, l, ih, false, T0, T1);
    const unsigned eb = wmax_exp(wmv);
    const float ysc = asf((eb + 2u) << 23) * 0.05f;
    float h[12];
    float s = 0.f;
    const int mp = 8 * q + 2 * (2 * ih + ii) + j;
    char* hs = smem + 4 * G1_STG - 2048;
    char* ns = smem + 5 * G1_STG - 2048;
    const int r8 = mp & 7;
#pragma unroll
    for (int ob = 0; ob < 3; ++ob) {
      f4 z = {0.f, 0.f, 0.f, 0.f};
      z = mfma_h(T0, C.t[2 + ob], z);
      u4 gb = {C.t[5][ob], 0u, 0u, 0u};
      z = mfma_h(T1, gb, z);
      unsigned y01 = pk_bf16(z[0] * ysc, z[1] * ysc), y23 = pk_bf16(z[2] * ysc, z[3] * ysc);
      unsigned h01 = pk_bf16(bf_lo(xv[ob][0]) + bf_lo(y01), bf_hi(xv[ob][0]) + bf_hi(y01));
      unsigned h23 = pk_bf16(bf_lo(xv[ob][1]) + bf_lo(y23), bf_hi(xv[ob][1]) + bf_hi(y23));
      if (ob == 2) { h01 = v2 ? h01 : 0u; h23 = v2 ? h23 : 0u; }
      if (ob < 2 || v2) *(u2*)(hs + ((2 * e0 + cb + 16 * ob) * 8 + r8) * 32 + g * 8) = u2{h01, h23};
      h[4 * ob] = bf_lo(h01); h[4 * ob + 1] = bf_hi(h01); h[4 * ob + 2] = bf_lo(h23); h[4 * ob + 3] = bf_hi(h23);
      s += (h[4 * ob] + h[4 * ob + 1]) + (h[4 * ob + 2] + h[4 * ob + 3]);
    }
    const float mloc = parity_sum(s) * (1.f / 320.f);
    float s2 = 0.f, s3 = 0.f;
#pragma unroll
    for (int t = 0; t < 8; ++t) { float d = h[t] - mloc; s2 += d * d; }
#pragma unroll
    for (int t = 8; t < 12; ++t) { float d = h[t] - mloc; s3 += d * d; }
    const float m2loc = parity_sum(v2 ? s2 + s3 : s2);
    f2* ex = (f2*)(pa + 10240);
    if (l < 2) ex[w * 2 + l] = f2{mloc, m2loc};
    raw_barrier();
    {
      const int hb = (int)OFF_HH + 8 * q * 32;
      __builtin_amdgcn_raw_buffer_store_b128(*(const u4*)(hs + tid * 16), rsrc, (tid >> 4) * (2048 * 32) + (tid & 15) * 16, hb, 16);
      if (tid < 128) __builtin_amdgcn_raw_buffer_store_b128(*(const u4*)(hs + 8192 + tid * 16), rsrc, (32 + (tid >> 4)) * (2048 * 32) + (tid & 15) * 16, hb, 16);
    }
    const f2 o = ex[(w ^ 1) * 2 + j];
    const float mean = 0.5f * (mloc + o[0]);
    const float dl = mloc - o[0];
    const float var = (m2loc + o[1] + dl * dl * 160.f) * (1.f / 640.f);
    const float mb = rbf(mean);
    const float sd = rbf(__builtin_sqrtf(rbf(rbf(var) + 1e-5f)));
    const float rs = __builtin_amdgcn_rcpf(sd);
#pragma unroll
    for (int ob = 0; ob < 3; ++ob) {
      unsigned d01 = pk_bf16(h[4 * ob] - mb, h[4 * ob + 1] - mb);
      unsigned d23 = pk_bf16(h[4 * ob + 2] - mb, h[4 * ob + 3] - mb);
      u2 st = {pk_bf16(bf_lo(d01) * rs, bf_hi(d01) * rs), pk_bf16(bf_lo(d23) * rs, bf_hi(d23) * rs)};
      if (ob < 2 || v2) *(u2*)(ns + (((8 * ob + e0) * 4 + 2 * cb + (g >> 1)) * 8 + r8) * 16 + (g & 1) * 8) = st;
    }
    raw_barrier();
    {
      const int nbase = (int)OFF_HP + (q >> 1) * 1024 + (q & 1) * 128;
      __builtin_amdgcn_raw_buffer_store_b128(*(const u4*)(ns + tid * 16), rsrc, (tid >> 5) * (128 * 1024) + ((tid >> 3) & 3) * 256 + (tid & 7) * 16, nbase, 16);
      if (tid < 128) __builtin_amdgcn_raw_buffer_store_b128(*(const u4*)(ns + 8192 + tid * 16), rsrc, (16 + (tid >> 5)) * (128 * 1024) + ((tid >> 3) & 3) * 256 + (tid & 7) * 16, nbase, 16);
    }
    ST(1);
    wait_vm<0>();
    raw_barrier();
    if (tid == 0) signal(ws, mt, nt);
    ST(2);
  }
  {
    int lb = threadIdx.x & 63;
    asm volatile("" : "+v"(lb));
    const unsigned loff = lb * 16;
    const int wm = w >> 1, wn = w & 1;
    const char* rdA = smem + (2 * wm) * 1024 + loff;
    const char* rdB = smem + (8 + 7 * wn) * 1024 + loff;
    f4 acc[7][2];
#pragma unroll
    for (int i = 0; i < 7; ++i) { acc[i][0] = f4{0.f, 0.f, 0.f, 0.f}; acc[i][1] = f4{0.f, 0.f, 0.f, 0.f}; }
    {
      u4 af[2][2], wf[2][7];
      raw_barrier();
      ST(3);
#pragma unroll
      for (int i = 0; i < 2; ++i) af[0][i] = *(const u4*)(rdA + i * 1024);
#pragma unroll
      for (int i = 0; i < 7; ++i) wf[0][i] = *(const u4*)(rdB + i * 1024);
      static_for<G1_MAIN>([&](auto S_) {
        constexpr int s = decltype(S_)::value;
        constexpr int cur = s & 1, nxt = cur ^ 1;
        constexpr bool more = s + 1 < G1_MAIN;
        if constexpr (more) {
          raw_barrier();
          constexpr int bo = ((s + 1) % G1_NB) * G1_STG;
#pragma unroll
          for (int i = 0; i < 2; ++i) af[nxt][i] = *(const u4*)(rdA + bo + i * 1024);
#pragma unroll
          for (int i = 0; i < 7; ++i) wf[nxt][i] = *(const u4*)(rdB + bo + i * 1024);
        }
#pragma unroll
        for (int i = 0; i < 7; ++i)
#pragma unroll
          for (int m = 0; m < 2; ++m) acc[i][m] = mfma_b(wf[cur][i], af[cur][m], acc[i][m]);
        if constexpr (more) {
#pragma unroll
          for (int i = 0; i < 9; ++i) { SGB(0x8, 1); SGB(0x100, 1); }
          SGB(0x8, 14);
        }
        SCHED_FENCE();
      });
    }
    raw_barrier();
    int lz = threadIdx.x & 63;
    asm volatile("" : "+v"(lz));
    const int g = lz >> 4, r16 = lz & 15;
    u4 af[G1_TAIL][2], wf[2][G1_TAIL];
#pragma unroll
    for (int t = 0; t < G1_TAIL; ++t) {
      const int bo = ((G1_MAIN + t) % G1_NB) * G1_STG;
      wf[0][t] = *(const u4*)(rdB + bo);
      af[t][0] = *(const u4*)(rdA + bo); af[t][1] = *(const u4*)(rdA + bo + 1024);
    }
    const int aoff = (int)OFF_AP + (mb0 + 2 * wm) * 1024 + r16 * 16 + 4 * g;
    auto epi = [&](int i) {
      const int pos = i < 4 ? 8 * nt + 4 * wn + i : 128 + 11 * (nt >> 1) + 5 * (nt & 1) + 3 * wn + (i - 4);
      const int so = (pos >> 2) * (128 * 1024) + (pos & 3) * 256;
      if (i == 6 && wn == 1 && !(nt & 1)) return;
#pragma unroll
      for (int m = 0; m < 2; ++m)
        __builtin_amdgcn_raw_buffer_store_b32(pk_bf16(geglu(acc[i][m][0], acc[i][m][1]), geglu(acc[i][m][2], acc[i][m][3])), rsrc,
                                              aoff + m * 1024, so, 16);
    };
    static_for<7>([&](auto I_) {
      constexpr int i = decltype(I_)::value;
      if constexpr (i + 1 < 7) {
#pragma unroll
        for (int t = 0; t < G1_TAIL; ++t) wf[(i + 1) & 1][t] = *(const u4*)(rdB + ((G1_MAIN + t) % G1_NB) * G1_STG + (i + 1) * 1024);
      }
#pragma unroll
      for (int t = 0; t < G1_TAIL; ++t)
#pragma unroll
        for (int m = 0; m < 2; ++m) acc[i][m] = mfma_b(wf[i & 1][t], af[t][m], acc[i][m]);
      if constexpr (i > 0) {
        epi(i - 1);
#pragma unroll
        for (int t = 0; t < 8; ++t) { SGB(0x8, 1); if (t < 4 && i + 1 < 7) SGB(0x100, 1); SGB(0x2, 4); SGB(0x400, 1); }
      }
      SCHED_FENCE();
      if constexpr (i == EARLY_AT) { wait_vm<2 * (EARLY_AT - 4)>(); arrive(smem, ws, 0, 16 + mt, lz, nt); }
    });
    epi(6);
    ST(4);
    raw_barrier();
    wait_vm<0>();
    arrive(smem, ws, 1, 32 + mt, lz, nt);
    if (w == 0) poll_ge16(ws, 16 + (mt2 >> 1));
  }
  {
    int l = threadIdx.x & 63;
    asm volatile("" : "+v"(l));
    const int g = l >> 4, r16 = l & 15;
    const unsigned loff = l * 16;
    const int kq = w & 3, mh = w >> 2;
    const int mb2 = 4 * mt2 + 2 * mh;
    f4 acc[2][5];
#pragma unroll
    for (int m = 0; m < 2; ++m)
#pragma unroll
      for (int n = 0; n < 5; ++n) acc[m][n] = f4{0.f, 0.f, 0.f, 0.f};
    const char* rdW = smem + kq * 5120 + loff;
    const int abase = (int)OFF_AP + (kq * 128 + mb2) * 1024;
    u4 fa[G2_AD][2], wf[2][5];
    auto loadA = [&](int i, u4 (&a)[2]) {
      const int so = abase + (i < 13 ? i : (kq < 2 ? 13 : 12)) * (512 * 1024);
      a[0] = __builtin_amdgcn_raw_buffer_load_b128(rsrc, loff, so, LDAUX);
      a[1] = __builtin_amdgcn_raw_buffer_load_b128(rsrc, loff + 1024, so, LDAUX);
    };
    raw_barrier();
    ST(5);
    static_for<G2_AD>([&](auto I_) { constexpr int i = decltype(I_)::value; loadA(i, fa[i]); SCHED_FENCE(); });
    u2 hv[3];
#pragma unroll
    for (int n = 0; n < 5; ++n) wf[0][n] = *(const u4*)(rdW + 6 * G1_STG + n * 1024);
    SCHED_FENCE();
    static_for<G2_NS - 1>([&](auto I_) {
      constexpr int i = decltype(I_)::value;
      constexpr int cur = i & 1, nxt = cur ^ 1;
      if constexpr (i == 2 || i == 4 || i == 6 || i == 9) raw_barrier();
#pragma unroll
      for (int n = 0; n < 5; ++n) wf[nxt][n] = *(const u4*)(rdW + ((i + 7) % G1_NB) * G1_STG + n * 1024);
#pragma unroll
      for (int m = 0; m < 2; ++m)
#pragma unroll
        for (int n = 0; n < 5; ++n) acc[m][n] = mfma_b(wf[cur][n], fa[i % G2_AD][m], acc[m][n]);
      if constexpr (i + G2_AD < G2_NS) loadA(i + G2_AD, fa[i % G2_AD]);
      if constexpr (i == 9) {
#pragma unroll
        for (int u = 0; u < 3; ++u) {
          int t = kq + 4 * u; t = t < 10 ? t : kq;
          const int m = t >= 5, n = t - 5 * m;
          hv[u] = __builtin_amdgcn_raw_buffer_load_b64(rsrc, r16 * 32 + g * 8, (int)OFF_HH + ((db0 + n) * 2048 + 16 * (mb2 + m)) * 32, 16);
        }
      }
      SGB(0x100, 5); SGB(0x8, 10); SGB(0x20, 3);
      SCHED_FENCE();
    });
    if (kq < 2) {
#pragma unroll
      for (int m = 0; m < 2; ++m)
#pragma unroll
        for (int n = 0; n < 5; ++n) acc[m][n] = mfma_b(wf[1][n], fa[13 % G2_AD][m], acc[m][n]);
    }
    ST(6);
    raw_barrier();
    f4* red = (f4*)smem + mh * (40 * 64) + l;
#pragma unroll
    for (int t = 0; t < 10; ++t) red[(t * 4 + kq) * 64] = acc[t / 5][t % 5];
    raw_barrier();
#pragma unroll
    for (int u = 0; u < 3; ++u) {
      const int t = kq + 4 * u < 10 ? kq + 4 * u : kq, m = t >= 5, n = t - 5 * m;
      const f4* sp = red + t * 256;
      f4 o = (sp[0] + sp[64]) + (sp[128] + sp[192]);
      unsigned o01 = pk_bf16(o[0], o[1]), o23 = pk_bf16(o[2], o[3]);
      const int mp = 16 * (mb2 + m) + r16;
      const int oo = (srow_of(mp >> 3, (mp >> 1) & 3, mp & 1) * DD + 16 * (db0 + n) + 4 * g) * 2;
      __builtin_amdgcn_raw_buffer_store_b64(u2{pk_bf16(bf_lo(hv[u][0]) + bf_lo(o01), bf_hi(hv[u][0]) + bf_hi(o01)),
                    pk_bf16(bf_lo(hv[u][1]) + bf_lo(o23), bf_hi(hv[u][1]) + bf_hi(o23))},
                    __builtin_amdgcn_make_buffer_rsrc(out, 0, 2048 * 640 * 2, 0x00020000), oo, 0, OAUX);
    }
  }
  ST_FLUSH(2);
}

extern "C" __device__ __attribute__((used, noinline)) void gf_end() {}

#if defined(__HIP_DEVICE_COMPILE__)
#pragma clang attribute pop
#endif

extern "C" void kernel_launch(void* const* d_in, const int* in_sizes, int n_in, void* d_out, int out_size, void* d_ws,
                              size_t ws_size, hipStream_t stream) {
  const u16* x = (const u16*)d_in[0];
  const u16* wr = (const u16*)d_in[1];
  const u16* wi = (const u16*)d_in[2];
  const u16* wg = (const u16*)d_in[4];
  const u16* wu = (const u16*)d_in[5];
  const u16* wd = (const u16*)d_in[6];
  u16* out = (u16*)d_out;
  char* ws = (char*)d_ws;
  (void)hipFuncSetAttribute((const void*)gf_k3, hipFuncAttributeMaxDynamicSharedMemorySize, LDS_TOTAL);
  gf_k1<<<448, 512, 0, stream>>>(x, wr, wi, wd, ws);
  gf_k2<<<328 + 160 + (WDK == 2 ? 112 : 0), 256, 0, stream>>>(wg, wu, wd, ws);
  gf_k3<<<256, 768, LDS_TOTAL, stream>>>(x, out, ws);
}
```

```cpp
#include <hip/hip_runtime.h>
#include <hip/hip_bf16.h>
#include <stdint.h>
#include <utility>

#if defined(__HIP_DEVICE_COMPILE__)
#pragma clang attribute push(__attribute__((target("no-packed-fp32-ops"))), apply_to = function)
#endif

typedef _Float16 h8 __attribute__((ext_vector_type(8)));
typedef _Float16 h2 __attribute__((ext_vector_type(2)));
typedef float f4 __attribute__((ext_vector_type(4)));
typedef float f2 __attribute__((ext_vector_type(2)));
typedef short s8 __attribute__((ext_vector_type(8)));
typedef short s4 __attribute__((ext_vector_type(4)));
typedef unsigned u4 __attribute__((ext_vector_type(4)));
typedef unsigned u4u __attribute__((ext_vector_type(4), aligned(2)));
typedef unsigned u2 __attribute__((ext_vector_type(2)));
typedef unsigned short us2 __attribute__((ext_vector_type(2)));
typedef __bf16 bf2 __attribute__((ext_vector_type(2)));
typedef unsigned short u16;
#define DI __device__ __forceinline__
#define LAS __attribute__((address_space(3)))
#define GAS __attribute__((address_space(1)))

constexpr int DD = 640, FF = 1728;

constexpr size_t OFF_Z1 = 0;
constexpr size_t OFF_Z2 = OFF_Z1 + (size_t)640 * 256 * 16;
constexpr size_t OFF_WT = OFF_Z2 + (size_t)640 * 256 * 16;
constexpr size_t OFF_WM = OFF_WT + (size_t)321 * 512 * 16;
constexpr size_t OFF_HP = OFF_WM + 1024;
constexpr size_t OFF_W1 = OFF_HP + (size_t)20 * 128 * 1024;
constexpr size_t OFF_AP = OFF_W1 + (size_t)20 * 216 * 1024;
constexpr size_t OFF_W2 = OFF_AP + (size_t)54 * 128 * 1024;
constexpr size_t OFF_HH = OFF_W2 + (size_t)54 * 40 * 1024;
constexpr size_t OFF_CNT = OFF_HH + (size_t)40 * 2048 * 32;
constexpr size_t WS_USED = OFF_CNT + 48 * 256;

#define SCHED_FENCE() __builtin_amdgcn_sched_barrier(0)
#define WT16(base, idx, v) __builtin_amdgcn_raw_buffer_store_b128((v), __builtin_amdgcn_make_buffer_rsrc(ws, 0, (int)WS_USED, 0x00020000), (int)((const char*)&(base)[idx] - ws), 0, 16)
#define Z1IDX(q, k) ((((k) >> 2) * 256 + (q)) * 4 + ((k) & 3))
#define Z2IDX(q, k) ((q) * 640 + (k))

template <class Fn, int... I>
DI void static_for_impl(Fn&& f, std::integer_sequence<int, I...>) { (f(std::integral_constant<int, I>{}), ...); }
template <int N, class Fn>
DI void static_for(Fn&& f) { static_for_impl(f, std::make_integer_sequence<int, N>{}); }

DI unsigned pk_bf16(float a, float b) { f2 v = {a, b}; return __builtin_bit_cast(unsigned, __builtin_convertvector(v, bf2)); }
DI unsigned pk_f16(float a, float b) { f2 v = {a, b}; return __builtin_bit_cast(unsigned, __builtin_convertvector(v, h2)); }
DI float bf_lo(unsigned u) { return __builtin_bit_cast(float, u << 16); }
DI float bf_hi(unsigned u) { return __builtin_bit_cast(float, u & 0xffff0000u); }
DI float rbf(float a) { return bf_hi(pk_bf16(0.f, a)); }
DI float asf(unsigned u) { return __builtin_bit_cast(float, u); }
DI unsigned asu(float f) { return __builtin_bit_cast(unsigned, f); }
DI float fsin(float r) { return __builtin_amdgcn_sinf(r); }
DI float fcos(float r) { return __builtin_amdgcn_cosf(r); }
DI f4 mfma_h(u4 a, u4 b, f4 c) { return __builtin_amdgcn_mfma_f32_16x16x32_f16(__builtin_bit_cast(h8, a), __builtin_bit_cast(h8, b), c, 0, 0, 0); }
DI f4 mfma_b(u4 a, u4 b, f4 c) { return __builtin_amdgcn_mfma_f32_16x16x32_bf16(__builtin_bit_cast(s8, a), __builtin_bit_cast(s8, b), c, 0, 0, 0); }
DI u2 tr_read(const char* p) { return __builtin_bit_cast(u2, __builtin_amdgcn_ds_read_tr16_b64_v4i16((LAS s4*)p)); }
DI unsigned perm_lo(unsigned hi, unsigned lo) { return __builtin_amdgcn_perm(hi, lo, 0x05040100u); }
DI unsigned perm_hi(unsigned hi, unsigned lo) { return __builtin_amdgcn_perm(hi, lo, 0x07060302u); }

template <int CTRL> DI unsigned dppu(unsigned v) { return (unsigned)__builtin_amdgcn_update_dpp(0, (int)v, CTRL, 0xf, 0xf, true); }
template <int CTRL> DI float dpp(float v) { return asf(dppu<CTRL>(__builtin_bit_cast(unsigned, v))); }
DI float swap16_sum(float v) {
  unsigned u = __builtin_bit_cast(unsigned, v);
  auto r = __builtin_amdgcn_permlane16_swap(u, u, false, false);
  return asf((unsigned)r[0]) + asf((unsigned)r[1]);
}
DI float swap32_sum(float v) {
  unsigned u = __builtin_bit_cast(unsigned, v);
  auto r = __builtin_amdgcn_permlane32_swap(u, u, false, false);
  return asf((unsigned)r[0]) + asf((unsigned)r[1]);
}
DI float half_sum(float v) {
  v += dpp<0x121>(v); v += dpp<0x122>(v); v += dpp<0x124>(v); v += dpp<0x128>(v);
  return swap16_sum(v);
}
DI float parity_sum(float v) {
  v += dpp<0x122>(v); v += dpp<0x124>(v); v += dpp<0x128>(v);
  v = swap16_sum(v);
  return swap32_sum(v);
}
DI unsigned umax(unsigned a, unsigned b) { return a > b ? a : b; }
DI unsigned wave_umax(unsigned v) {
  v = umax(v, dppu<0x121>(v)); v = umax(v, dppu<0x122>(v)); v = umax(v, dppu<0x124>(v)); v = umax(v, dppu<0x128>(v));
  auto r = __builtin_amdgcn_permlane16_swap(v, v, false, false);
  v = umax((unsigned)r[0], (unsigned)r[1]);
  auto r2 = __builtin_amdgcn_permlane32_swap(v, v, false, false);
  return umax((unsigned)r2[0], (unsigned)r2[1]);
}
DI void raw_barrier() {
  asm volatile("s_waitcnt lgkmcnt(0)" ::: "memory");
  __builtin_amdgcn_s_barrier();
  asm volatile("" ::: "memory");
}
DI int srow_of(int q, int i, int j) { return 32 * (4 * (q >> 4) + i) + (q & 15) + 16 * j; }
DI int blk_of_pos(int pos) {
  if (pos < 128) return ((27 * (pos >> 3)) >> 1) + 7 * ((pos >> 2) & 1) + (pos & 3);
  const int r = pos - 128, pr = r / 11, o = r - 11 * pr;
  const int nt = 2 * pr + (o >= 5), oo = o >= 5 ? o - 5 : o;
  return ((27 * nt) >> 1) + (oo >= 3 ? 8 + oo : 4 + oo);
}
DI unsigned pkmax_abs(unsigned m, unsigned v) {
  us2 a = __builtin_bit_cast(us2, m), b = __builtin_bit_cast(us2, v & 0x7fff7fffu);
  return __builtin_bit_cast(unsigned, __builtin_elementwise_max(a, b));
}

struct RowC { u4 t[9]; };
template <int SGN> DI void rc_F(char* ct, int l, int cb) {
  const float gf = (float)(l >> 4), cf = (float)(16 * cb + (l & 15));
  const float c32 = cf * (SGN / 32.f), a0 = gf * 8.f * c32;
  u4 fr, fi;
#pragma unroll
  for (int jj = 0; jj < 4; ++jj) {
    float r0 = (float)(2 * jj) * c32 + a0, r1 = (float)(2 * jj + 1) * c32 + a0;
    fr[jj] = pk_f16(fcos(r0), fcos(r1));
    fi[jj] = pk_f16(fsin(r0), fsin(r1));
  }
  *(u4*)(ct) = fr; *(u4*)(ct + 1024) = fi;
}
template <int SGN, int OB> DI void rc_G(char* ct, int l) {
  const int r16 = l & 15;
  const float gf = (float)(l >> 4), po4 = (float)(r16 & 1) * 0.25f;
  const float e20 = (float)((r16 >> 1) + 8 * OB) * (-SGN / 20.f), g0 = gf * e20 + po4;
  u4 v;
#pragma unroll
  for (int blk = 0; blk < 4; ++blk) { float r = (float)(4 * blk) * e20 + g0; v[blk] = pk_f16(fcos(r), fsin(r)); }
  *(u4*)(ct + (2 + OB) * 1024) = v;
  float r = 16.f * e20 + g0;
  *(unsigned*)(ct + 5 * 1024 + OB * 4) = pk_f16(fcos(r), fsin(r));
}
template <int SGN, int BLK> DI void rc_T(char* ct, int l, int cb, float tws) {
  const float r = (float)(((l >> 4) + 4 * BLK) * (16 * cb + (l & 15))) * (SGN / 640.f);
  const float tc = fcos(r) * tws, ts = fsin(r) * tws;
  if (BLK < 4) { *(float*)(ct + 6 * 1024 + BLK * 4) = tc; *(float*)(ct + 7 * 1024 + BLK * 4) = ts; }
  else { *(float*)(ct + 5 * 1024 + 12) = tc; *(float*)(ct + 8 * 1024 - l * 12) = ts; }
}
template <int SGN> DI void rc_part(char* ct, int l, int cb, float tws, int part) {
  if (part == 0) rc_F<SGN>(ct, l, cb);
  else if (part == 1) { rc_G<SGN, 0>(ct, l); rc_T<SGN, 0>(ct, l, cb, tws); rc_T<SGN, 1>(ct, l, cb, tws); }
  else if (part == 2) { rc_G<SGN, 1>(ct, l); rc_T<SGN, 2>(ct, l, cb, tws); rc_T<SGN, 3>(ct, l, cb, tws); }
  else { rc_G<SGN, 2>(ct, l); rc_T<SGN, 4>(ct, l, cb, tws); }
}
DI void rc_load(RowC& C, const char* ct, int l) {
#pragma unroll
  for (int j = 0; j < 8; ++j) C.t[j] = *(const u4*)(ct + j * 1024);
  C.t[8][0] = *(const unsigned*)(ct + 8 * 1024 - l * 12);
}
DI void row_stage_a(const RowC& C, const char* lds, int l, int ih, bool ii, u4& T0, u4& T1) {
  const int g = l >> 4, q4 = (l & 15) >> 2, p = l & 3;
  const char* base = lds + (8 * g + q4) * 320 + p * 16 + ih * 8;
  u2 lo[5], hi[5];
#pragma unroll
  for (int blk = 0; blk < 5; ++blk) { lo[blk] = tr_read(base + blk * 64); hi[blk] = tr_read(base + blk * 64 + 4 * 320); }
  T1 = u4{0u, 0u, 0u, 0u};
#pragma unroll
  for (int blk = 0; blk < 5; ++blk) {
    u4 a = {lo[blk][0], lo[blk][1], hi[blk][0], hi[blk][1]};
    f4 z = {0.f, 0.f, 0.f, 0.f};
    f4 pr = mfma_h(a, C.t[0], z);
    f4 pi = mfma_h(a, C.t[1], z);
    float pr0, pr1, pi0, pi1;
    if (ii) { pr0 = pr[2]; pr1 = pr[3]; pi0 = pi[2]; pi1 = pi[3]; } else { pr0 = pr[0]; pr1 = pr[1]; pi0 = pi[0]; pi1 = pi[1]; }
    const float tc = asf(blk < 4 ? C.t[6][blk & 3] : C.t[5][3]), ts = asf(blk < 4 ? C.t[7][blk & 3] : C.t[8][0]);
    float tr = pr0 - pi1, ti = pi0 + pr1;
    unsigned v = pk_f16(tr * tc - ti * ts, ti * tc + tr * ts);
    if (blk < 4) T0[blk & 3] = v; else T1[0] = v;
  }
}

template <bool II> DI void k1_dft(const RowC& C, const char* lin, char* lout, int l, int ih, int cb) {
  u4 T0, T1;
  row_stage_a(C, lin, l, ih, II, T0, T1);
  const int g = l >> 4, cc = 16 * cb + (l & 15);
  char* ob_ = lout + (cc + 64 * g) * 16 + (2 * ih + (int)II) * 4;
#pragma unroll
  for (int ob = 0; ob < 3; ++ob) {
    f4 z = {0.f, 0.f, 0.f, 0.f};
    z = mfma_h(C.t[2 + ob], T0, z);
    u4 ga = {C.t[5][ob], 0u, 0u, 0u};
    z = mfma_h(ga, T1, z);
    *(unsigned*)(ob_ + ob * 4096) = pk_f16(z[0], z[1]);
    *(unsigned*)(ob_ + ob * 4096 + 512) = pk_f16(z[2], z[3]);
  }
}
extern "C" __global__ __launch_bounds__(512) void gf_k1(const u16* __restrict__ x, const u16* __restrict__ wr, const u16* __restrict__ wi,
                                                 const u16* __restrict__ wd, char* __restrict__ ws) {
  __shared__ __attribute__((aligned(16))) char smem[32 * 1296];
  const int b = blockIdx.x, tid = threadIdx.x, l = tid & 63;
  const int w = __builtin_amdgcn_readfirstlane(tid >> 6);
  if (b >= 440) {
    unsigned long long pc; unsigned o1, len;
    asm volatile("s_getpc_b64 %0\n.Lgfpc%=:\n\ts_mov_b32 %1, .Lgfpc%=-gf_k1\n\ts_mov_b32 %2, gf_end-gf_k1" : "=s"(pc), "=s"(o1), "=s"(len));
    const char* base = (const char*)(pc - o1);
    u4 cw[2];
#pragma unroll
    for (int it = 0; it < 2; ++it) {
      unsigned off = (it * 512 + tid) * 64u;
      off = off + 16u <= len ? off : 0u;
      cw[it] = *(const GAS u4*)(base + off);
    }
    asm volatile("" ::"v"(cw[0]), "v"(cw[1]));
    return;
  }
  if (b >= 384) {
    const int ks = b - 384;
    if (ks >= 54) return;
    u4 a[5];
#pragma unroll
    for (int it = 0; it < 5; ++it) {
      int idx = tid + 512 * it, k = idx / 80, seg = idx - k * 80;
      a[it] = *(const u4*)(wd + (size_t)(8 * blk_of_pos(4 * ks + (k >> 3)) + (k & 7)) * DD + 8 * seg);
    }
    SCHED_FENCE();
#pragma unroll
    for (int it = 0; it < 5; ++it) {
      int idx = tid + 512 * it, k = idx / 80, seg = idx - k * 80;
      *(u4*)(smem + k * 1296 + seg * 16) = a[it];
    }
    raw_barrier();
    u4* W2 = (u4*)(ws + OFF_W2) + (size_t)(ks * 40 + 5 * w) * 64 + l;
    const char* base = smem + (8 * (l >> 4) + ((l & 15) >> 2)) * 1296 + (l & 3) * 8 + w * 160;
#pragma unroll
    for (int t = 0; t < 5; ++t) {
      u2 lo = tr_read(base + t * 32), hi = tr_read(base + t * 32 + 4 * 1296);
      WT16(W2, t * 64, (u4{lo[0], lo[1], hi[0], hi[1]}));
    }
    return;
  }
  if (b >= 256) {
    const int task = b - 256, u = task & 7, v = (task >> 3) & 7, hf = task >> 6;
    u4 d[3];
#pragma unroll
    for (int it = 0; it < 3; ++it) {
      int idx = tid + 512 * it; if (idx >= 1280) idx = tid;
      int rho = idx / 20, pc = idx - rho * 20;
      int row = 64 * (4 * v + (rho & 3)) + 8 * u + ((rho >> 2) & 7);
      const u16* src = (rho >= 32 ? wi : wr) + (size_t)row * 321 + 160 * hf + 8 * pc;
      d[it] = *(const u4u*)src;
    }
    unsigned last = 0;
    if (tid < 64) {
      int row = 64 * (4 * v + (tid & 3)) + 8 * u + ((tid >> 2) & 7);
      last = ((tid >= 32 ? wi : wr) + (size_t)row * 321)[320];
    }
    SCHED_FENCE();
    unsigned mx = last & 0x7fffu;
#pragma unroll
    for (int it = 0; it < 3; ++it) {
      int idx = tid + 512 * it; if (idx >= 1280) idx = tid;
      int rho = idx / 20, pc = idx - rho * 20;
      *(u4*)(smem + rho * 336 + pc * 16) = d[it];
#pragma unroll
      for (int e = 0; e < 4; ++e) mx = pkmax_abs(mx, d[it][e]);
    }
    if (tid < 64) *(u16*)(smem + tid * 336 + 320) = (u16)last;
    mx = wave_umax(umax(mx & 0xffffu, mx >> 16));
    if (l == 0) *(unsigned*)(smem + 21504 + w * 4) = mx;
    raw_barrier();
    u4* Wt = (u4*)(ws + OFF_WT);
    const int obase = ((u >> 1) * 2 + (v >> 2)) * 64 + 16 * (v & 3) + 8 * (u & 1);
    const int g = l >> 4, q4 = (l & 15) >> 2, p = l & 3;
#pragma unroll
    for (int rd = 0; rd < 3; ++rd) {
      int job = w + 8 * rd;
      if (job < 20) {
        int cbk = job >> 1, r8 = 4 * (job & 1) + g;
        const char* a = smem + (r8 * 4 + q4) * 336 + (16 * cbk + 4 * p) * 2;
        u2 re = tr_read(a), im = tr_read(a + 32 * 336);
        u4 o = {perm_lo(im[0], re[0]), perm_hi(im[0], re[0]), perm_lo(im[1], re[1]), perm_hi(im[1], re[1])};
        Wt[(160 * hf + 16 * cbk + (l & 15)) * 512 + obase + r8] = o;
      }
    }
    if (hf == 1 && tid < 8) {
      u4 o;
#pragma unroll
      for (int reg = 0; reg < 4; ++reg)
        o[reg] = (unsigned)*(const u16*)(smem + (tid * 4 + reg) * 336 + 320) | ((unsigned)*(const u16*)(smem + (32 + tid * 4 + reg) * 336 + 320) << 16);
      Wt[320 * 512 + obase + tid] = o;
    }
    if (tid == 0) {
      const unsigned* m = (const unsigned*)(smem + 21504);
      ((unsigned*)(ws + OFF_WM))[task] = umax(umax(umax(m[0], m[1]), umax(m[2], m[3])), umax(umax(m[4], m[5]), umax(m[6], m[7])));
    }
    return;
  }
  if (b < 3 && tid >= 256) *(unsigned*)(ws + OFF_CNT + (b * 16 + ((tid - 256) >> 4)) * 256 + (tid & 15) * 4) = 0u;
  const int q = (b & 7) * 32 + (b >> 3);
  const int ih = (w >> 1) & 1, cb = w & 1;
  char* lin = smem;
  char* lout = smem + 10240;
  char* ct = smem + 22528 + cb * 8448 + l * 16;
  RowC C;
  if (w < 4) {
    const int j = l >> 5, li = l & 31;
    const u2* xr = (const u2*)(x + (size_t)srow_of(q, w, j) * DD);
    u2 xv[5];
#pragma unroll
    for (int c = 0; c < 5; ++c) xv[c] = xr[li + 32 * c];
    float v[20];
#pragma unroll
    for (int c = 0; c < 5; ++c) {
      v[4 * c] = bf_lo(xv[c][0]); v[4 * c + 1] = bf_hi(xv[c][0]);
      v[4 * c + 2] = bf_lo(xv[c][1]); v[4 * c + 3] = bf_hi(xv[c][1]);
    }
    float sa = 0.f, sb = 0.f;
#pragma unroll
    for (int t = 0; t < 10; ++t) { sa += v[2 * t]; sb += v[2 * t + 1]; }
    const float mean = half_sum(sa + sb) * (1.f / 640.f);
    sa = 0.f; sb = 0.f;
#pragma unroll
    for (int t = 0; t < 10; ++t) { float d0 = v[2 * t] - mean, d1 = v[2 * t + 1] - mean; sa += d0 * d0; sb += d1 * d1; }
    const float var = half_sum(sa + sb) * (1.f / 640.f);
    const float sd = rbf(__builtin_sqrtf(rbf(rbf(var) + 1e-5f)));
    const float rs = 16.f * __builtin_amdgcn_rcpf(sd);
    const float sh = -rbf(mean) * rs;
    char* wp = lin + (4 * li + 2 * j) * 16 + w * 4;
#pragma unroll
    for (int c = 0; c < 5; ++c) {
      unsigned p01 = pk_f16(v[4 * c] * rs + sh, v[4 * c + 1] * rs + sh);
      unsigned p23 = pk_f16(v[4 * c + 2] * rs + sh, v[4 * c + 3] * rs + sh);
      auto r = __builtin_amdgcn_permlane32_swap(p01, p23, false, false);
      unsigned pa = r[0], pb = r[1];
      *(unsigned*)(wp + c * 2048) = perm_lo(pb, pa);
      *(unsigned*)(wp + c * 2048 + 16) = perm_hi(pb, pa);
    }
    raw_barrier();
    rc_load(C, ct, l);
    k1_dft<false>(C, lin, lout, l, ih, cb);
  } else {
    rc_part<-1>(ct, l, cb, 1.f / 256.f, 2 * ih);
    rc_part<-1>(ct, l, cb, 1.f / 256.f, 2 * ih + 1);
    raw_barrier();
    rc_load(C, ct, l);
    k1_dft<true>(C, lin, lout, l, ih, cb);
  }
  raw_barrier();
  u4* Z1 = (u4*)(ws + OFF_Z1);
  Z1[Z1IDX(q, tid)] = *(const u4*)(lout + tid * 16);
  if (tid < 128) Z1[Z1IDX(q, tid + 512)] = *(const u4*)(lout + (tid + 512) * 16);
}

DI unsigned wmax_exp(u2 m) {
  unsigned eb = wave_umax(umax(m[0], m[1])) >> 7;
  return eb > 251u ? 251u : eb;
}
extern "C" __global__ __launch_bounds__(256) void gf_k2(const u16* __restrict__ wg, const u16* __restrict__ wu, char* __restrict__ ws) {
  __shared__ __attribute__((aligned(16))) char smem[32 * 880];
  const int b = blockIdx.x, tid = threadIdx.x, l = tid & 63;
  const int w = __builtin_amdgcn_readfirstlane(tid >> 6);
  const int g = l >> 4, r16 = l & 15;
  if (b >= 328) {
    const int fg = b & 7, ks = (b - 328) >> 3;
    u4 a[4], c[4];
#pragma unroll
    for (int it = 0; it < 4; ++it) {
      int idx = tid + 256 * it; idx = idx < 864 ? idx : tid;
      int k = idx / 27, seg = idx - k * 27;
      const size_t off = (size_t)(32 * ks + k) * FF + 216 * fg + 8 * seg;
      a[it] = *(const u4*)(wg + off); c[it] = *(const u4*)(wu + off);
    }
    SCHED_FENCE();
#pragma unroll
    for (int it = 0; it < 4; ++it) {
      int idx = tid + 256 * it; idx = idx < 864 ? idx : tid;
      int k = idx / 27, seg = idx - k * 27;
      u4 o0 = {perm_lo(c[it][0], a[it][0]), perm_hi(c[it][0], a[it][0]), perm_lo(c[it][1], a[it][1]), perm_hi(c[it][1], a[it][1])};
      u4 o1 = {perm_lo(c[it][2], a[it][2]), perm_hi(c[it][2], a[it][2]), perm_lo(c[it][3], a[it][3]), perm_hi(c[it][3], a[it][3])};
      *(u4*)(smem + k * 880 + seg * 32) = o0;
      *(u4*)(smem + k * 880 + seg * 32 + 16) = o1;
    }
    raw_barrier();
    u4* W1 = (u4*)(ws + OFF_W1) + (size_t)(ks * 216 + fg * 27 + w) * 64 + l;
    const char* base = smem + (8 * g + (r16 >> 2)) * 880 + (l & 3) * 8 + w * 32;
#pragma unroll
    for (int t = 0; t < 7; ++t) {
      if (t < 6 || w < 3) {
        u2 lo = tr_read(base + t * 128), hi = tr_read(base + t * 128 + 4 * 880);
        WT16(W1, t * 256, (u4{lo[0], lo[1], hi[0], hi[1]}));
      }
    }
    return;
  }
  const int xi = b >> 3;
  int kd = 40 * (b & 7) + xi;
  if (xi == 40) { if ((b & 7) != 7) return; kd = 320; }
  const int kdm = kd == 0 ? 0 : 640 - kd;
  const u4* Z1 = (const u4*)(ws + OFF_Z1);
  const u4 z1 = Z1[Z1IDX(tid, kd)], z2 = Z1[Z1IDX(tid, kdm)];
  u4 wv[2];
  {
    const u4* Wt = (const u4*)(ws + OFF_WT) + kd * 512 + 128 * w + l;
    wv[0] = Wt[0]; wv[1] = Wt[64];
  }
  const u2 wmv = ((const u2*)(ws + OFF_WM))[l];
  SCHED_FENCE();
  u4 M1[4], M2[4], N1[4], N2[4];
  float TC[4][4], TS[4][4];
  {
    const float g4 = (float)(4 * g), k1f = (float)(16 * w + r16);
    const float ka = k1f * (1.f / 64.f), ka0 = g4 * ka;
    const bool odd = r16 & 1;
    const unsigned s1 = (r16 & 2) ? 0x80008000u : 0u;
    const unsigned s1b = (((r16 & 3) + 1) & 2) ? 0x80008000u : 0u;
    const unsigned s2 = odd ? 0x80008000u : 0u;
    const unsigned s3 = ((3 * (r16 & 3)) & 2) ? 0x80008000u : 0u, s3b = ((3 * (r16 & 3) + 1) & 2) ? 0x80008000u : 0u;
#pragma unroll
    for (int jj = 0; jj < 4; ++jj) {
      float r = (float)jj * ka + ka0, c = fcos(r), sn = fsin(r);
      const unsigned a = pk_f16(c, sn), bq = pk_f16(-sn, c);
      const unsigned p = odd ? bq : a, q2 = odd ? a : bq;
      M1[0][jj] = a; M2[0][jj] = bq;
      M1[1][jj] = p ^ s1; M2[1][jj] = q2 ^ s1b;
      M1[2][jj] = a ^ s2; M2[2][jj] = bq ^ s2;
      M1[3][jj] = p ^ s3; M2[3][jj] = q2 ^ s3b;
    }
    const float ro = (float)r16 * (1.f / 32.f), ro0 = g4 * ro;
#pragma unroll
    for (int jj = 0; jj < 4; ++jj) {
      float r = (float)jj * ro + ro0, c = fcos(r), sn = fsin(r);
      const unsigned a = pk_f16(c, sn), bq = pk_f16(-sn, c), sj = (jj & 1) ? 0x80008000u : 0u;
      N1[0][jj] = a; N2[0][jj] = bq;
      N1[1][jj] = a ^ s2; N2[1][jj] = bq ^ s2;
      N1[2][jj] = a ^ sj; N2[2][jj] = bq ^ sj;
      N1[3][jj] = a ^ (s2 ^ sj); N2[3][jj] = bq ^ (s2 ^ sj);
    }
    {
      const float kb = k1f * (1.f / 2048.f);
      const float c1 = fcos(kb), d1 = fsin(kb), c16 = fcos(16.f * kb), d16 = fsin(16.f * kb);
      TC[0][0] = fcos(g4 * kb); TS[0][0] = fsin(g4 * kb);
#pragma unroll
      for (int rg = 1; rg < 4; ++rg) {
        TC[0][rg] = TC[0][rg - 1] * c1 - TS[0][rg - 1] * d1; TS[0][rg] = TS[0][rg - 1] * c1 + TC[0][rg - 1] * d1;
      }
#pragma unroll
      for (int rg = 0; rg < 4; ++rg) { TC[1][rg] = TC[0][rg] * c16 - TS[0][rg] * d16; TS[1][rg] = TS[0][rg] * c16 + TC[0][rg] * d16; }
    }
#pragma unroll
    for (int xx = 0; xx < 2; ++xx) {
      const float nu = (float)(16 * xx + r16) * (1.f / 2048.f), nu0 = ((float)(16 * w) + g4) * nu;
      const float c1 = fcos(nu), d1 = fsin(nu);
      TC[2 + xx][0] = fcos(nu0); TS[2 + xx][0] = fsin(nu0);
#pragma unroll
      for (int rg = 1; rg < 4; ++rg) {
        TC[2 + xx][rg] = TC[2 + xx][rg - 1] * c1 - TS[2 + xx][rg - 1] * d1;
        TS[2 + xx][rg] = TS[2 + xx][rg - 1] * c1 + TC[2 + xx][rg - 1] * d1;
      }
    }
  }
  SCHED_FENCE();
  u4* sa = (u4*)smem;
  u4* ex = (u4*)(smem + 8192);
  {
    u4 A0, A1;
#pragma unroll
    for (int jj = 0; jj < 4; ++jj) {
      h2 p = __builtin_bit_cast(h2, (unsigned)z1[jj]), qq = __builtin_bit_cast(h2, (unsigned)z2[jj]);
      h2 qa = {qq[0], -qq[1]};
      h2 pb = {p[1], -p[0]}, qb = {qq[1], qq[0]};
      A0[jj] = __builtin_bit_cast(unsigned, p + qa);
      A1[jj] = __builtin_bit_cast(unsigned, pb + qb);
    }
    sa[tid] = A0;
    sa[256 + tid] = A1;
  }
  raw_barrier();
  const f4 zero = {0.f, 0.f, 0.f, 0.f};
  f4 Tr[2] = {zero, zero}, Ti[2] = {zero, zero};
#pragma unroll
  for (int t = 0; t < 4; ++t) {
    u4 A0 = sa[64 * t + l], A1 = sa[256 + 64 * t + l];
    Tr[0] = mfma_h(A0, M1[t], Tr[0]); Tr[1] = mfma_h(A1, M1[t], Tr[1]);
    Ti[0] = mfma_h(A0, M2[t], Ti[0]); Ti[1] = mfma_h(A1, M2[t], Ti[1]);
  }
  const unsigned eb = wmax_exp(wmv);
  const float fs = asf((252u - eb) << 23) * (1.f / 256.f);
  u4 X[2];
#pragma unroll
  for (int rb = 0; rb < 2; ++rb)
#pragma unroll
    for (int rg = 0; rg < 4; ++rg) {
      float a = Tr[rb][rg], c = Ti[rb][rg], tc = TC[rb][rg], ts = TS[rb][rg];
      X[rb][rg] = pk_f16(a * tc + c * ts, c * tc - a * ts);
    }
  u4 Y[2];
#pragma unroll
  for (int ob = 0; ob < 2; ++ob) {
    f4 xr = mfma_h(N1[2 * ob], X[0], zero); xr = mfma_h(N1[2 * ob + 1], X[1], xr);
    f4 xim = mfma_h(N2[2 * ob], X[0], zero); xim = mfma_h(N2[2 * ob + 1], X[1], xim);
#pragma unroll
    for (int rg = 0; rg < 4; ++rg) {
      float wr_ = bf_lo(wv[ob][rg]) * fs, wi_ = bf_hi(wv[ob][rg]) * fs;
      float yr = xr[rg] * wr_ - xim[rg] * wi_;
      float yi = xr[rg] * wi_ + xim[rg] * wr_;
      Y[ob][rg] = pk_f16(yi, yr);
    }
  }
#pragma unroll
  for (int cb = 0; cb < 2; ++cb) {
    f4 ui = mfma_h(Y[0], N1[2 * cb], zero); ui = mfma_h(Y[1], N1[2 * cb + 1], ui);
    f4 ur = mfma_h(Y[0], N2[2 * cb], zero); ur = mfma_h(Y[1], N2[2 * cb + 1], ur);
    u4 U;
#pragma unroll
    for (int rg = 0; rg < 4; ++rg) {
      float tc = TC[2 + cb][rg], ts = TS[2 + cb][rg];
      U[rg] = pk_f16(ui[rg] * tc + ur[rg] * ts, ur[rg] * tc - ui[rg] * ts);
    }
    ex[(w * 2 + cb) * 64 + l] = U;
  }
  raw_barrier();
  f4 Vi[2] = {zero, zero}, Vr[2] = {zero, zero};
#pragma unroll
  for (int t = 0; t < 4; ++t)
#pragma unroll
    for (int cb = 0; cb < 2; ++cb) {
      u4 U = ex[(t * 2 + cb) * 64 + l];
      Vi[cb] = mfma_h(M1[t], U, Vi[cb]);
      Vr[cb] = mfma_h(M2[t], U, Vr[cb]);
    }
  u4* Z2 = (u4*)(ws + OFF_Z2);
  const float sg = (kd == 0 || kd == 320) ? 0.f : 1.f;
  u4 o, om;
#pragma unroll
  for (int rg = 0; rg < 4; ++rg) {
    float ia = Vi[0][rg] * sg, ib = Vi[1][rg] * sg;
    o[rg] = pk_f16(Vr[0][rg] - ib, ia + Vr[1][rg]);
    om[rg] = pk_f16(Vr[0][rg] + ib, Vr[1][rg] - ia);
  }
  Z2[Z2IDX(tid, kd)] = o;
  if (kdm != kd) Z2[Z2IDX(tid, kdm)] = om;
}

#define GLDS(src, dst) __builtin_amdgcn_global_load_lds((const GAS void*)(src), (LAS void*)(dst), 16, 0, 0)
#define GLDS_SC1(src, dst) __builtin_amdgcn_global_load_lds((const GAS void*)(src), (LAS void*)(dst), 16, 0, 16)
template <int N> DI void wait_vm() { asm volatile("s_waitcnt vmcnt(%0)" ::"n"(N) : "memory"); }
#define SGB(mask, n) __builtin_amdgcn_sched_group_barrier(mask, n, 0)

DI float geglu(float g, float u) {
  float t = g * g;
  float z = g * (t * (-2.f * 1.4426950408889634f * 0.7978845608028654f * 0.044715f) + (-2.f * 1.4426950408889634f * 0.7978845608028654f));
  float e = __builtin_amdgcn_exp2f(z);
  return (g * u) * __builtin_amdgcn_rcpf(1.f + e);
}
DI void poll_ge16(const char* ws, int cnt) {
  const GAS unsigned* p = (const GAS unsigned*)(ws + OFF_CNT + cnt * 256) + (threadIdx.x & 15);
  for (int spins = 0; spins < (1 << 20); ++spins) {
    unsigned v = __hip_atomic_load(p, __ATOMIC_RELAXED, __HIP_MEMORY_SCOPE_AGENT);
    if (__builtin_amdgcn_ballot_w64(v == 0u) == 0ull) break;
    __builtin_amdgcn_s_sleep(1);
  }
  asm volatile("" ::: "memory");
}
DI void signal(char* ws, int cnt, int slot) {
  __hip_atomic_store((GAS unsigned*)(ws + OFF_CNT + cnt * 256) + slot, 1u, __ATOMIC_RELAXED, __HIP_MEMORY_SCOPE_AGENT);
}

constexpr int G1_STG = 22 * 1024, G1_NB = 7, G1_NS = 20, G1_TAIL = 4, G1_MAIN = G1_NS - G1_TAIL;
constexpr int LDS_TOTAL = 160 * 1024, PA_OFF = 6 * G1_STG;
constexpr int G2_NS = 14, G2_AD = 6;

DI void arrive_n(char* smem, char* ws, int word, int cnt, int l, unsigned last, int slot) {
  if (l == 0) {
    unsigned old = __hip_atomic_fetch_add((LAS unsigned*)(smem + LDS_TOTAL - 64 + word * 4), 1u, __ATOMIC_RELAXED, __HIP_MEMORY_SCOPE_WORKGROUP);
    if (old == last) signal(ws, cnt, slot);
  }
}
template <int NW> DI void k3_loader(char* smem, char* ws, int mt, int nb0, int mt2, int db0, int l, int j) {
  const unsigned loff = l * 16;
  constexpr int NL = NW + 2;
  const char* sA = ws + OFF_HP + (size_t)(8 * mt + j) * 1024 + loff;
  const char* sB = ws + OFF_W1 + (size_t)(nb0 + j) * 1024 + loff;
  auto issueA = [&](int ks) {
    char* d = smem + (ks % G1_NB) * G1_STG + j * 1024;
    GLDS_SC1(sA + (size_t)ks * (128 * 1024), d);
    GLDS_SC1(sA + (size_t)ks * (128 * 1024) + 4096, d + 4096);
  };
  auto issueW = [&](int ks) {
    char* d = smem + (ks % G1_NB) * G1_STG + 8192 + j * 1024;
#pragma unroll
    for (int t = 0; t < NW; ++t) GLDS(sB + (size_t)ks * (216 * 1024) + t * 4096, d + t * 4096);
  };
  int foff[5];
#pragma unroll
  for (int t = 0; t < 5; ++t) { int f = j + 4 * t, kq = f / 5; foff[t] = (kq * 40 + db0 + f - 5 * kq) * 1024; }
  const char* s2 = ws + OFF_W2 + loff;
  auto issue2 = [&](int i) {
    char* d = smem + ((i + 6) % G1_NB) * G1_STG + j * 1024;
#pragma unroll
    for (int t = 0; t < 5; ++t) GLDS(s2 + (size_t)i * (160 * 1024) + foff[t], d + t * 4096);
  };
  raw_barrier();
  issueW(0); issueW(1); issueW(2); issueW(5);
  raw_barrier(); raw_barrier(); raw_barrier();
  issueW(3); issueW(4); issueW(6);
  poll_ge16(ws, mt);
  u4 pk[33];
  auto fetch = [&](int e) {
    const int i = 7 + e / 5, t = e % 5;
    pk[e] = *(const u4*)(s2 + (size_t)i * (160 * 1024) + (e == 32 && j >= 2 ? foff[1] : foff[t]));
  };
  auto park = [&](int i) {
    char* d = smem + ((i + 6) % G1_NB) * G1_STG + j * 1024 + loff;
#pragma unroll
    for (int t = 0; t < (i < 13 ? 5 : 3); ++t) *(u4*)(d + t * 4096) = pk[(i - 7) * 5 + t];
  };
#pragma unroll
  for (int s = 0; s < 7; ++s) issueA(s);
  wait_vm<12>();
  raw_barrier();
  static_for<G1_MAIN - 1>([&](auto S_) {
    constexpr int s = decltype(S_)::value;
    constexpr int n = s <= 5 ? 2 * (5 - s) + NL * s : (s <= 13 ? NL * ((s + 6 < 19 ? s + 6 : 19) - (s + 1)) : NL * (19 - (s + 1)) + 5 * (s - 13));
    constexpr int lo = s - 6 > 0 ? s - 6 : 0, hi = s - 1 < 10 ? s - 1 : 10;
    wait_vm<n + (hi >= lo ? 3 * (hi - lo + 1) : 0)>();
    raw_barrier();
    if constexpr (s + G1_NB < G1_NS) { issueA(s + G1_NB); issueW(s + G1_NB); }
    else issue2(s - 13);
    if constexpr (s <= 10) { SCHED_FENCE(); fetch(3 * s); fetch(3 * s + 1); fetch(3 * s + 2); SCHED_FENCE(); }
  });
  wait_vm<10>();
  raw_barrier();
  issue2(2);
  raw_barrier();
  issue2(3); issue2(4); issue2(5); issue2(6);
  wait_vm<0>();
  raw_barrier();
  if (j == 0) poll_ge16(ws, 32 + (mt2 >> 1));
  raw_barrier();
  park(7); park(8); park(9);
  raw_barrier();
  park(10); park(11);
  raw_barrier();
  park(12); park(13);
  raw_barrier();
}
DI void arrive(char* smem, char* ws, int word, int cnt, int l, int slot) { arrive_n(smem, ws, word, cnt, l, 7u, slot); }

extern "C" __global__ __launch_bounds__(768, 3) void gf_k3(const u16* __restrict__ x, u16* __restrict__ out, char* __restrict__ ws) {
  extern __shared__ __attribute__((aligned(16))) char smem[];
  const int b = blockIdx.x, tid = threadIdx.x, l = tid & 63;
  const int w = __builtin_amdgcn_readfirstlane(tid >> 6);
  const int xcd = b & 7, idx = b >> 3;
  const int mt = 8 * (xcd & 1) + (idx & 7), nt = 4 * (xcd >> 1) + (idx >> 3);
  const int mt2 = 16 * (xcd & 1) + 8 * ((xcd >> 1) & 1) + (idx & 7), nt2 = 4 * (xcd >> 2) + (idx >> 3);
  const int nb0 = (27 * nt) >> 1, mb0 = 8 * mt, db0 = 5 * nt2;
  const unsigned loff = l * 16;
  if (w >= 8) {
    if (w < 10) k3_loader<4>(smem, ws, mt, nb0, mt2, db0, l, w - 8); else k3_loader<3>(smem, ws, mt, nb0, mt2, db0, l, w - 8);
    return;
  }
  const int g = l >> 4, r16 = l & 15;
  const auto rsrc = __builtin_amdgcn_make_buffer_rsrc(ws, 0, (int)WS_USED, 0x00020000);
  {
    char* pa = smem + PA_OFF;
    const int q = 16 * mt + nt;
    const int ih = (w >> 1) & 1, cb = w & 1, ii = w >> 2, j = l & 1;
    const u4* Z2 = (const u4*)(ws + OFF_Z2);
    const u4 zv0 = Z2[Z2IDX(q, tid)];
    const u4 zv1 = Z2[Z2IDX(q, 512 + (tid & 127))];
    const int e0 = r16 >> 1;
    const bool v2 = e0 < 4;
    const size_t roff = (size_t)srow_of(q, 2 * ih + ii, j) * DD + 16 * cb + 4 * g + 32 * e0;
    u2 xv[3];
    xv[0] = *(const u2*)(x + roff); xv[1] = *(const u2*)(x + roff + 256); xv[2] = *(const u2*)(x + roff + (v2 ? 512 : 0));
    const u2 wmv = ((const u2*)(ws + OFF_WM))[l];
    SCHED_FENCE();
    char* ct = pa + 10368 + cb * 8448 + l * 16;
    rc_part<1>(ct, l, cb, 1.f / 32.f, w >> 1);
    SCHED_FENCE();
    *(u4*)(pa + tid * 16) = zv0;
    if (tid < 128) *(u4*)(pa + (512 + tid) * 16) = zv1;
    if (tid < 3) *(unsigned*)(smem + LDS_TOTAL - 64 + tid * 4) = 0u;
    raw_barrier();
    RowC C;
    rc_load(C, ct, l);
    u4 T0, T1;
    if (ii) row_stage_a(C, pa, l, ih, true, T0, T1); else row_stage_a(C, pa, l, ih, false, T0, T1);
    const unsigned eb = wmax_exp(wmv);
    const float ysc = asf((eb + 2u) << 23) * 0.05f;
    float h[12];
    float s = 0.f;
    const int mp = 8 * q + 2 * (2 * ih + ii) + j;
    char* hs = smem + 4 * G1_STG - 2048;
    char* ns = smem + 5 * G1_STG - 2048;
    const int r8 = mp & 7;
#pragma unroll
    for (int ob = 0; ob < 3; ++ob) {
      f4 z = {0.f, 0.f, 0.f, 0.f};
      z = mfma_h(T0, C.t[2 + ob], z);
      u4 gb = {C.t[5][ob], 0u, 0u, 0u};
      z = mfma_h(T1, gb, z);
      unsigned y01 = pk_bf16(z[0] * ysc, z[1] * ysc), y23 = pk_bf16(z[2] * ysc, z[3] * ysc);
      unsigned h01 = pk_bf16(bf_lo(xv[ob][0]) + bf_lo(y01), bf_hi(xv[ob][0]) + bf_hi(y01));
      unsigned h23 = pk_bf16(bf_lo(xv[ob][1]) + bf_lo(y23), bf_hi(xv[ob][1]) + bf_hi(y23));
      if (ob == 2) { h01 = v2 ? h01 : 0u; h23 = v2 ? h23 : 0u; }
      if (ob < 2 || v2) *(u2*)(hs + ((2 * e0 + cb + 16 * ob) * 8 + r8) * 32 + g * 8) = u2{h01, h23};
      h[4 * ob] = bf_lo(h01); h[4 * ob + 1] = bf_hi(h01); h[4 * ob + 2] = bf_lo(h23); h[4 * ob + 3] = bf_hi(h23);
      s += (h[4 * ob] + h[4 * ob + 1]) + (h[4 * ob + 2] + h[4 * ob + 3]);
    }
    const float mloc = parity_sum(s) * (1.f / 320.f);
    float s2 = 0.f, s3 = 0.f;
#pragma unroll
    for (int t = 0; t < 8; ++t) { float d = h[t] - mloc; s2 += d * d; }
#pragma unroll
    for (int t = 8; t < 12; ++t) { float d = h[t] - mloc; s3 += d * d; }
    const float m2loc = parity_sum(v2 ? s2 + s3 : s2);
    f2* ex = (f2*)(pa + 10240);
    if (l < 2) ex[w * 2 + l] = f2{mloc, m2loc};
    raw_barrier();
    {
      const int hb = (int)OFF_HH + 8 * q * 32;
      __builtin_amdgcn_raw_buffer_store_b128(*(const u4*)(hs + tid * 16), rsrc, (tid >> 4) * (2048 * 32) + (tid & 15) * 16, hb, 16);
      if (tid < 128) __builtin_amdgcn_raw_buffer_store_b128(*(const u4*)(hs + 8192 + tid * 16), rsrc, (32 + (tid >> 4)) * (2048 * 32) + (tid & 15) * 16, hb, 16);
    }
    const f2 o = ex[(w ^ 1) * 2 + j];
    const float mean = 0.5f * (mloc + o[0]);
    const float dl = mloc - o[0];
    const float var = (m2loc + o[1] + dl * dl * 160.f) * (1.f / 640.f);
    const float mb = rbf(mean);
    const float sd = rbf(__builtin_sqrtf(rbf(rbf(var) + 1e-5f)));
    const float rs = __builtin_amdgcn_rcpf(sd);
#pragma unroll
    for (int ob = 0; ob < 3; ++ob) {
      unsigned d01 = pk_bf16(h[4 * ob] - mb, h[4 * ob + 1] - mb);
      unsigned d23 = pk_bf16(h[4 * ob + 2] - mb, h[4 * ob + 3] - mb);
      u2 st = {pk_bf16(bf_lo(d01) * rs, bf_hi(d01) * rs), pk_bf16(bf_lo(d23) * rs, bf_hi(d23) * rs)};
      if (ob < 2 || v2) *(u2*)(ns + (((8 * ob + e0) * 4 + 2 * cb + (g >> 1)) * 8 + r8) * 16 + (g & 1) * 8) = st;
    }
    raw_barrier();
    {
      const int nbase = (int)OFF_HP + (q >> 1) * 1024 + (q & 1) * 128;
      __builtin_amdgcn_raw_buffer_store_b128(*(const u4*)(ns + tid * 16), rsrc, (tid >> 5) * (128 * 1024) + ((tid >> 3) & 3) * 256 + (tid & 7) * 16, nbase, 16);
      if (tid < 128) __builtin_amdgcn_raw_buffer_store_b128(*(const u4*)(ns + 8192 + tid * 16), rsrc, (16 + (tid >> 5)) * (128 * 1024) + ((tid >> 3) & 3) * 256 + (tid & 7) * 16, nbase, 16);
    }
    wait_vm<0>();
    raw_barrier();
    if (tid == 0) signal(ws, mt, nt);
  }
  {
    int lb = threadIdx.x & 63;
    asm volatile("" : "+v"(lb));
    const unsigned loff = lb * 16;
    const int wm = w >> 1, wn = w & 1;
    const char* rdA = smem + (2 * wm) * 1024 + loff;
    const char* rdB = smem + (8 + 7 * wn) * 1024 + loff;
    f4 acc[7][2];
#pragma unroll
    for (int i = 0; i < 7; ++i) { acc[i][0] = f4{0.f, 0.f, 0.f, 0.f}; acc[i][1] = f4{0.f, 0.f, 0.f, 0.f}; }
    {
      u4 af[2][2], wf[2][7];
      raw_barrier();
#pragma unroll
      for (int i = 0; i < 2; ++i) af[0][i] = *(const u4*)(rdA + i * 1024);
#pragma unroll
      for (int i = 0; i < 7; ++i) wf[0][i] = *(const u4*)(rdB + i * 1024);
      static_for<G1_MAIN>([&](auto S_) {
        constexpr int s = decltype(S_)::value;
        constexpr int cur = s & 1, nxt = cur ^ 1;
        constexpr bool more = s + 1 < G1_MAIN;
        if constexpr (more) {
          raw_barrier();
          constexpr int bo = ((s + 1) % G1_NB) * G1_STG;
#pragma unroll
          for (int i = 0; i < 2; ++i) af[nxt][i] = *(const u4*)(rdA + bo + i * 1024);
#pragma unroll
          for (int i = 0; i < 7; ++i) wf[nxt][i] = *(const u4*)(rdB + bo + i * 1024);
        }
#pragma unroll
        for (int i = 0; i < 7; ++i)
#pragma unroll
          for (int m = 0; m < 2; ++m) acc[i][m] = mfma_b(wf[cur][i], af[cur][m], acc[i][m]);
        if constexpr (more) {
#pragma unroll
          for (int i = 0; i < 9; ++i) { SGB(0x8, 1); SGB(0x100, 1); }
          SGB(0x8, 14);
        }
        SCHED_FENCE();
      });
    }
    raw_barrier();
    int lz = threadIdx.x & 63;
    asm volatile("" : "+v"(lz));
    const int g = lz >> 4, r16 = lz & 15;
    u4 af[G1_TAIL][2], wf[2][G1_TAIL];
#pragma unroll
    for (int t = 0; t < G1_TAIL; ++t) {
      const int bo = ((G1_MAIN + t) % G1_NB) * G1_STG;
      wf[0][t] = *(const u4*)(rdB + bo);
      af[t][0] = *(const u4*)(rdA + bo); af[t][1] = *(const u4*)(rdA + bo + 1024);
    }
    const int aoff = (int)OFF_AP + (mb0 + 2 * wm) * 1024 + r16 * 16 + 4 * g;
    auto epi = [&](int i) {
      const int pos = i < 4 ? 8 * nt + 4 * wn + i : 128 + 11 * (nt >> 1) + 5 * (nt & 1) + 3 * wn + (i - 4);
      const int so = (pos >> 2) * (128 * 1024) + (pos & 3) * 256;
      if (i == 6 && wn == 1 && !(nt & 1)) return;
#pragma unroll
      for (int m = 0; m < 2; ++m)
        __builtin_amdgcn_raw_buffer_store_b32(pk_bf16(geglu(acc[i][m][0], acc[i][m][1]), geglu(acc[i][m][2], acc[i][m][3])), rsrc,
                                              aoff + m * 1024, so, 16);
    };
    static_for<7>([&](auto I_) {
      constexpr int i = decltype(I_)::value;
      if constexpr (i + 1 < 7) {
#pragma unroll
        for (int t = 0; t < G1_TAIL; ++t) wf[(i + 1) & 1][t] = *(const u4*)(rdB + ((G1_MAIN + t) % G1_NB) * G1_STG + (i + 1) * 1024);
      }
#pragma unroll
      for (int t = 0; t < G1_TAIL; ++t)
#pragma unroll
        for (int m = 0; m < 2; ++m) acc[i][m] = mfma_b(wf[i & 1][t], af[t][m], acc[i][m]);
      if constexpr (i > 0) {
        epi(i - 1);
#pragma unroll
        for (int t = 0; t < 8; ++t) { SGB(0x8, 1); if (t < 4 && i + 1 < 7) SGB(0x100, 1); SGB(0x2, 4); SGB(0x400, 1); }
      }
      SCHED_FENCE();
      if constexpr (i == 5) { wait_vm<2 * (5 - 4)>(); arrive(smem, ws, 0, 16 + mt, lz, nt); }
    });
    epi(6);
    raw_barrier();
    wait_vm<0>();
    arrive(smem, ws, 1, 32 + mt, lz, nt);
    if (w == 0) poll_ge16(ws, 16 + (mt2 >> 1));
  }
  {
    int l = threadIdx.x & 63;
    asm volatile("" : "+v"(l));
    const int g = l >> 4, r16 = l & 15;
    const unsigned loff = l * 16;
    const int kq = w & 3, mh = w >> 2;
    const int mb2 = 4 * mt2 + 2 * mh;
    f4 acc[2][5];
#pragma unroll
    for (int m = 0; m < 2; ++m)
#pragma unroll
      for (int n = 0; n < 5; ++n) acc[m][n] = f4{0.f, 0.f, 0.f, 0.f};
    const char* rdW = smem + kq * 5120 + loff;
    const int abase = (int)OFF_AP + (kq * 128 + mb2) * 1024;
    u4 fa[G2_AD][2], wf[2][5];
    auto loadA = [&](int i, u4 (&a)[2]) {
      const int so = abase + (i < 13 ? i : (kq < 2 ? 13 : 12)) * (512 * 1024);
      a[0] = __builtin_amdgcn_raw_buffer_load_b128(rsrc, loff, so, 16);
      a[1] = __builtin_amdgcn_raw_buffer_load_b128(rsrc, loff + 1024, so, 16);
    };
    raw_barrier();
    static_for<G2_AD>([&](auto I_) { constexpr int i = decltype(I_)::value; loadA(i, fa[i]); SCHED_FENCE(); });
    u2 hv[3];
#pragma unroll
    for (int n = 0; n < 5; ++n) wf[0][n] = *(const u4*)(rdW + 6 * G1_STG + n * 1024);
    SCHED_FENCE();
    static_for<G2_NS - 1>([&](auto I_) {
      constexpr int i = decltype(I_)::value;
      constexpr int cur = i & 1, nxt = cur ^ 1;
      if constexpr (i == 2 || i == 4 || i == 6 || i == 9) raw_barrier();
#pragma unroll
      for (int n = 0; n < 5; ++n) wf[nxt][n] = *(const u4*)(rdW + ((i + 7) % G1_NB) * G1_STG + n * 1024);
#pragma unroll
      for (int m = 0; m < 2; ++m)
#pragma unroll
        for (int n = 0; n < 5; ++n) acc[m][n] = mfma_b(wf[cur][n], fa[i % G2_AD][m], acc[m][n]);
      if constexpr (i + G2_AD < G2_NS) loadA(i + G2_AD, fa[i % G2_AD]);
      if constexpr (i == 9) {
#pragma unroll
        for (int u = 0; u < 3; ++u) {
          int t = kq + 4 * u; t = t < 10 ? t : kq;
          const int m = t >= 5, n = t - 5 * m;
          hv[u] = __builtin_amdgcn_raw_buffer_load_b64(rsrc, r16 * 32 + g * 8, (int)OFF_HH + ((db0 + n) * 2048 + 16 * (mb2 + m)) * 32, 16);
        }
      }
      SGB(0x100, 5); SGB(0x8, 10); SGB(0x20, 3);
      SCHED_FENCE();
    });
    if (kq < 2) {
#pragma unroll
      for (int m = 0; m < 2; ++m)
#pragma unroll
        for (int n = 0; n < 5; ++n) acc[m][n] = mfma_b(wf[1][n], fa[13 % G2_AD][m], acc[m][n]);
    }
    raw_barrier();
    f4* red = (f4*)smem + mh * (40 * 64) + l;
#pragma unroll
    for (int t = 0; t < 10; ++t) red[(t * 4 + kq) * 64] = acc[t / 5][t % 5];
    raw_barrier();
#pragma unroll
    for (int u = 0; u < 3; ++u) {
      const int t = kq + 4 * u < 10 ? kq + 4 * u : kq, m = t >= 5, n = t - 5 * m;
      const f4* sp = red + t * 256;
      f4 o = (sp[0] + sp[64]) + (sp[128] + sp[192]);
      unsigned o01 = pk_bf16(o[0], o[1]), o23 = pk_bf16(o[2], o[3]);
      const int mp = 16 * (mb2 + m) + r16;
      u16* op = out + (size_t)srow_of(mp >> 3, (mp >> 1) & 3, mp & 1) * DD + 16 * (db0 + n) + 4 * g;
      *(u2*)op = u2{pk_bf16(bf_lo(hv[u][0]) + bf_lo(o01), bf_hi(hv[u][0]) + bf_hi(o01)),
                    pk_bf16(bf_lo(hv[u][1]) + bf_lo(o23), bf_hi(hv[u][1]) + bf_hi(o23))};
    }
  }
}

extern "C" __device__ __attribute__((used, noinline)) void gf_end() {}

#if defined(__HIP_DEVICE_COMPILE__)
#pragma clang attribute pop
#endif

extern "C" void kernel_launch(void* const* d_in, const int* in_sizes, int n_in, void* d_out, int out_size, void* d_ws,
                              size_t ws_size, hipStream_t stream) {
  const u16* x = (const u16*)d_in[0];
  const u16* wr = (const u16*)d_in[1];
  const u16* wi = (const u16*)d_in[2];
  const u16* wg = (const u16*)d_in[4];
  const u16* wu = (const u16*)d_in[5];
  const u16* wd = (const u16*)d_in[6];
  u16* out = (u16*)d_out;
  char* ws = (char*)d_ws;
  (void)hipFuncSetAttribute((const void*)gf_k3, hipFuncAttributeMaxDynamicSharedMemorySize, LDS_TOTAL);
  gf_k1<<<448, 512, 0, stream>>>(x, wr, wi, wd, ws);
  gf_k2<<<328 + 160, 256, 0, stream>>>(wg, wu, ws);
  gf_k3<<<256, 768, LDS_TOTAL, stream>>>(x, out, ws);
}
```

```cpp
#include <hip/hip_runtime.h>
#include <hip/hip_bf16.h>
#include <stdint.h>
#include <utility>

#if defined(__HIP_DEVICE_COMPILE__)
#pragma clang attribute push(__attribute__((target("no-packed-fp32-ops"))), apply_to = function)
#endif

typedef _Float16 h8 __attribute__((ext_vector_type(8)));
typedef _Float16 h2 __attribute__((ext_vector_type(2)));
typedef float f4 __attribute__((ext_vector_type(4)));
typedef float f2 __attribute__((ext_vector_type(2)));
typedef short s8 __attribute__((ext_vector_type(8)));
typedef short s4 __attribute__((ext_vector_type(4)));
typedef unsigned u4 __attribute__((ext_vector_type(4)));
typedef unsigned u4u __attribute__((ext_vector_type(4), aligned(2)));
typedef unsigned u2 __attribute__((ext_vector_type(2)));
typedef unsigned short us2 __attribute__((ext_vector_type(2)));
typedef __bf16 bf2 __attribute__((ext_vector_type(2)));
typedef unsigned short u16;
#define DI __device__ __forceinline__
#define LAS __attribute__((address_space(3)))
#define GAS __attribute__((address_space(1)))

constexpr int DD = 640, FF = 1728;

constexpr size_t OFF_Z1 = 0;
constexpr size_t OFF_Z2 = OFF_Z1 + (size_t)640 * 256 * 16;
constexpr size_t OFF_WT = OFF_Z2 + (size_t)640 * 256 * 16;
constexpr size_t OFF_WM = OFF_WT + (size_t)321 * 512 * 16;
constexpr size_t OFF_HP = OFF_WM + 1024;
constexpr size_t OFF_W1 = OFF_HP + (size_t)20 * 128 * 1024;
constexpr size_t OFF_AP = OFF_W1 + (size_t)20 * 216 * 1024;
constexpr size_t OFF_W2 = OFF_AP + (size_t)54 * 128 * 1024;
constexpr size_t OFF_HH = OFF_W2 + (size_t)54 * 40 * 1024;
constexpr size_t OFF_CNT = OFF_HH + (size_t)40 * 2048 * 32;
constexpr size_t WS_USED = OFF_CNT + 48 * 256;

#define SCHED_FENCE() __builtin_amdgcn_sched_barrier(0)
#define WT16(base, idx, v) __builtin_amdgcn_raw_buffer_store_b128((v), __builtin_amdgcn_make_buffer_rsrc(ws, 0, (int)WS_USED, 0x00020000), (int)((const char*)&(base)[idx] - ws), 0, 16)
#define Z1IDX(q, k) ((((k) >> 2) * 256 + (q)) * 4 + ((k) & 3))
#define Z2IDX(q, k) ((q) * 640 + (k))

template <class Fn, int... I>
DI void static_for_impl(Fn&& f, std::integer_sequence<int, I...>) { (f(std::integral_constant<int, I>{}), ...); }
template <int N, class Fn>
DI void static_for(Fn&& f) { static_for_impl(f, std::make_integer_sequence<int, N>{}); }

DI unsigned pk_bf16(float a, float b) { f2 v = {a, b}; return __builtin_bit_cast(unsigned, __builtin_convertvector(v, bf2)); }
DI unsigned pk_f16(float a, float b) { f2 v = {a, b}; return __builtin_bit_cast(unsigned, __builtin_convertvector(v, h2)); }
DI float bf_lo(unsigned u) { return __builtin_bit_cast(float, u << 16); }
DI float bf_hi(unsigned u) { return __builtin_bit_cast(float, u & 0xffff0000u); }
DI float rbf(float a) { return bf_hi(pk_bf16(0.f, a)); }
DI float asf(unsigned u) { return __builtin_bit_cast(float, u); }
DI unsigned asu(float f) { return __builtin_bit_cast(unsigned, f); }
DI float fsin(float r) { return __builtin_amdgcn_sinf(r); }
DI float fcos(float r) { return __builtin_amdgcn_cosf(r); }
DI f4 mfma_h(u4 a, u4 b, f4 c) { return __builtin_amdgcn_mfma_f32_16x16x32_f16(__builtin_bit_cast(h8, a), __builtin_bit_cast(h8, b), c, 0, 0, 0); }
DI f4 mfma_b(u4 a, u4 b, f4 c) { return __builtin_amdgcn_mfma_f32_16x16x32_bf16(__builtin_bit_cast(s8, a), __builtin_bit_cast(s8, b), c, 0, 0, 0); }
DI u2 tr_read(const char* p) { return __builtin_bit_cast(u2, __builtin_amdgcn_ds_read_tr16_b64_v4i16((LAS s4*)p)); }
DI unsigned perm_lo(unsigned hi, unsigned lo) { return __builtin_amdgcn_perm(hi, lo, 0x05040100u); }
DI unsigned perm_hi(unsigned hi, unsigned lo) { return __builtin_amdgcn_perm(hi, lo, 0x07060302u); }

template <int CTRL> DI unsigned dppu(unsigned v) { return (unsigned)__builtin_amdgcn_update_dpp(0, (int)v, CTRL, 0xf, 0xf, true); }
template <int CTRL> DI float dpp(float v) { return asf(dppu<CTRL>(__builtin_bit_cast(unsigned, v))); }
DI float swap16_sum(float v) {
  unsigned u = __builtin_bit_cast(unsigned, v);
  auto r = __builtin_amdgcn_permlane16_swap(u, u, false, false);
  return asf((unsigned)r[0]) + asf((unsigned)r[1]);
}
DI float swap32_sum(float v) {
  unsigned u = __builtin_bit_cast(unsigned, v);
  auto r = __builtin_amdgcn_permlane32_swap(u, u, false, false);
  return asf((unsigned)r[0]) + asf((unsigned)r[1]);
}
DI float half_sum(float v) {
  v += dpp<0x121>(v); v += dpp<0x122>(v); v += dpp<0x124>(v); v += dpp<0x128>(v);
  return swap16_sum(v);
}
DI float parity_sum(float v) {
  v += dpp<0x122>(v); v += dpp<0x124>(v); v += dpp<0x128>(v);
  v = swap16_sum(v);
  return swap32_sum(v);
}
DI unsigned umax(unsigned a, unsigned b) { return a > b ? a : b; }
DI unsigned wave_umax(unsigned v) {
  v = umax(v, dppu<0x121>(v)); v = umax(v, dppu<0x122>(v)); v = umax(v, dppu<0x124>(v)); v = umax(v, dppu<0x128>(v));
  auto r = __builtin_amdgcn_permlane16_swap(v, v, false, false);
  v = umax((unsigned)r[0], (unsigned)r[1]);
  auto r2 = __builtin_amdgcn_permlane32_swap(v, v, false, false);
  return umax((unsigned)r2[0], (unsigned)r2[1]);
}
DI void raw_barrier() {
  asm volatile("s_waitcnt lgkmcnt(0)" ::: "memory");
  __builtin_amdgcn_s_barrier();
  asm volatile("" ::: "memory");
}
DI int srow_of(int q, int i, int j) { return 32 * (4 * (q >> 4) + i) + (q & 15) + 16 * j; }
DI int blk_of_pos(int pos) {
  if (pos < 128) return ((27 * (pos >> 3)) >> 1) + 7 * ((pos >> 2) & 1) + (pos & 3);
  const int r = pos - 128, pr = r / 11, o = r - 11 * pr;
  const int nt = 2 * pr + (o >= 5), oo = o >= 5 ? o - 5 : o;
  return ((27 * nt) >> 1) + (oo >= 3 ? 8 + oo : 4 + oo);
}
DI unsigned pkmax_abs(unsigned m, unsigned v) {
  us2 a = __builtin_bit_cast(us2, m), b = __builtin_bit_cast(us2, v & 0x7fff7fffu);
  return __builtin_bit_cast(unsigned, __builtin_elementwise_max(a, b));
}

struct RowC { u4 t[9]; };
template <int SGN> DI void rc_F(char* ct, int l, int cb) {
  const float gf = (float)(l >> 4), cf = (float)(16 * cb + (l & 15));
  const float c32 = cf * (SGN / 32.f), a0 = gf * 8.f * c32;
  u4 fr, fi;
#pragma unroll
  for (int jj = 0; jj < 4; ++jj) {
    float r0 = (float)(2 * jj) * c32 + a0, r1 = (float)(2 * jj + 1) * c32 + a0;
    fr[jj] = pk_f16(fcos(r0), fcos(r1));
    fi[jj] = pk_f16(fsin(r0), fsin(r1));
  }
  *(u4*)(ct) = fr; *(u4*)(ct + 1024) = fi;
}
template <int SGN, int OB> DI void rc_G(char* ct, int l) {
  const int r16 = l & 15;
  const float gf = (float)(l >> 4), po4 = (float)(r16 & 1) * 0.25f;
  const float e20 = (float)((r16 >> 1) + 8 * OB) * (-SGN / 20.f), g0 = gf * e20 + po4;
  u4 v;
#pragma unroll
  for (int blk = 0; blk < 4; ++blk) { float r = (float)(4 * blk) * e20 + g0; v[blk] = pk_f16(fcos(r), fsin(r)); }
  *(u4*)(ct + (2 + OB) * 1024) = v;
  float r = 16.f * e20 + g0;
  *(unsigned*)(ct + 5 * 1024 + OB * 4) = pk_f16(fcos(r), fsin(r));
}
template <int SGN, int BLK> DI void rc_T(char* ct, int l, int cb, float tws) {
  const float r = (float)(((l >> 4) + 4 * BLK) * (16 * cb + (l & 15))) * (SGN / 640.f);
  const float tc = fcos(r) * tws, ts = fsin(r) * tws;
  if (BLK < 4) { *(float*)(ct + 6 * 1024 + BLK * 4) = tc; *(float*)(ct + 7 * 1024 + BLK * 4) = ts; }
  else { *(float*)(ct + 5 * 1024 + 12) = tc; *(float*)(ct + 8 * 1024 - l * 12) = ts; }
}
template <int SGN> DI void rc_part(char* ct, int l, int cb, float tws, int part) {
  if (part == 0) rc_F<SGN>(ct, l, cb);
  else if (part == 1) { rc_G<SGN, 0>(ct, l); rc_T<SGN, 0>(ct, l, cb, tws); rc_T<SGN, 1>(ct, l, cb, tws); }
  else if (part == 2) { rc_G<SGN, 1>(ct, l); rc_T<SGN, 2>(ct, l, cb, tws); rc_T<SGN, 3>(ct, l, cb, tws); }
  else { rc_G<SGN, 2>(ct, l); rc_T<SGN, 4>(ct, l, cb, tws); }
}
DI void rc_load(RowC& C, const char* ct, int l) {
#pragma unroll
  for (int j = 0; j < 8; ++j) C.t[j] = *(const u4*)(ct + j * 1024);
  C.t[8][0] = *(const unsigned*)(ct + 8 * 1024 - l * 12);
}
DI void row_stage_a(const RowC& C, const char* lds, int l, int ih, bool ii, u4& T0, u4& T1) {
  const int g = l >> 4, q4 = (l & 15) >> 2, p = l & 3;
  const char* base = lds + (8 * g + q4) * 320 + p * 16 + ih * 8;
  u2 lo[5], hi[5];
#pragma unroll
  for (int blk = 0; blk < 5; ++blk) { lo[blk] = tr_read(base + blk * 64); hi[blk] = tr_read(base + blk * 64 + 4 * 320); }
  T1 = u4{0u, 0u, 0u, 0u};
#pragma unroll
  for (int blk = 0; blk < 5; ++blk) {
    u4 a = {lo[blk][0], lo[blk][1], hi[blk][0], hi[blk][1]};
    f4 z = {0.f, 0.f, 0.f, 0.f};
    f4 pr = mfma_h(a, C.t[0], z);
    f4 pi = mfma_h(a, C.t[1], z);
    float pr0, pr1, pi0, pi1;
    if (ii) { pr0 = pr[2]; pr1 = pr[3]; pi0 = pi[2]; pi1 = pi[3]; } else { pr0 = pr[0]; pr1 = pr[1]; pi0 = pi[0]; pi1 = pi[1]; }
    const float tc = asf(blk < 4 ? C.t[6][blk & 3] : C.t[5][3]), ts = asf(blk < 4 ? C.t[7][blk & 3] : C.t[8][0]);
    float tr = pr0 - pi1, ti = pi0 + pr1;
    unsigned v = pk_f16(tr * tc - ti * ts, ti * tc + tr * ts);
    if (blk < 4) T0[blk & 3] = v; else T1[0] = v;
  }
}

template <bool II> DI void k1_dft(const RowC& C, const char* lin, char* lout, int l, int ih, int cb) {
  u4 T0, T1;
  row_stage_a(C, lin, l, ih, II, T0, T1);
  const int g = l >> 4, cc = 16 * cb + (l & 15);
  char* ob_ = lout + (cc + 64 * g) * 16 + (2 * ih + (int)II) * 4;
#pragma unroll
  for (int ob = 0; ob < 3; ++ob) {
    f4 z = {0.f, 0.f, 0.f, 0.f};
    z = mfma_h(C.t[2 + ob], T0, z);
    u4 ga = {C.t[5][ob], 0u, 0u, 0u};
    z = mfma_h(ga, T1, z);
    *(unsigned*)(ob_ + ob * 4096) = pk_f16(z[0], z[1]);
    *(unsigned*)(ob_ + ob * 4096 + 512) = pk_f16(z[2], z[3]);
  }
}
extern "C" __global__ __launch_bounds__(512) void gf_k1(const u16* __restrict__ x, const u16* __restrict__ wr, const u16* __restrict__ wi,
                                                 const u16* __restrict__ wd, char* __restrict__ ws) {
  __shared__ __attribute__((aligned(16))) char smem[32 * 1296];
  const int b = blockIdx.x, tid = threadIdx.x, l = tid & 63;
  const int w = __builtin_amdgcn_readfirstlane(tid >> 6);
  if (b >= 440) {
    unsigned long long pc; unsigned o1, len;
    asm volatile("s_getpc_b64 %0\n.Lgfpc%=:\n\ts_mov_b32 %1, .Lgfpc%=-gf_k1\n\ts_mov_b32 %2, gf_end-gf_k1" : "=s"(pc), "=s"(o1), "=s"(len));
    const char* base = (const char*)(pc - o1);
    u4 cw[2];
#pragma unroll
    for (int it = 0; it < 2; ++it) {
      unsigned off = (it * 512 + tid) * 64u;
      off = off + 16u <= len ? off : 0u;
      cw[it] = *(const GAS u4*)(base + off);
    }
    asm volatile("" ::"v"(cw[0]), "v"(cw[1]));
    return;
  }
  if (b >= 384) {
    const int ks = b - 384;
    if (ks >= 54) return;
    u4 a[5];
#pragma unroll
    for (int it = 0; it < 5; ++it) {
      int idx = tid + 512 * it, k = idx / 80, seg = idx - k * 80;
      a[it] = *(const u4*)(wd + (size_t)(8 * blk_of_pos(4 * ks + (k >> 3)) + (k & 7)) * DD + 8 * seg);
    }
    SCHED_FENCE();
#pragma unroll
    for (int it = 0; it < 5; ++it) {
      int idx = tid + 512 * it, k = idx / 80, seg = idx - k * 80;
      *(u4*)(smem + k * 1296 + seg * 16) = a[it];
    }
    raw_barrier();
    u4* W2 = (u4*)(ws + OFF_W2) + (size_t)(ks * 40 + 5 * w) * 64 + l;
    const char* base = smem + (8 * (l >> 4) + ((l & 15) >> 2)) * 1296 + (l & 3) * 8 + w * 160;
#pragma unroll
    for (int t = 0; t < 5; ++t) {
      u2 lo = tr_read(base + t * 32), hi = tr_read(base + t * 32 + 4 * 1296);
      WT16(W2, t * 64, (u4{lo[0], lo[1], hi[0], hi[1]}));
    }
    return;
  }
  if (b >= 256) {
    const int task = b - 256, u = task & 7, v = (task >> 3) & 7, hf = task >> 6;
    u4 d[3];
#pragma unroll
    for (int it = 0; it < 3; ++it) {
      int idx = tid + 512 * it; if (idx >= 1280) idx = tid;
      int rho = idx / 20, pc = idx - rho * 20;
      int row = 64 * (4 * v + (rho & 3)) + 8 * u + ((rho >> 2) & 7);
      const u16* src = (rho >= 32 ? wi : wr) + (size_t)row * 321 + 160 * hf + 8 * pc;
      d[it] = *(const u4u*)src;
    }
    unsigned last = 0;
    if (tid < 64) {
      int row = 64 * (4 * v + (tid & 3)) + 8 * u + ((tid >> 2) & 7);
      last = ((tid >= 32 ? wi : wr) + (size_t)row * 321)[320];
    }
    SCHED_FENCE();
    unsigned mx = last & 0x7fffu;
#pragma unroll
    for (int it = 0; it < 3; ++it) {
      int idx = tid + 512 * it; if (idx >= 1280) idx = tid;
      int rho = idx / 20, pc = idx - rho * 20;
      *(u4*)(smem + rho * 336 + pc * 16) = d[it];
#pragma unroll
      for (int e = 0; e < 4; ++e) mx = pkmax_abs(mx, d[it][e]);
    }
    if (tid < 64) *(u16*)(smem + tid * 336 + 320) = (u16)last;
    mx = wave_umax(umax(mx & 0xffffu, mx >> 16));
    if (l == 0) *(unsigned*)(smem + 21504 + w * 4) = mx;
    raw_barrier();
    u4* Wt = (u4*)(ws + OFF_WT);
    const int obase = ((u >> 1) * 2 + (v >> 2)) * 64 + 16 * (v & 3) + 8 * (u & 1);
    const int g = l >> 4, q4 = (l & 15) >> 2, p = l & 3;
#pragma unroll
    for (int rd = 0; rd < 3; ++rd) {
      int job = w + 8 * rd;
      if (job < 20) {
        int cbk = job >> 1, r8 = 4 * (job & 1) + g;
        const char* a = smem + (r8 * 4 + q4) * 336 + (16 * cbk + 4 * p) * 2;
        u2 re = tr_read(a), im = tr_read(a + 32 * 336);
        u4 o = {perm_lo(im[0], re[0]), perm_hi(im[0], re[0]), perm_lo(im[1], re[1]), perm_hi(im[1], re[1])};
        Wt[(160 * hf + 16 * cbk + (l & 15)) * 512 + obase + r8] = o;
      }
    }
    if (hf == 1 && tid < 8) {
      u4 o;
#pragma unroll
      for (int reg = 0; reg < 4; ++reg)
        o[reg] = (unsigned)*(const u16*)(smem + (tid * 4 + reg) * 336 + 320) | ((unsigned)*(const u16*)(smem + (32 + tid * 4 + reg) * 336 + 320) << 16);
      Wt[320 * 512 + obase + tid] = o;
    }
    if (tid == 0) {
      const unsigned* m = (const unsigned*)(smem + 21504);
      ((unsigned*)(ws + OFF_WM))[task] = umax(umax(umax(m[0], m[1]), umax(m[2], m[3])), umax(umax(m[4], m[5]), umax(m[6], m[7])));
    }
    return;
  }
  if (b < 3 && tid >= 256) *(unsigned*)(ws + OFF_CNT + (b * 16 + ((tid - 256) >> 4)) * 256 + (tid & 15) * 4) = 0u;
  const int q = (b & 7) * 32 + (b >> 3);
  const int ih = (w >> 1) & 1, cb = w & 1;
  char* lin = smem;
  char* lout = smem + 10240;
  char* ct = smem + 22528 + cb * 8448 + l * 16;
  RowC C;
  if (w < 4) {
    const int j = l >> 5, li = l & 31;
    const u2* xr = (const u2*)(x + (size_t)srow_of(q, w, j) * DD);
    u2 xv[5];
#pragma unroll
    for (int c = 0; c < 5; ++c) xv[c] = xr[li + 32 * c];
    float v[20];
#pragma unroll
    for (int c = 0; c < 5; ++c) {
      v[4 * c] = bf_lo(xv[c][0]); v[4 * c + 1] = bf_hi(xv[c][0]);
      v[4 * c + 2] = bf_lo(xv[c][1]); v[4 * c + 3] = bf_hi(xv[c][1]);
    }
    float sa = 0.f, sb = 0.f;
#pragma unroll
    for (int t = 0; t < 10; ++t) { sa += v[2 * t]; sb += v[2 * t + 1]; }
    const float mean = half_sum(sa + sb) * (1.f / 640.f);
    sa = 0.f; sb = 0.f;
#pragma unroll
    for (int t = 0; t < 10; ++t) { float d0 = v[2 * t] - mean, d1 = v[2 * t + 1] - mean; sa += d0 * d0; sb += d1 * d1; }
    const float var = half_sum(sa + sb) * (1.f / 640.f);
    const float sd = rbf(__builtin_sqrtf(rbf(rbf(var) + 1e-5f)));
    const float rs = __builtin_amdgcn_rcpf(sd);
    const float mb = rbf(mean);
    char* wp = lin + (4 * li + 2 * j) * 16 + w * 4;
#pragma unroll
    for (int c = 0; c < 5; ++c) {
      unsigned d01 = pk_bf16(v[4 * c] - mb, v[4 * c + 1] - mb), d23 = pk_bf16(v[4 * c + 2] - mb, v[4 * c + 3] - mb);
      unsigned n01 = pk_bf16(bf_lo(d01) * rs, bf_hi(d01) * rs), n23 = pk_bf16(bf_lo(d23) * rs, bf_hi(d23) * rs);
      unsigned p01 = pk_f16(bf_lo(n01) * 16.f, bf_hi(n01) * 16.f);
      unsigned p23 = pk_f16(bf_lo(n23) * 16.f, bf_hi(n23) * 16.f);
      auto r = __builtin_amdgcn_permlane32_swap(p01, p23, false, false);
      unsigned pa = r[0], pb = r[1];
      *(unsigned*)(wp + c * 2048) = perm_lo(pb, pa);
      *(unsigned*)(wp + c * 2048 + 16) = perm_hi(pb, pa);
    }
    raw_barrier();
    rc_load(C, ct, l);
    k1_dft<false>(C, lin, lout, l, ih, cb);
  } else {
    rc_part<-1>(ct, l, cb, 1.f / 256.f, 2 * ih);
    rc_part<-1>(ct, l, cb, 1.f / 256.f, 2 * ih + 1);
    raw_barrier();
    rc_load(C, ct, l);
    k1_dft<true>(C, lin, lout, l, ih, cb);
  }
  raw_barrier();
  u4* Z1 = (u4*)(ws + OFF_Z1);
  Z1[Z1IDX(q, tid)] = *(const u4*)(lout + tid * 16);
  if (tid < 128) Z1[Z1IDX(q, tid + 512)] = *(const u4*)(lout + (tid + 512) * 16);
}

DI unsigned wmax_exp(u2 m) {
  unsigned eb = wave_umax(umax(m[0], m[1])) >> 7;
  return eb > 251u ? 251u : eb;
}
extern "C" __global__ __launch_bounds__(256) void gf_k2(const u16* __restrict__ wg, const u16* __restrict__ wu, char* __restrict__ ws) {
  __shared__ __attribute__((aligned(16))) char smem[32 * 880];
  const int b = blockIdx.x, tid = threadIdx.x, l = tid & 63;
  const int w = __builtin_amdgcn_readfirstlane(tid >> 6);
  const int g = l >> 4, r16 = l & 15;
  if (b >= 328) {
    const int fg = b & 7, ks = (b - 328) >> 3;
    u4 a[4], c[4];
#pragma unroll
    for (int it = 0; it < 4; ++it) {
      int idx = tid + 256 * it; idx = idx < 864 ? idx : tid;
      int k = idx / 27, seg = idx - k * 27;
      const size_t off = (size_t)(32 * ks + k) * FF + 216 * fg + 8 * seg;
      a[it] = *(const u4*)(wg + off); c[it] = *(const u4*)(wu + off);
    }
    SCHED_FENCE();
#pragma unroll
    for (int it = 0; it < 4; ++it) {
      int idx = tid + 256 * it; idx = idx < 864 ? idx : tid;
      int k = idx / 27, seg = idx - k * 27;
      u4 o0 = {perm_lo(c[it][0], a[it][0]), perm_hi(c[it][0], a[it][0]), perm_lo(c[it][1], a[it][1]), perm_hi(c[it][1], a[it][1])};
      u4 o1 = {perm_lo(c[it][2], a[it][2]), perm_hi(c[it][2], a[it][2]), perm_lo(c[it][3], a[it][3]), perm_hi(c[it][3], a[it][3])};
      *(u4*)(smem + k * 880 + seg * 32) = o0;
      *(u4*)(smem + k * 880 + seg * 32 + 16) = o1;
    }
    raw_barrier();
    u4* W1 = (u4*)(ws + OFF_W1) + (size_t)(ks * 216 + fg * 27 + w) * 64 + l;
    const char* base = smem + (8 * g + (r16 >> 2)) * 880 + (l & 3) * 8 + w * 32;
#pragma unroll
    for (int t = 0; t < 7; ++t) {
      if (t < 6 || w < 3) {
        u2 lo = tr_read(base + t * 128), hi = tr_read(base + t * 128 + 4 * 880);
        WT16(W1, t * 256, (u4{lo[0], lo[1], hi[0], hi[1]}));
      }
    }
    return;
  }
  const int xi = b >> 3;
  int kd = 40 * (b & 7) + xi;
  if (xi == 40) { if ((b & 7) != 7) return; kd = 320; }
  const int kdm = kd == 0 ? 0 : 640 - kd;
  const u4* Z1 = (const u4*)(ws + OFF_Z1);
  const u4 z1 = Z1[Z1IDX(tid, kd)], z2 = Z1[Z1IDX(tid, kdm)];
  u4 wv[2];
  {
    const u4* Wt = (const u4*)(ws + OFF_WT) + kd * 512 + 128 * w + l;
    wv[0] = Wt[0]; wv[1] = Wt[64];
  }
  const u2 wmv = ((const u2*)(ws + OFF_WM))[l];
  SCHED_FENCE();
  u4 M1[4], M2[4], N1[4], N2[4];
  float TC[4][4], TS[4][4];
  {
    const float g4 = (float)(4 * g), k1f = (float)(16 * w + r16);
    const float ka = k1f * (1.f / 64.f), ka0 = g4 * ka;
    const bool odd = r16 & 1;
    const unsigned s1 = (r16 & 2) ? 0x80008000u : 0u;
    const unsigned s1b = (((r16 & 3) + 1) & 2) ? 0x80008000u : 0u;
    const unsigned s2 = odd ? 0x80008000u : 0u;
    const unsigned s3 = ((3 * (r16 & 3)) & 2) ? 0x80008000u : 0u, s3b = ((3 * (r16 & 3) + 1) & 2) ? 0x80008000u : 0u;
#pragma unroll
    for (int jj = 0; jj < 4; ++jj) {
      float r = (float)jj * ka + ka0, c = fcos(r), sn = fsin(r);
      const unsigned a = pk_f16(c, sn), bq = pk_f16(-sn, c);
      const unsigned p = odd ? bq : a, q2 = odd ? a : bq;
      M1[0][jj] = a; M2[0][jj] = bq;
      M1[1][jj] = p ^ s1; M2[1][jj] = q2 ^ s1b;
      M1[2][jj] = a ^ s2; M2[2][jj] = bq ^ s2;
      M1[3][jj] = p ^ s3; M2[3][jj] = q2 ^ s3b;
    }
    const float ro = (float)r16 * (1.f / 32.f), ro0 = g4 * ro;
#pragma unroll
    for (int jj = 0; jj < 4; ++jj) {
      float r = (float)jj * ro + ro0, c = fcos(r), sn = fsin(r);
      const unsigned a = pk_f16(c, sn), bq = pk_f16(-sn, c), sj = (jj & 1) ? 0x80008000u : 0u;
      N1[0][jj] = a; N2[0][jj] = bq;
      N1[1][jj] = a ^ s2; N2[1][jj] = bq ^ s2;
      N1[2][jj] = a ^ sj; N2[2][jj] = bq ^ sj;
      N1[3][jj] = a ^ (s2 ^ sj); N2[3][jj] = bq ^ (s2 ^ sj);
    }
    {
      const float kb = k1f * (1.f / 2048.f);
      const float c1 = fcos(kb), d1 = fsin(kb), c16 = fcos(16.f * kb), d16 = fsin(16.f * kb);
      TC[0][0] = fcos(g4 * kb); TS[0][0] = fsin(g4 * kb);
#pragma unroll
      for (int rg = 1; rg < 4; ++rg) {
        TC[0][rg] = TC[0][rg - 1] * c1 - TS[0][rg - 1] * d1; TS[0][rg] = TS[0][rg - 1] * c1 + TC[0][rg - 1] * d1;
      }
#pragma unroll
      for (int rg = 0; rg < 4; ++rg) { TC[1][rg] = TC[0][rg] * c16 - TS[0][rg] * d16; TS[1][rg] = TS[0][rg] * c16 + TC[0][rg] * d16; }
    }
#pragma unroll
    for (int xx = 0; xx < 2; ++xx) {
      const float nu = (float)(16 * xx + r16) * (1.f / 2048.f), nu0 = ((float)(16 * w) + g4) * nu;
      const float c1 = fcos(nu), d1 = fsin(nu);
      TC[2 + xx][0] = fcos(nu0); TS[2 + xx][0] = fsin(nu0);
#pragma unroll
      for (int rg = 1; rg < 4; ++rg) {
        TC[2 + xx][rg] = TC[2 + xx][rg - 1] * c1 - TS[2 + xx][rg - 1] * d1;
        TS[2 + xx][rg] = TS[2 + xx][rg - 1] * c1 + TC[2 + xx][rg - 1] * d1;
      }
    }
  }
  SCHED_FENCE();
  u4* sa = (u4*)smem;
  u4* ex = (u4*)(smem + 8192);
  {
    u4 A0, A1;
#pragma unroll
    for (int jj = 0; jj < 4; ++jj) {
      h2 p = __builtin_bit_cast(h2, (unsigned)z1[jj]), qq = __builtin_bit_cast(h2, (unsigned)z2[jj]);
      h2 qa = {qq[0], -qq[1]};
      h2 pb = {p[1], -p[0]}, qb = {qq[1], qq[0]};
      A0[jj] = __builtin_bit_cast(unsigned, p + qa);
      A1[jj] = __builtin_bit_cast(unsigned, pb + qb);
    }
    sa[tid] = A0;
    sa[256 + tid] = A1;
  }
  raw_barrier();
  const f4 zero = {0.f, 0.f, 0.f, 0.f};
  f4 Tr[2] = {zero, zero}, Ti[2] = {zero, zero};
#pragma unroll
  for (int t = 0; t < 4; ++t) {
    u4 A0 = sa[64 * t + l], A1 = sa[256 + 64 * t + l];
    Tr[0] = mfma_h(A0, M1[t], Tr[0]); Tr[1] = mfma_h(A1, M1[t], Tr[1]);
    Ti[0] = mfma_h(A0, M2[t], Ti[0]); Ti[1] = mfma_h(A1, M2[t], Ti[1]);
  }
  const unsigned eb = wmax_exp(wmv);
  const float fs = asf((252u - eb) << 23) * (1.f / 256.f);
  u4 X[2];
#pragma unroll
  for (int rb = 0; rb < 2; ++rb)
#pragma unroll
    for (int rg = 0; rg < 4; ++rg) {
      float a = Tr[rb][rg], c = Ti[rb][rg], tc = TC[rb][rg], ts = TS[rb][rg];
      X[rb][rg] = pk_f16(a * tc + c * ts, c * tc - a * ts);
    }
  u4 Y[2];
#pragma unroll
  for (int ob = 0; ob < 2; ++ob) {
    f4 xr = mfma_h(N1[2 * ob], X[0], zero); xr = mfma_h(N1[2 * ob + 1], X[1], xr);
    f4 xim = mfma_h(N2[2 * ob], X[0], zero); xim = mfma_h(N2[2 * ob + 1], X[1], xim);
#pragma unroll
    for (int rg = 0; rg < 4; ++rg) {
      float wr_ = bf_lo(wv[ob][rg]) * fs, wi_ = bf_hi(wv[ob][rg]) * fs;
      float yr = xr[rg] * wr_ - xim[rg] * wi_;
      float yi = xr[rg] * wi_ + xim[rg] * wr_;
      Y[ob][rg] = pk_f16(yi, yr);
    }
  }
#pragma unroll
  for (int cb = 0; cb < 2; ++cb) {
    f4 ui = mfma_h(Y[0], N1[2 * cb], zero); ui = mfma_h(Y[1], N1[2 * cb + 1], ui);
    f4 ur = mfma_h(Y[0], N2[2 * cb], zero); ur = mfma_h(Y[1], N2[2 * cb + 1], ur);
    u4 U;
#pragma unroll
    for (int rg = 0; rg < 4; ++rg) {
      float tc = TC[2 + cb][rg], ts = TS[2 + cb][rg];
      U[rg] = pk_f16(ui[rg] * tc + ur[rg] * ts, ur[rg] * tc - ui[rg] * ts);
    }
    ex[(w * 2 + cb) * 64 + l] = U;
  }
  raw_barrier();
  f4 Vi[2] = {zero, zero}, Vr[2] = {zero, zero};
#pragma unroll
  for (int t = 0; t < 4; ++t)
#pragma unroll
    for (int cb = 0; cb < 2; ++cb) {
      u4 U = ex[(t * 2 + cb) * 64 + l];
      Vi[cb] = mfma_h(M1[t], U, Vi[cb]);
      Vr[cb] = mfma_h(M2[t], U, Vr[cb]);
    }
  u4* Z2 = (u4*)(ws + OFF_Z2);
  const float sg = (kd == 0 || kd == 320) ? 0.f : 1.f;
  u4 o, om;
#pragma unroll
  for (int rg = 0; rg < 4; ++rg) {
    float ia = Vi[0][rg] * sg, ib = Vi[1][rg] * sg;
    o[rg] = pk_f16(Vr[0][rg] - ib, ia + Vr[1][rg]);
    om[rg] = pk_f16(Vr[0][rg] + ib, Vr[1][rg] - ia);
  }
  Z2[Z2IDX(tid, kd)] = o;
  if (kdm != kd) Z2[Z2IDX(tid, kdm)] = om;
}

#define GLDS(src, dst) __builtin_amdgcn_global_load_lds((const GAS void*)(src), (LAS void*)(dst), 16, 0, 0)
#define GLDS_SC1(src, dst) __builtin_amdgcn_global_load_lds((const GAS void*)(src), (LAS void*)(dst), 16, 0, 16)
template <int N> DI void wait_vm() { asm volatile("s_waitcnt vmcnt(%0)" ::"n"(N) : "memory"); }
#define SGB(mask, n) __builtin_amdgcn_sched_group_barrier(mask, n, 0)

DI float geglu(float g, float u) {
  float t = g * g;
  float z = g * (t * (-2.f * 1.4426950408889634f * 0.7978845608028654f * 0.044715f) + (-2.f * 1.4426950408889634f * 0.7978845608028654f));
  float e = __builtin_amdgcn_exp2f(z);
  return (g * u) * __builtin_amdgcn_rcpf(1.f + e);
}
DI void poll_ge16(const char* ws, int cnt) {
  const GAS unsigned* p = (const GAS unsigned*)(ws + OFF_CNT + cnt * 256) + (threadIdx.x & 15);
  for (int spins = 0; spins < (1 << 20); ++spins) {
    unsigned v = __hip_atomic_load(p, __ATOMIC_RELAXED, __HIP_MEMORY_SCOPE_AGENT);
    if (__builtin_amdgcn_ballot_w64(v == 0u) == 0ull) break;
    __builtin_amdgcn_s_sleep(1);
  }
  asm volatile("" ::: "memory");
}
DI void signal(char* ws, int cnt, int slot) {
  __hip_atomic_store((GAS unsigned*)(ws + OFF_CNT + cnt * 256) + slot, 1u, __ATOMIC_RELAXED, __HIP_MEMORY_SCOPE_AGENT);
}

constexpr int G1_STG = 22 * 1024, G1_NB = 7, G1_NS = 20, G1_TAIL = 4, G1_MAIN = G1_NS - G1_TAIL;
constexpr int LDS_TOTAL = 160 * 1024, PA_OFF = 6 * G1_STG;
constexpr int G2_NS = 14, G2_AD = 6;

DI void arrive_n(char* smem, char* ws, int word, int cnt, int l, unsigned last, int slot) {
  if (l == 0) {
    unsigned old = __hip_atomic_fetch_add((LAS unsigned*)(smem + LDS_TOTAL - 64 + word * 4), 1u, __ATOMIC_RELAXED, __HIP_MEMORY_SCOPE_WORKGROUP);
    if (old == last) signal(ws, cnt, slot);
  }
}
template <int NW> DI void k3_loader(char* smem, char* ws, int mt, int nb0, int mt2, int db0, int l, int j) {
  const unsigned loff = l * 16;
  constexpr int NL = NW + 2;
  const char* sA = ws + OFF_HP + (size_t)(8 * mt + j) * 1024 + loff;
  const char* sB = ws + OFF_W1 + (size_t)(nb0 + j) * 1024 + loff;
  auto issueA = [&](int ks) {
    char* d = smem + (ks % G1_NB) * G1_STG + j * 1024;
    GLDS_SC1(sA + (size_t)ks * (128 * 1024), d);
    GLDS_SC1(sA + (size_t)ks * (128 * 1024) + 4096, d + 4096);
  };
  auto issueW = [&](int ks) {
    char* d = smem + (ks % G1_NB) * G1_STG + 8192 + j * 1024;
#pragma unroll
    for (int t = 0; t < NW; ++t) GLDS(sB + (size_t)ks * (216 * 1024) + t * 4096, d + t * 4096);
  };
  int foff[5];
#pragma unroll
  for (int t = 0; t < 5; ++t) { int f = j + 4 * t, kq = f / 5; foff[t] = (kq * 40 + db0 + f - 5 * kq) * 1024; }
  const char* s2 = ws + OFF_W2 + loff;
  auto issue2 = [&](int i) {
    char* d = smem + ((i + 6) % G1_NB) * G1_STG + j * 1024;
#pragma unroll
    for (int t = 0; t < 5; ++t) GLDS(s2 + (size_t)i * (160 * 1024) + foff[t], d + t * 4096);
  };
  raw_barrier();
  issueW(0); issueW(1); issueW(2); issueW(5);
  raw_barrier(); raw_barrier(); raw_barrier();
  issueW(3); issueW(4); issueW(6);
  poll_ge16(ws, mt);
  u4 pk[33];
  auto fetch = [&](int e) {
    const int i = 7 + e / 5, t = e % 5;
    pk[e] = *(const u4*)(s2 + (size_t)i * (160 * 1024) + (e == 32 && j >= 2 ? foff[1] : foff[t]));
  };
  auto park = [&](int i) {
    char* d = smem + ((i + 6) % G1_NB) * G1_STG + j * 1024 + loff;
#pragma unroll
    for (int t = 0; t < (i < 13 ? 5 : 3); ++t) *(u4*)(d + t * 4096) = pk[(i - 7) * 5 + t];
  };
#pragma unroll
  for (int s = 0; s < 7; ++s) issueA(s);
  wait_vm<12>();
  raw_barrier();
  static_for<G1_MAIN - 1>([&](auto S_) {
    constexpr int s = decltype(S_)::value;
    constexpr int n = s <= 5 ? 2 * (5 - s) + NL * s : (s <= 13 ? NL * ((s + 6 < 19 ? s + 6 : 19) - (s + 1)) : NL * (19 - (s + 1)) + 5 * (s - 13));
    constexpr int lo = s - 6 > 0 ? s - 6 : 0, hi = s - 1 < 10 ? s - 1 : 10;
    wait_vm<n + (hi >= lo ? 3 * (hi - lo + 1) : 0)>();
    raw_barrier();
    if constexpr (s + G1_NB < G1_NS) { issueA(s + G1_NB); issueW(s + G1_NB); }
    else issue2(s - 13);
    if constexpr (s <= 10) { SCHED_FENCE(); fetch(3 * s); fetch(3 * s + 1); fetch(3 * s + 2); SCHED_FENCE(); }
  });
  wait_vm<10>();
  raw_barrier();
  issue2(2);
  raw_barrier();
  issue2(3); issue2(4); issue2(5); issue2(6);
  wait_vm<0>();
  raw_barrier();
  if (j == 0) poll_ge16(ws, 32 + (mt2 >> 1));
  raw_barrier();
  park(7); park(8); park(9);
  raw_barrier();
  park(10); park(11);
  raw_barrier();
  park(12); park(13);
  raw_barrier();
}
DI void arrive(char* smem, char* ws, int word, int cnt, int l, int slot) { arrive_n(smem, ws, word, cnt, l, 7u, slot); }

extern "C" __global__ __launch_bounds__(768, 3) void gf_k3(const u16* __restrict__ x, u16* __restrict__ out, char* __restrict__ ws) {
  extern __shared__ __attribute__((aligned(16))) char smem[];
  const int b = blockIdx.x, tid = threadIdx.x, l = tid & 63;
  const int w = __builtin_amdgcn_readfirstlane(tid >> 6);
  const int xcd = b & 7, idx = b >> 3;
  const int mt = 8 * (xcd & 1) + (idx & 7), nt = 4 * (xcd >> 1) + (idx >> 3);
  const int mt2 = 16 * (xcd & 1) + 8 * ((xcd >> 1) & 1) + (idx & 7), nt2 = 4 * (xcd >> 2) + (idx >> 3);
  const int nb0 = (27 * nt) >> 1, mb0 = 8 * mt, db0 = 5 * nt2;
  const unsigned loff = l * 16;
  if (w >= 8) {
    if (w < 10) k3_loader<4>(smem, ws, mt, nb0, mt2, db0, l, w - 8); else k3_loader<3>(smem, ws, mt, nb0, mt2, db0, l, w - 8);
    return;
  }
  const int g = l >> 4, r16 = l & 15;
  const auto rsrc = __builtin_amdgcn_make_buffer_rsrc(ws, 0, (int)WS_USED, 0x00020000);
  {
    char* pa = smem + PA_OFF;
    const int q = 16 * mt + nt;
    const int ih = (w >> 1) & 1, cb = w & 1, ii = w >> 2, j = l & 1;
    const u4* Z2 = (const u4*)(ws + OFF_Z2);
    const u4 zv0 = Z2[Z2IDX(q, tid)];
    const u4 zv1 = Z2[Z2IDX(q, 512 + (tid & 127))];
    const int e0 = r16 >> 1;
    const bool v2 = e0 < 4;
    const size_t roff = (size_t)srow_of(q, 2 * ih + ii, j) * DD + 16 * cb + 4 * g + 32 * e0;
    u2 xv[3];
    xv[0] = *(const u2*)(x + roff); xv[1] = *(const u2*)(x + roff + 256); xv[2] = *(const u2*)(x + roff + (v2 ? 512 : 0));
    const u2 wmv = ((const u2*)(ws + OFF_WM))[l];
    SCHED_FENCE();
    char* ct = pa + 10368 + cb * 8448 + l * 16;
    rc_part<1>(ct, l, cb, 1.f / 32.f, w >> 1);
    SCHED_FENCE();
    *(u4*)(pa + tid * 16) = zv0;
    if (tid < 128) *(u4*)(pa + (512 + tid) * 16) = zv1;
    if (tid < 3) *(unsigned*)(smem + LDS_TOTAL - 64 + tid * 4) = 0u;
    raw_barrier();
    RowC C;
    rc_load(C, ct, l);
    u4 T0, T1;
    if (ii) row_stage_a(C, pa, l, ih, true, T0, T1); else row_stage_a(C, pa, l, ih, false, T0, T1);
    const unsigned eb = wmax_exp(wmv);
    const float ysc = asf((eb + 2u) << 23) * 0.05f;
    float h[12];
    float s = 0.f;
    const int mp = 8 * q + 2 * (2 * ih + ii) + j;
    char* hs = smem + 4 * G1_STG - 2048;
    char* ns = smem + 5 * G1_STG - 2048;
    const int r8 = mp & 7;
#pragma unroll
    for (int ob = 0; ob < 3; ++ob) {
      f4 z = {0.f, 0.f, 0.f, 0.f};
      z = mfma_h(T0, C.t[2 + ob], z);
      u4 gb = {C.t[5][ob], 0u, 0u, 0u};
      z = mfma_h(T1, gb, z);
      unsigned y01 = pk_bf16(z[0] * ysc, z[1] * ysc), y23 = pk_bf16(z[2] * ysc, z[3] * ysc);
      unsigned h01 = pk_bf16(bf_lo(xv[ob][0]) + bf_lo(y01), bf_hi(xv[ob][0]) + bf_hi(y01));
      unsigned h23 = pk_bf16(bf_lo(xv[ob][1]) + bf_lo(y23), bf_hi(xv[ob][1]) + bf_hi(y23));
      if (ob == 2) { h01 = v2 ? h01 : 0u; h23 = v2 ? h23 : 0u; }
      if (ob < 2 || v2) *(u2*)(hs + ((2 * e0 + cb + 16 * ob) * 8 + r8) * 32 + g * 8) = u2{h01, h23};
      h[4 * ob] = bf_lo(h01); h[4 * ob + 1] = bf_hi(h01); h[4 * ob + 2] = bf_lo(h23); h[4 * ob + 3] = bf_hi(h23);
      s += (h[4 * ob] + h[4 * ob + 1]) + (h[4 * ob + 2] + h[4 * ob + 3]);
    }
    const float mloc = parity_sum(s) * (1.f / 320.f);
    float s2 = 0.f, s3 = 0.f;
#pragma unroll
    for (int t = 0; t < 8; ++t) { float d = h[t] - mloc; s2 += d * d; }
#pragma unroll
    for (int t = 8; t < 12; ++t) { float d = h[t] - mloc; s3 += d * d; }
    const float m2loc = parity_sum(v2 ? s2 + s3 : s2);
    f2* ex = (f2*)(pa + 10240);
    if (l < 2) ex[w * 2 + l] = f2{mloc, m2loc};
    raw_barrier();
    {
      const int hb = (int)OFF_HH + 8 * q * 32;
      __builtin_amdgcn_raw_buffer_store_b128(*(const u4*)(hs + tid * 16), rsrc, (tid >> 4) * (2048 * 32) + (tid & 15) * 16, hb, 16);
      if (tid < 128) __builtin_amdgcn_raw_buffer_store_b128(*(const u4*)(hs + 8192 + tid * 16), rsrc, (32 + (tid >> 4)) * (2048 * 32) + (tid & 15) * 16, hb, 16);
    }
    const f2 o = ex[(w ^ 1) * 2 + j];
    const float mean = 0.5f * (mloc + o[0]);
    const float dl = mloc - o[0];
    const float var = (m2loc + o[1] + dl * dl * 160.f) * (1.f / 640.f);
    const float mb = rbf(mean);
    const float sd = rbf(__builtin_sqrtf(rbf(rbf(var) + 1e-5f)));
    const float rs = __builtin_amdgcn_rcpf(sd);
#pragma unroll
    for (int ob = 0; ob < 3; ++ob) {
      unsigned d01 = pk_bf16(h[4 * ob] - mb, h[4 * ob + 1] - mb);
      unsigned d23 = pk_bf16(h[4 * ob + 2] - mb, h[4 * ob + 3] - mb);
      u2 st = {pk_bf16(bf_lo(d01) * rs, bf_hi(d01) * rs), pk_bf16(bf_lo(d23) * rs, bf_hi(d23) * rs)};
      if (ob < 2 || v2) *(u2*)(ns + (((8 * ob + e0) * 4 + 2 * cb + (g >> 1)) * 8 + r8) * 16 + (g & 1) * 8) = st;
    }
    raw_barrier();
    {
      const int nbase = (int)OFF_HP + (q >> 1) * 1024 + (q & 1) * 128;
      __builtin_amdgcn_raw_buffer_store_b128(*(const u4*)(ns + tid * 16), rsrc, (tid >> 5) * (128 * 1024) + ((tid >> 3) & 3) * 256 + (tid & 7) * 16, nbase, 16);
      if (tid < 128) __builtin_amdgcn_raw_buffer_store_b128(*(const u4*)(ns + 8192 + tid * 16), rsrc, (16 + (tid >> 5)) * (128 * 1024) + ((tid >> 3) & 3) * 256 + (tid & 7) * 16, nbase, 16);
    }
    wait_vm<0>();
    raw_barrier();
    if (tid == 0) signal(ws, mt, nt);
  }
  {
    int lb = threadIdx.x & 63;
    asm volatile("" : "+v"(lb));
    const unsigned loff = lb * 16;
    const int wm = w >> 1, wn = w & 1;
    const char* rdA = smem + (2 * wm) * 1024 + loff;
    const char* rdB = smem + (8 + 7 * wn) * 1024 + loff;
    f4 acc[7][2];
#pragma unroll
    for (int i = 0; i < 7; ++i) { acc[i][0] = f4{0.f, 0.f, 0.f, 0.f}; acc[i][1] = f4{0.f, 0.f, 0.f, 0.f}; }
    {
      u4 af[2][2], wf[2][7];
      raw_barrier();
#pragma unroll
      for (int i = 0; i < 2; ++i) af[0][i] = *(const u4*)(rdA + i * 1024);
#pragma unroll
      for (int i = 0; i < 7; ++i) wf[0][i] = *(const u4*)(rdB + i * 1024);
      static_for<G1_MAIN>([&](auto S_) {
        constexpr int s = decltype(S_)::value;
        constexpr int cur = s & 1, nxt = cur ^ 1;
        constexpr bool more = s + 1 < G1_MAIN;
        if constexpr (more) {
          raw_barrier();
          constexpr int bo = ((s + 1) % G1_NB) * G1_STG;
#pragma unroll
          for (int i = 0; i < 2; ++i) af[nxt][i] = *(const u4*)(rdA + bo + i * 1024);
#pragma unroll
          for (int i = 0; i < 7; ++i) wf[nxt][i] = *(const u4*)(rdB + bo + i * 1024);
        }
#pragma unroll
        for (int i = 0; i < 7; ++i)
#pragma unroll
          for (int m = 0; m < 2; ++m) acc[i][m] = mfma_b(wf[cur][i], af[cur][m], acc[i][m]);
        if constexpr (more) {
#pragma unroll
          for (int i = 0; i < 9; ++i) { SGB(0x8, 1); SGB(0x100, 1); }
          SGB(0x8, 14);
        }
        SCHED_FENCE();
      });
    }
    raw_barrier();
    int lz = threadIdx.x & 63;
    asm volatile("" : "+v"(lz));
    const int g = lz >> 4, r16 = lz & 15;
    u4 af[G1_TAIL][2], wf[2][G1_TAIL];
#pragma unroll
    for (int t = 0; t < G1_TAIL; ++t) {
      const int bo = ((G1_MAIN + t) % G1_NB) * G1_STG;
      wf[0][t] = *(const u4*)(rdB + bo);
      af[t][0] = *(const u4*)(rdA + bo); af[t][1] = *(const u4*)(rdA + bo + 1024);
    }
    const int aoff = (int)OFF_AP + (mb0 + 2 * wm) * 1024 + r16 * 16 + 4 * g;
    auto epi = [&](int i) {
      const int pos = i < 4 ? 8 * nt + 4 * wn + i : 128 + 11 * (nt >> 1) + 5 * (nt & 1) + 3 * wn + (i - 4);
      const int so = (pos >> 2) * (128 * 1024) + (pos & 3) * 256;
      if (i == 6 && wn == 1 && !(nt & 1)) return;
#pragma unroll
      for (int m = 0; m < 2; ++m)
        __builtin_amdgcn_raw_buffer_store_b32(pk_bf16(geglu(acc[i][m][0], acc[i][m][1]), geglu(acc[i][m][2], acc[i][m][3])), rsrc,
                                              aoff + m * 1024, so, 16);
    };
    static_for<7>([&](auto I_) {
      constexpr int i = decltype(I_)::value;
      if constexpr (i + 1 < 7) {
#pragma unroll
        for (int t = 0; t < G1_TAIL; ++t) wf[(i + 1) & 1][t] = *(const u4*)(rdB + ((G1_MAIN + t) % G1_NB) * G1_STG + (i + 1) * 1024);
      }
#pragma unroll
      for (int t = 0; t < G1_TAIL; ++t)
#pragma unroll
        for (int m = 0; m < 2; ++m) acc[i][m] = mfma_b(wf[i & 1][t], af[t][m], acc[i][m]);
      if constexpr (i > 0) {
        epi(i - 1);
#pragma unroll
        for (int t = 0; t < 8; ++t) { SGB(0x8, 1); if (t < 4 && i + 1 < 7) SGB(0x100, 1); SGB(0x2, 4); SGB(0x400, 1); }
      }
      SCHED_FENCE();
      if constexpr (i == 5) { wait_vm<2 * (5 - 4)>(); arrive(smem, ws, 0, 16 + mt, lz, nt); }
    });
    epi(6);
    raw_barrier();
    wait_vm<0>();
    arrive(smem, ws, 1, 32 + mt, lz, nt);
    if (w == 0) poll_ge16(ws, 16 + (mt2 >> 1));
  }
  {
    int l = threadIdx.x & 63;
    asm volatile("" : "+v"(l));
    const int g = l >> 4, r16 = l & 15;
    const unsigned loff = l * 16;
    const int kq = w & 3, mh = w >> 2;
    const int mb2 = 4 * mt2 + 2 * mh;
    f4 acc[2][5];
#pragma unroll
    for (int m = 0; m < 2; ++m)
#pragma unroll
      for (int n = 0; n < 5; ++n) acc[m][n] = f4{0.f, 0.f, 0.f, 0.f};
    const char* rdW = smem + kq * 5120 + loff;
    const int abase = (int)OFF_AP + (kq * 128 + mb2) * 1024;
    u4 fa[G2_AD][2], wf[2][5];
    auto loadA = [&](int i, u4 (&a)[2]) {
      const int so = abase + (i < 13 ? i : (kq < 2 ? 13 : 12)) * (512 * 1024);
      a[0] = __builtin_amdgcn_raw_buffer_load_b128(rsrc, loff, so, 16);
      a[1] = __builtin_amdgcn_raw_buffer_load_b128(rsrc, loff + 1024, so, 16);
    };
    raw_barrier();
    static_for<G2_AD>([&](auto I_) { constexpr int i = decltype(I_)::value; loadA(i, fa[i]); SCHED_FENCE(); });
    u2 hv[3];
#pragma unroll
    for (int n = 0; n < 5; ++n) wf[0][n] = *(const u4*)(rdW + 6 * G1_STG + n * 1024);
    SCHED_FENCE();
    static_for<G2_NS - 1>([&](auto I_) {
      constexpr int i = decltype(I_)::value;
      constexpr int cur = i & 1, nxt = cur ^ 1;
      if constexpr (i == 2 || i == 4 || i == 6 || i == 9) raw_barrier();
#pragma unroll
      for (int n = 0; n < 5; ++n) wf[nxt][n] = *(const u4*)(rdW + ((i + 7) % G1_NB) * G1_STG + n * 1024);
#pragma unroll
      for (int m = 0; m < 2; ++m)
#pragma unroll
        for (int n = 0; n < 5; ++n) acc[m][n] = mfma_b(wf[cur][n], fa[i % G2_AD][m], acc[m][n]);
      if constexpr (i + G2_AD < G2_NS) loadA(i + G2_AD, fa[i % G2_AD]);
      if constexpr (i == 9) {
#pragma unroll
        for (int u = 0; u < 3; ++u) {
          int t = kq + 4 * u; t = t < 10 ? t : kq;
          const int m = t >= 5, n = t - 5 * m;
          hv[u] = __builtin_amdgcn_raw_buffer_load_b64(rsrc, r16 * 32 + g * 8, (int)OFF_HH + ((db0 + n) * 2048 + 16 * (mb2 + m)) * 32, 16);
        }
      }
      SGB(0x100, 5); SGB(0x8, 10); SGB(0x20, 3);
      SCHED_FENCE();
    });
    if (kq < 2) {
#pragma unroll
      for (int m = 0; m < 2; ++m)
#pragma unroll
        for (int n = 0; n < 5; ++n) acc[m][n] = mfma_b(wf[1][n], fa[13 % G2_AD][m], acc[m][n]);
    }
    raw_barrier();
    f4* red = (f4*)smem + mh * (40 * 64) + l;
#pragma unroll
    for (int t = 0; t < 10; ++t) red[(t * 4 + kq) * 64] = acc[t / 5][t % 5];
    raw_barrier();
#pragma unroll
    for (int u = 0; u < 3; ++u) {
      const int t = kq + 4 * u < 10 ? kq + 4 * u : kq, m = t >= 5, n = t - 5 * m;
      const f4* sp = red + t * 256;
      f4 o = (sp[0] + sp[64]) + (sp[128] + sp[192]);
      unsigned o01 = pk_bf16(o[0], o[1]), o23 = pk_bf16(o[2], o[3]);
      const int mp = 16 * (mb2 + m) + r16;
      u16* op = out + (size_t)srow_of(mp >> 3, (mp >> 1) & 3, mp & 1) * DD + 16 * (db0 + n) + 4 * g;
      *(u2*)op = u2{pk_bf16(bf_lo(hv[u][0]) + bf_lo(o01), bf_hi(hv[u][0]) + bf_hi(o01)),
                    pk_bf16(bf_lo(hv[u][1]) + bf_lo(o23), bf_hi(hv[u][1]) + bf_hi(o23))};
    }
  }
}

extern "C" __device__ __attribute__((used, noinline)) void gf_end() {}

#if defined(__HIP_DEVICE_COMPILE__)
#pragma clang attribute pop
#endif

extern "C" void kernel_launch(void* const* d_in, const int* in_sizes, int n_in, void* d_out, int out_size, void* d_ws,
                              size_t ws_size, hipStream_t stream) {
  const u16* x = (const u16*)d_in[0];
  const u16* wr = (const u16*)d_in[1];
  const u16* wi = (const u16*)d_in[2];
  const u16* wg = (const u16*)d_in[4];
  const u16* wu = (const u16*)d_in[5];
  const u16* wd = (const u16*)d_in[6];
  u16* out = (u16*)d_out;
  char* ws = (char*)d_ws;
  (void)hipFuncSetAttribute((const void*)gf_k3, hipFuncAttributeMaxDynamicSharedMemorySize, LDS_TOTAL);
  gf_k1<<<448, 512, 0, stream>>>(x, wr, wi, wd, ws);
  gf_k2<<<328 + 160, 256, 0, stream>>>(wg, wu, ws);
  gf_k3<<<256, 768, LDS_TOTAL, stream>>>(x, out, ws);
}
```
